```python
import torch

def _softmax(x, axis=-1):
    return torch.softmax(x, dim=axis)

def _gelu(x):
    return torch.nn.functional.gelu(x, approximate='tanh')

def _silu(x):
    return torch.nn.functional.silu(x)

def _sigmoid(x):
    return torch.sigmoid(x)

def _layernorm(x, axis=-1, eps=1e-05):
    m = x.mean(dim=axis, keepdim=True)
    v = x.var(dim=axis, keepdim=True, unbiased=False)
    return (x - m) / torch.sqrt(v + eps)

def _scatter_add(tgt, idx, src, axis=0):
    return tgt.clone().index_add_(axis, idx, src)

def _segment_sum(src, seg, n):
    out = torch.zeros((n,) + src.shape[1:], dtype=src.dtype)
    return out.index_add_(0, seg, src)

def _pad(x, pw):
    flat = tuple((v for pair in reversed(pw) for v in pair))
    return torch.nn.functional.pad(x, flat)
kernel_name = 'sequential_preLN__memory_aug_recurrent_mem_tr__mixer_channel_mlp__prompt'

def setup_inputs():

    _w = lambda a: (lambda t: t.to(torch.bfloat16) if t.is_floating_point() else t)(torch.as_tensor(a))

    def _A_setup():
        B, S, Dm = (1, 2048, 448)
        H, Dh = (16, 256)
        M, seg = (8, 32)
        seg = min(seg, S)
        S = S // seg * seg
        return {'x': _w((lambda *s: torch.randn(*s, dtype=torch.bfloat16) * 0.05)(B, S, Dm)), 'mem0': _w((lambda *s: torch.randn(*s, dtype=torch.bfloat16) * 0.2)(B, M, Dm)), 'Wq': _w((lambda *s: torch.randn(*s, dtype=torch.bfloat16) * 1.6)(Dm, H, Dh) * Dm ** (-0.5)), 'Wk': _w((lambda *s: torch.randn(*s, dtype=torch.bfloat16) * 1.6)(Dm, H, Dh) * Dm ** (-0.5)), 'Wv': _w((lambda *s: torch.randn(*s, dtype=torch.bfloat16) * 0.2)(Dm, H, Dh) * Dm ** (-0.5)), 'Wo': _w((lambda *s: torch.randn(*s, dtype=torch.bfloat16) * 0.2)(H, Dh, Dm) * (H * Dh) ** (-0.5)), 'seg_len': seg}
    _A_in = _A_setup()

    def _B_setup():
        B, S, D, F = (1, 2048, 448, 1792)
        return {'x': _w((lambda *s: torch.randn(*s, dtype=torch.bfloat16) * 0.05)(B, S, D)), 'w1': _w((lambda *s: torch.randn(*s, dtype=torch.bfloat16) * 0.8)(D, F) * D ** (-0.5)), 'b1': _w((lambda *s: torch.randn(*s, dtype=torch.bfloat16) * 0.4)(F)), 'w2': _w((lambda *s: torch.randn(*s, dtype=torch.bfloat16) * 0.05)(F, D) * F ** (-0.5)), 'b2': _w((lambda *s: torch.randn(*s, dtype=torch.bfloat16) * 0.05)(D))}
    _B_in = _B_setup()
    return {'A_x': _A_in['x'], 'A_mem0': _A_in['mem0'], 'A_Wq': _A_in['Wq'], 'A_Wk': _A_in['Wk'], 'A_Wv': _A_in['Wv'], 'A_Wo': _A_in['Wo'], 'A_seg_len': _A_in['seg_len'], 'B_x': _B_in['x'], 'B_w1': _B_in['w1'], 'B_b1': _B_in['b1'], 'B_w2': _B_in['w2'], 'B_b2': _B_in['b2']}

def reference(A_x, A_mem0, A_Wq, A_Wk, A_Wv, A_Wo, A_seg_len, B_x, B_w1, B_b1, B_w2, B_b2):

    def _A_ref(x, mem0, Wq, Wk, Wv, Wo, seg_len):
        B, S, Dm = x.shape
        H, Dh = (Wq.shape[1], Wq.shape[2])
        M = mem0.shape[1]
        scale = Dh ** (-0.5)
        n_seg = S // seg_len
        L = M + seg_len
        idx = torch.arange(L)
        mask = (idx[:, None] < M) | (idx[:, None] >= idx[None, :])
        mem = mem0
        outs = []
        for s in range(n_seg):
            xs = x[:, s * seg_len:(s + 1) * seg_len, :].clone()
            h = torch.concatenate([mem, xs], axis=1)
            q = torch.einsum('btd,dhe->bhte', h, Wq)
            k = torch.einsum('btd,dhe->bhte', h, Wk)
            v = torch.einsum('btd,dhe->bhte', h, Wv)
            scores = torch.einsum('bhqe,bhke->bhqk', q, k) * scale
            scores = torch.where(mask[None, None], scores, -1000000000.0)
            p = _softmax(scores, axis=-1)
            o = torch.einsum('bhqk,bhke->bhqe', p, v)
            o = torch.einsum('bhte,hed->btd', o, Wo)
            mem = o[:, :M, :].clone()
            outs.append(o[:, M:, :])
        return torch.concatenate(outs, axis=1)

    def _B_ref(x, w1, b1, w2, b2):
        y = _layernorm(x, axis=-1)
        h = _gelu(torch.einsum('bsd,df->bsf', y, w1) + b1)
        return torch.einsum('bsf,fd->bsd', h, w2) + b2
    x = A_x
    h = x + _A_ref(_layernorm(x, axis=-1), A_mem0, A_Wq, A_Wk, A_Wv, A_Wo, A_seg_len)
    h2 = h + _B_ref(_layernorm(h, axis=-1), B_w1, B_b1, B_w2, B_b2)
    return h2
```

```cpp
#include <hip/hip_runtime.h>
#include <hip/hip_bf16.h>

typedef unsigned u32;
typedef u32 u32x2 __attribute__((ext_vector_type(2)));
typedef u32 u32x4 __attribute__((ext_vector_type(4)));
typedef float f32x4 __attribute__((ext_vector_type(4)));
typedef __bf16 bf16x8 __attribute__((ext_vector_type(8)));
typedef __bf16 bf16x2 __attribute__((ext_vector_type(2)));
typedef short s16x4 __attribute__((ext_vector_type(4)));
typedef __amdgpu_buffer_rsrc_t rsrc_t;
#define LAS __attribute__((address_space(3)))

constexpr u32 WS_FLAGS = 0x0u;
constexpr u32 WS_LAT = 0xf000u;
constexpr u32 WS_M0 = 0x10000u;
constexpr u32 WS_DBG = 0x12000u;
constexpr u32 WS_RING = 0x52000u;
constexpr u32 WS_WIMG = 0xd3000u;
constexpr u32 WS_XP = 0xd13000u;
constexpr u32 WS_KX = 0xed3000u;
constexpr u32 WS_VX = 0x2ad3000u;
constexpr u32 WS_KM = 0x46d3000u;
constexpr u32 WS_VM = 0x4dd3000u;
constexpr u32 WS_PX = 0x54d3000u;
constexpr u32 WS_H = 0x5753000u;
constexpr u32 WS_G = 0x5913000u;
constexpr u32 WS_END = 0x6013000u;
constexpr u32 FL_F1K = 0u;
constexpr u32 FL_F1V = 3584u;
constexpr u32 FL_FS = 7168u;
constexpr u32 FL_FV = 9216u;
constexpr u32 FL_FP = 12800u;
constexpr u32 FL_FH = 16896u;
constexpr u32 FL_FG = 20480u;
constexpr int NFLAG = 216;
constexpr int LDS_BYTES = 130304;
constexpr int NPAGE = 96;
constexpr u32 SPIN_CAP = 30000;
#define PROBE_REPS 2
#define WARM_AHEAD 4
#define WARM_SLEEP 6
#define WARM_SLEEPV 12
#define WARM_WSTEP 0
#define WARM_STAG 6
constexpr float SCORE_SCALE = 0.0625f * 1.4426950408889634f;

__device__ __forceinline__ rsrc_t mk_rsrc(const void* p, u32 bytes) { return __builtin_amdgcn_make_buffer_rsrc((void*)p, 0, bytes, 0x00020000); }
__device__ __forceinline__ u32x4 ld16(rsrc_t r, u32 v, u32 s) { return __builtin_amdgcn_raw_buffer_load_b128(r, v, s, 0); }
__device__ __forceinline__ u32x4 ld16c(rsrc_t r, u32 v, u32 s) { return __builtin_amdgcn_raw_buffer_load_b128(r, v, s, 16); }
__device__ __forceinline__ void st16(u32x4 x, rsrc_t r, u32 v, u32 s) { __builtin_amdgcn_raw_buffer_store_b128(x, r, v, s, 0); }
__device__ __forceinline__ void st16c(u32x4 x, rsrc_t r, u32 v, u32 s) { __builtin_amdgcn_raw_buffer_store_b128(x, r, v, s, 16); }
__device__ __forceinline__ f32x4 mfma(u32x4 a, u32x4 b, f32x4 c) {
    return __builtin_amdgcn_mfma_f32_16x16x32_bf16(__builtin_bit_cast(bf16x8, a), __builtin_bit_cast(bf16x8, b), c, 0, 0, 0);
}
__device__ __forceinline__ u32 pk(float a, float b) { bf16x2 v = {(__bf16)a, (__bf16)b}; return __builtin_bit_cast(u32, v); }
__device__ __forceinline__ float lo(u32 x) { return __uint_as_float(x << 16); }
__device__ __forceinline__ float hi(u32 x) { return __uint_as_float(x & 0xffff0000u); }
__device__ __forceinline__ u32x4 pk8(f32x4 a, f32x4 b) { return u32x4{pk(a[0], a[1]), pk(a[2], a[3]), pk(b[0], b[1]), pk(b[2], b[3])}; }
__device__ __forceinline__ int kappa(int g, int e) { return 16 * (g >> 1) + 8 * ((e >> 1) & 1) + 4 * (g & 1) + 2 * (e >> 2) + (e & 1); }
__device__ __forceinline__ int lane_id() { int l; asm volatile("v_mbcnt_lo_u32_b32 %0, -1, 0\n\tv_mbcnt_hi_u32_b32 %0, -1, %0" : "=v"(l)); return l; }
template <int N> __device__ __forceinline__ void vm_wait() { asm volatile("s_waitcnt vmcnt(%0)" :: "i"(N) : "memory"); }
__device__ __forceinline__ void drain() { asm volatile("s_waitcnt vmcnt(0)" ::: "memory"); }
template <int C> __device__ __forceinline__ float dpp(float v) { return __builtin_bit_cast(float, __builtin_amdgcn_update_dpp(0, __builtin_bit_cast(int, v), C, 0xf, 0xf, true)); }
__device__ __forceinline__ float x16(float v, bool mx) {
    const auto r = __builtin_amdgcn_permlane16_swap(__float_as_uint(v), __float_as_uint(v), false, false);
    return mx ? fmaxf(__uint_as_float(r[0]), __uint_as_float(r[1])) : __uint_as_float(r[0]) + __uint_as_float(r[1]);
}
__device__ __forceinline__ float x32(float v, bool mx) {
    const auto r = __builtin_amdgcn_permlane32_swap(__float_as_uint(v), __float_as_uint(v), false, false);
    return mx ? fmaxf(__uint_as_float(r[0]), __uint_as_float(r[1])) : __uint_as_float(r[0]) + __uint_as_float(r[1]);
}
__device__ __forceinline__ float xsum(float v) { return x32(x16(v, false), false); }
__device__ __forceinline__ float xmax(float v) { return x32(x16(v, true), true); }

__device__ __forceinline__ void put_flag(rsrc_t ws, u32 off, u32 v, int lane) {
    if (lane == 0) __builtin_amdgcn_raw_buffer_store_b32(v, ws, off, 0, 16);
}
__device__ __forceinline__ bool wait_flags(rsrc_t ws, u32 off, int n, u32 need, u32& seen, int lane) {
    if (seen >= need) return true;
    const u32 a = off + 128u * (u32)min(lane, n - 1);
    for (u32 spin = 0; spin < SPIN_CAP; ++spin) {
        asm volatile("" ::: "memory");
        const u32 f = __builtin_amdgcn_raw_buffer_load_b32(ws, a, 0, 16);
        if (__builtin_amdgcn_ballot_w64(f < need) == 0) {
            seen = need;
#pragma unroll
            for (u32 step = 32; step; step >>= 1) if (__builtin_amdgcn_ballot_w64(f < seen + step) == 0) seen += step;
            asm volatile("" ::: "memory");
            return true;
        }
        __builtin_amdgcn_s_sleep(1);
    }
    return false;
}

#define TL(st, s, c)
#define KST(i)
#define CLOB10(p, a) p #a "0", p #a "1", p #a "2", p #a "3", p #a "4", p #a "5", p #a "6", p #a "7", p #a "8", p #a "9"
#define CLOB254(p) p "0", p "1", p "2", p "3", p "4", p "5", p "6", p "7", p "8", p "9", \
    CLOB10(p, 1), CLOB10(p, 2), CLOB10(p, 3), CLOB10(p, 4), CLOB10(p, 5), CLOB10(p, 6), CLOB10(p, 7), CLOB10(p, 8), CLOB10(p, 9), \
    CLOB10(p, 10), CLOB10(p, 11), CLOB10(p, 12), CLOB10(p, 13), CLOB10(p, 14), CLOB10(p, 15), CLOB10(p, 16), CLOB10(p, 17), CLOB10(p, 18), CLOB10(p, 19), \
    CLOB10(p, 20), CLOB10(p, 21), CLOB10(p, 22), CLOB10(p, 23), CLOB10(p, 24), p "250", p "251", p "252", p "253"
#define CHAIN_CLOBBERS CLOB254("v"), CLOB254("a"), "a254", "a255", "s28", "s29", "s30", "s31", "s33", "s34", "s35", "s36", "s37", "s38", "s39", CLOB10("s", 4), CLOB10("s", 5), CLOB10("s", 6), CLOB10("s", 7), "s96", "s97", "s98", "s99", "vcc", "scc", "memory"

struct Params {
    const unsigned short *x, *mem0, *wq, *wk, *wv, *wo, *w1, *b1, *w2, *b2;
    unsigned short* out;
    unsigned char* ws;
    u32 ws_bytes, ws0, ws1, wm, wv_t, pad;
};

__device__ __forceinline__ void dma16(rsrc_t r, LAS unsigned char* dst, u32 v, u32 s) { __builtin_amdgcn_raw_ptr_buffer_load_lds(r, dst, 16, v, s, 0, 0); }
__device__ __forceinline__ u32x4 tr_frag(LAS unsigned char* a, int second) {
    const u32x2 r0 = __builtin_bit_cast(u32x2, __builtin_amdgcn_ds_read_tr16_b64_v4i16((LAS s16x4*)a));
    const u32x2 r1 = __builtin_bit_cast(u32x2, __builtin_amdgcn_ds_read_tr16_b64_v4i16((LAS s16x4*)(a + second)));
    return u32x4{r0.x, r0.y, r1.x, r1.y};
}

__global__ void __launch_bounds__(256, 1) prep_kernel(Params p) {
    extern __shared__ __attribute__((aligned(16))) unsigned char smem[];
    LAS unsigned char* sm = (LAS unsigned char*)smem;
    const int b = blockIdx.x, w = __builtin_amdgcn_readfirstlane(threadIdx.x >> 6);
    if (b < 224) {
        const int q7 = ((b >> 3) * 37) >> 8, mi = (b & 7) * 4 + q7, part = (b >> 3) - 7 * q7, h = mi & 15;
        const u32 wlo = (u32)(size_t)p.ws, whi = (u32)((size_t)p.ws >> 32), img = WS_WIMG + (mi * 28 + part * 4) * 14336;
        if (mi < 16) {
            asm volatile(
    "s_mov_b32 s40, %0\n"
    "s_and_b32 s41, %1, 0xffff\n"
    "s_mov_b32 s42, %2\n"
    "s_mov_b32 s43, 0x00020000\n"
    "s_mov_b32 s44, %4\n"
    "s_and_b32 s45, %5, 0xffff\n"
    "s_mov_b32 s46, 3670016\n"
    "s_mov_b32 s47, 0x00020000\n"
    "s_mov_b32 s48, %6\n"
    "s_and_b32 s49, %7, 0xffff\n"
    "s_mov_b32 s50, 3670016\n"
    "s_mov_b32 s51, 0x00020000\n"
    "v_mbcnt_lo_u32_b32 v10, -1, 0\n"
    "v_mbcnt_hi_u32_b32 v10, -1, v10\n"
    "v_lshlrev_b32 v0, 4, v10\n"
    "v_lshrrev_b32 v2, 5, v10\n"
    "v_and_b32 v3, 31, v10\n"
    "v_lshlrev_b32 v4, 2, v2\n"
    "v_xor_b32 v3, v3, v4\n"
    "v_xor_b32 v4, 0, v3\n"
    "v_lshlrev_b32 v4, 4, v4\n"
    "v_lshl_add_u32 v16, v2, 13, v4\n"
    "v_xor_b32 v4, 8, v3\n"
    "v_lshlrev_b32 v4, 4, v4\n"
    "v_lshl_add_u32 v17, v2, 13, v4\n"
    "s_mul_i32 s57, %3, 8192\n"
    "s_mov_b32 m0, s57\n"
    "s_lshl_b32 s56, %9, 6\n"
    "s_lshl_b32 s57, %3, 4\n"
    "s_add_u32 s56, s56, s57\n"
    "s_lshl_b32 s56, s56, 13\n"
    "s_lshl_b32 s57, %8, 9\n"
    "s_add_u32 s56, s56, s57\n"
    "buffer_load_dwordx4 v16, s[48:51], s56 offen lds\n"
    "s_add_u32 m0, m0, 1024\n"
    "s_add_u32 s56, s56, 16384\n"
    "buffer_load_dwordx4 v16, s[48:51], s56 offen lds\n"
    "s_add_u32 m0, m0, 1024\n"
    "s_add_u32 s56, s56, 16384\n"
    "buffer_load_dwordx4 v16, s[48:51], s56 offen lds\n"
    "s_add_u32 m0, m0, 1024\n"
    "s_add_u32 s56, s56, 16384\n"
    "buffer_load_dwordx4 v16, s[48:51], s56 offen lds\n"
    "s_add_u32 m0, m0, 1024\n"
    "s_add_u32 s56, s56, 16384\n"
    "buffer_load_dwordx4 v17, s[48:51], s56 offen lds\n"
    "s_add_u32 m0, m0, 1024\n"
    "s_add_u32 s56, s56, 16384\n"
    "buffer_load_dwordx4 v17, s[48:51], s56 offen lds\n"
    "s_add_u32 m0, m0, 1024\n"
    "s_add_u32 s56, s56, 16384\n"
    "buffer_load_dwordx4 v17, s[48:51], s56 offen lds\n"
    "s_add_u32 m0, m0, 1024\n"
    "s_add_u32 s56, s56, 16384\n"
    "buffer_load_dwordx4 v17, s[48:51], s56 offen lds\n"
    "s_add_u32 m0, m0, 1024\n"
    "s_add_u32 s56, s56, 16384\n"
    "s_and_b32 s58, %3, 1\n"
    "s_lshl_b32 s52, %9, 1\n"
    "s_add_u32 s56, s52, s58\n"
    "s_lshl_b32 s56, s56, 18\n"
    "s_lshr_b32 s55, %3, 1\n"
    "s_lshl_b32 s57, s55, 17\n"
    "s_add_u32 s56, s56, s57\n"
    "s_lshl_b32 s57, %8, 9\n"
    "s_add_u32 s56, s56, s57\n"
    "s_lshl_b32 s58, s58, 14\n"
    "s_lshl_b32 s57, s55, 13\n"
    "s_add_u32 s57, s57, s58\n"
    "s_add_u32 m0, s57, 34816\n"
    "s_nop 0\n"
    "buffer_load_dwordx4 v16, s[44:47], s56 offen lds\n"
    "s_add_u32 m0, m0, 1024\n"
    "s_add_u32 s56, s56, 16384\n"
    "buffer_load_dwordx4 v16, s[44:47], s56 offen lds\n"
    "s_add_u32 m0, m0, 1024\n"
    "s_add_u32 s56, s56, 16384\n"
    "buffer_load_dwordx4 v16, s[44:47], s56 offen lds\n"
    "s_add_u32 m0, m0, 1024\n"
    "s_add_u32 s56, s56, 16384\n"
    "buffer_load_dwordx4 v16, s[44:47], s56 offen lds\n"
    "s_add_u32 m0, m0, 1024\n"
    "s_add_u32 s56, s56, 16384\n"
    "buffer_load_dwordx4 v17, s[44:47], s56 offen lds\n"
    "s_add_u32 m0, m0, 1024\n"
    "s_add_u32 s56, s56, 16384\n"
    "buffer_load_dwordx4 v17, s[44:47], s56 offen lds\n"
    "s_add_u32 m0, m0, 1024\n"
    "s_add_u32 s56, s56, 16384\n"
    "buffer_load_dwordx4 v17, s[44:47], s56 offen lds\n"
    "s_add_u32 m0, m0, 1024\n"
    "s_add_u32 s56, s56, 16384\n"
    "buffer_load_dwordx4 v17, s[44:47], s56 offen lds\n"
    "s_add_u32 m0, m0, 1024\n"
    "s_add_u32 s56, s56, 16384\n"
    "s_add_u32 s52, s52, %3\n"
    "s_cmp_ge_u32 s52, 14\n"
    "s_cselect_b32 s57, 14, 0\n"
    "s_sub_u32 s52, s52, s57\n"
    "s_mov_b32 s55, s52\n"
    "s_lshl_b32 s54, %3, 14\n"
    "s_add_u32 s54, s54, 34816\n"
    "v_and_b32 v11, 15, v10\n"
    "v_lshrrev_b32 v12, 4, v10\n"
    "v_and_b32 v13, 8, v11\n"
    "v_lshlrev_b32 v13, 1, v13\n"
    "v_and_b32 v14, 2, v11\n"
    "v_lshl_add_u32 v13, v14, 2, v13\n"
    "v_and_b32 v14, 5, v11\n"
    "v_add_u32 v13, v13, v14\n"
    "v_and_b32 v14, 3, v11\n"
    "v_lshl_add_u32 v14, v14, 2, v12\n"
    "v_xor_b32 v15, 0, v14\n"
    "v_lshlrev_b32 v15, 4, v15\n"
    "v_lshl_add_u32 v2, v13, 9, v15\n"
    "v_xor_b32 v15, 4, v14\n"
    "v_lshlrev_b32 v15, 4, v15\n"
    "v_lshl_add_u32 v3, v13, 9, v15\n"
    "v_xor_b32 v15, 8, v14\n"
    "v_lshlrev_b32 v15, 4, v15\n"
    "v_lshl_add_u32 v4, v13, 9, v15\n"
    "v_xor_b32 v15, 12, v14\n"
    "v_lshlrev_b32 v15, 4, v15\n"
    "v_lshl_add_u32 v5, v13, 9, v15\n"
    "s_mov_b32 s60, %11\n"
    "s_cmp_lt_u32 %3, 2\n"
    "s_cselect_b32 s53, 4, 3\n"
    "s_waitcnt vmcnt(0)\n"
    "s_barrier\n"
    "s_cbranch_scc1 LK0_%=_have\n"
    "s_lshl_b32 s56, s52, 18\n"
    "s_lshl_b32 s57, %8, 9\n"
    "s_add_u32 s56, s56, s57\n"
    "s_mov_b32 m0, s54\n"
    "s_cmp_eq_u32 s53, 0\n"
    "s_cselect_b32 s56, 0x70000000, s56\n"
    "buffer_load_dwordx4 v16, s[44:47], s56 offen lds\n"
    "s_add_u32 m0, m0, 1024\n"
    "s_add_u32 s56, s56, 16384\n"
    "buffer_load_dwordx4 v16, s[44:47], s56 offen lds\n"
    "s_add_u32 m0, m0, 1024\n"
    "s_add_u32 s56, s56, 16384\n"
    "buffer_load_dwordx4 v16, s[44:47], s56 offen lds\n"
    "s_add_u32 m0, m0, 1024\n"
    "s_add_u32 s56, s56, 16384\n"
    "buffer_load_dwordx4 v16, s[44:47], s56 offen lds\n"
    "s_add_u32 m0, m0, 1024\n"
    "s_add_u32 s56, s56, 16384\n"
    "buffer_load_dwordx4 v17, s[44:47], s56 offen lds\n"
    "s_add_u32 m0, m0, 1024\n"
    "s_add_u32 s56, s56, 16384\n"
    "buffer_load_dwordx4 v17, s[44:47], s56 offen lds\n"
    "s_add_u32 m0, m0, 1024\n"
    "s_add_u32 s56, s56, 16384\n"
    "buffer_load_dwordx4 v17, s[44:47], s56 offen lds\n"
    "s_add_u32 m0, m0, 1024\n"
    "s_add_u32 s56, s56, 16384\n"
    "buffer_load_dwordx4 v17, s[44:47], s56 offen lds\n"
    "s_add_u32 m0, m0, 1024\n"
    "s_add_u32 s56, s56, 16384\n"
    "buffer_load_dwordx4 v16, s[44:47], s56 offen lds\n"
    "s_add_u32 m0, m0, 1024\n"
    "s_add_u32 s56, s56, 16384\n"
    "buffer_load_dwordx4 v16, s[44:47], s56 offen lds\n"
    "s_add_u32 m0, m0, 1024\n"
    "s_add_u32 s56, s56, 16384\n"
    "buffer_load_dwordx4 v16, s[44:47], s56 offen lds\n"
    "s_add_u32 m0, m0, 1024\n"
    "s_add_u32 s56, s56, 16384\n"
    "buffer_load_dwordx4 v16, s[44:47], s56 offen lds\n"
    "s_add_u32 m0, m0, 1024\n"
    "s_add_u32 s56, s56, 16384\n"
    "buffer_load_dwordx4 v17, s[44:47], s56 offen lds\n"
    "s_add_u32 m0, m0, 1024\n"
    "s_add_u32 s56, s56, 16384\n"
    "buffer_load_dwordx4 v17, s[44:47], s56 offen lds\n"
    "s_add_u32 m0, m0, 1024\n"
    "s_add_u32 s56, s56, 16384\n"
    "buffer_load_dwordx4 v17, s[44:47], s56 offen lds\n"
    "s_add_u32 m0, m0, 1024\n"
    "s_add_u32 s56, s56, 16384\n"
    "buffer_load_dwordx4 v17, s[44:47], s56 offen lds\n"
    "s_add_u32 m0, m0, 1024\n"
    "s_add_u32 s56, s56, 16384\n"
    "LK0_%=_have:\n"
    "ds_read_b128 a[0:3], v2 offset:0\n"
    "ds_read_b128 a[4:7], v3 offset:0\n"
    "ds_read_b128 a[8:11], v4 offset:0\n"
    "ds_read_b128 a[12:15], v5 offset:0\n"
    "ds_read_b128 a[16:19], v2 offset:256\n"
    "ds_read_b128 a[20:23], v3 offset:256\n"
    "ds_read_b128 a[24:27], v4 offset:256\n"
    "ds_read_b128 a[28:31], v5 offset:256\n"
    "ds_read_b128 a[32:35], v2 offset:1024\n"
    "ds_read_b128 a[36:39], v3 offset:1024\n"
    "ds_read_b128 a[40:43], v4 offset:1024\n"
    "ds_read_b128 a[44:47], v5 offset:1024\n"
    "ds_read_b128 a[48:51], v2 offset:1280\n"
    "ds_read_b128 a[52:55], v3 offset:1280\n"
    "ds_read_b128 a[56:59], v4 offset:1280\n"
    "ds_read_b128 a[60:63], v5 offset:1280\n"
    "ds_read_b128 a[64:67], v2 offset:16384\n"
    "ds_read_b128 a[68:71], v3 offset:16384\n"
    "ds_read_b128 a[72:75], v4 offset:16384\n"
    "ds_read_b128 a[76:79], v5 offset:16384\n"
    "ds_read_b128 a[80:83], v2 offset:16640\n"
    "ds_read_b128 a[84:87], v3 offset:16640\n"
    "ds_read_b128 a[88:91], v4 offset:16640\n"
    "ds_read_b128 a[92:95], v5 offset:16640\n"
    "ds_read_b128 a[96:99], v2 offset:17408\n"
    "ds_read_b128 a[100:103], v3 offset:17408\n"
    "ds_read_b128 a[104:107], v4 offset:17408\n"
    "ds_read_b128 a[108:111], v5 offset:17408\n"
    "ds_read_b128 a[112:115], v2 offset:17664\n"
    "ds_read_b128 a[116:119], v3 offset:17664\n"
    "ds_read_b128 a[120:123], v4 offset:17664\n"
    "ds_read_b128 a[124:127], v5 offset:17664\n"
    "v_add_u32 v2, s54, v2\n"
    "v_add_u32 v3, s54, v3\n"
    "v_add_u32 v4, s54, v4\n"
    "v_add_u32 v5, s54, v5\n"
    "s_waitcnt vmcnt(0)\n"
    "ds_read_b128 v[32:35], v2 offset:0\n"
    "ds_read_b128 v[36:39], v2 offset:1024\n"
    "ds_read_b128 v[40:43], v3 offset:0\n"
    "ds_read_b128 v[44:47], v3 offset:1024\n"
    "ds_read_b128 v[48:51], v4 offset:0\n"
    "ds_read_b128 v[52:55], v4 offset:1024\n"
    "ds_read_b128 v[56:59], v5 offset:0\n"
    "ds_read_b128 v[60:63], v5 offset:1024\n"
    "ds_read_b128 v[64:67], v2 offset:256\n"
    "ds_read_b128 v[68:71], v2 offset:1280\n"
    "ds_read_b128 v[72:75], v3 offset:256\n"
    "ds_read_b128 v[76:79], v3 offset:1280\n"
    "ds_read_b128 v[80:83], v4 offset:256\n"
    "ds_read_b128 v[84:87], v4 offset:1280\n"
    "ds_read_b128 v[88:91], v5 offset:256\n"
    "ds_read_b128 v[92:95], v5 offset:1280\n"
    "s_waitcnt lgkmcnt(0)\n"
    "LK0_%=_loop:\n"
    "s_lshl_b32 s59, s52, 10\n"
    "s_add_u32 s59, s59, %10\n"
    "s_add_u32 s52, s52, 4\n"
    "s_cmp_ge_u32 s52, 14\n"
    "s_cselect_b32 s57, 14, 0\n"
    "s_sub_u32 s52, s52, s57\n"
    "s_sub_u32 s53, s53, 1\n"
    "s_lshl_b32 s56, s52, 18\n"
    "s_lshl_b32 s57, %8, 9\n"
    "s_add_u32 s56, s56, s57\n"
    "s_mov_b32 m0, s54\n"
    "s_cmp_eq_u32 s53, 0\n"
    "s_cselect_b32 s56, 0x70000000, s56\n"
    "v_mfma_f32_16x16x32_bf16 v[160:163], v[32:35], a[0:3], 0\n"
    "buffer_load_dwordx4 v16, s[44:47], s56 offen lds\n"
    "s_add_u32 m0, m0, 1024\n"
    "s_add_u32 s56, s56, 16384\n"
    "v_mfma_f32_16x16x32_bf16 v[164:167], v[36:39], a[0:3], 0\n"
    "v_mfma_f32_16x16x32_bf16 v[160:163], v[40:43], a[4:7], v[160:163]\n"
    "buffer_load_dwordx4 v16, s[44:47], s56 offen lds\n"
    "s_add_u32 m0, m0, 1024\n"
    "s_add_u32 s56, s56, 16384\n"
    "v_mfma_f32_16x16x32_bf16 v[164:167], v[44:47], a[4:7], v[164:167]\n"
    "v_mfma_f32_16x16x32_bf16 v[160:163], v[48:51], a[8:11], v[160:163]\n"
    "buffer_load_dwordx4 v16, s[44:47], s56 offen lds\n"
    "s_add_u32 m0, m0, 1024\n"
    "s_add_u32 s56, s56, 16384\n"
    "v_mfma_f32_16x16x32_bf16 v[164:167], v[52:55], a[8:11], v[164:167]\n"
    "v_mfma_f32_16x16x32_bf16 v[160:163], v[56:59], a[12:15], v[160:163]\n"
    "buffer_load_dwordx4 v16, s[44:47], s56 offen lds\n"
    "s_add_u32 m0, m0, 1024\n"
    "s_add_u32 s56, s56, 16384\n"
    "v_mfma_f32_16x16x32_bf16 v[164:167], v[60:63], a[12:15], v[164:167]\n"
    "v_mfma_f32_16x16x32_bf16 v[160:163], v[64:67], a[16:19], v[160:163]\n"
    "buffer_load_dwordx4 v17, s[44:47], s56 offen lds\n"
    "s_add_u32 m0, m0, 1024\n"
    "s_add_u32 s56, s56, 16384\n"
    "v_mfma_f32_16x16x32_bf16 v[164:167], v[68:71], a[16:19], v[164:167]\n"
    "v_mfma_f32_16x16x32_bf16 v[160:163], v[72:75], a[20:23], v[160:163]\n"
    "buffer_load_dwordx4 v17, s[44:47], s56 offen lds\n"
    "s_add_u32 m0, m0, 1024\n"
    "s_add_u32 s56, s56, 16384\n"
    "v_mfma_f32_16x16x32_bf16 v[164:167], v[76:79], a[20:23], v[164:167]\n"
    "v_mfma_f32_16x16x32_bf16 v[160:163], v[80:83], a[24:27], v[160:163]\n"
    "buffer_load_dwordx4 v17, s[44:47], s56 offen lds\n"
    "s_add_u32 m0, m0, 1024\n"
    "s_add_u32 s56, s56, 16384\n"
    "v_mfma_f32_16x16x32_bf16 v[164:167], v[84:87], a[24:27], v[164:167]\n"
    "v_mfma_f32_16x16x32_bf16 v[160:163], v[88:91], a[28:31], v[160:163]\n"
    "buffer_load_dwordx4 v17, s[44:47], s56 offen lds\n"
    "s_add_u32 m0, m0, 1024\n"
    "s_add_u32 s56, s56, 16384\n"
    "v_mfma_f32_16x16x32_bf16 v[164:167], v[92:95], a[28:31], v[164:167]\n"
    "v_mfma_f32_16x16x32_bf16 v[168:171], v[32:35], a[32:35], 0\n"
    "buffer_load_dwordx4 v16, s[44:47], s56 offen lds\n"
    "s_add_u32 m0, m0, 1024\n"
    "s_add_u32 s56, s56, 16384\n"
    "v_mfma_f32_16x16x32_bf16 v[172:175], v[36:39], a[32:35], 0\n"
    "v_mfma_f32_16x16x32_bf16 v[168:171], v[40:43], a[36:39], v[168:171]\n"
    "buffer_load_dwordx4 v16, s[44:47], s56 offen lds\n"
    "s_add_u32 m0, m0, 1024\n"
    "s_add_u32 s56, s56, 16384\n"
    "v_mfma_f32_16x16x32_bf16 v[172:175], v[44:47], a[36:39], v[172:175]\n"
    "v_mfma_f32_16x16x32_bf16 v[168:171], v[48:51], a[40:43], v[168:171]\n"
    "buffer_load_dwordx4 v16, s[44:47], s56 offen lds\n"
    "s_add_u32 m0, m0, 1024\n"
    "s_add_u32 s56, s56, 16384\n"
    "v_mfma_f32_16x16x32_bf16 v[172:175], v[52:55], a[40:43], v[172:175]\n"
    "v_mfma_f32_16x16x32_bf16 v[168:171], v[56:59], a[44:47], v[168:171]\n"
    "buffer_load_dwordx4 v16, s[44:47], s56 offen lds\n"
    "s_add_u32 m0, m0, 1024\n"
    "s_add_u32 s56, s56, 16384\n"
    "v_mfma_f32_16x16x32_bf16 v[172:175], v[60:63], a[44:47], v[172:175]\n"
    "v_mul_f32 v160, s60, v160\n"
    "v_mul_f32 v161, s60, v161\n"
    "v_mfma_f32_16x16x32_bf16 v[168:171], v[64:67], a[48:51], v[168:171]\n"
    "buffer_load_dwordx4 v17, s[44:47], s56 offen lds\n"
    "s_add_u32 m0, m0, 1024\n"
    "s_add_u32 s56, s56, 16384\n"
    "v_mfma_f32_16x16x32_bf16 v[172:175], v[68:71], a[48:51], v[172:175]\n"
    "v_mul_f32 v162, s60, v162\n"
    "v_mul_f32 v163, s60, v163\n"
    "v_mfma_f32_16x16x32_bf16 v[168:171], v[72:75], a[52:55], v[168:171]\n"
    "buffer_load_dwordx4 v17, s[44:47], s56 offen lds\n"
    "s_add_u32 m0, m0, 1024\n"
    "s_add_u32 s56, s56, 16384\n"
    "v_mfma_f32_16x16x32_bf16 v[172:175], v[76:79], a[52:55], v[172:175]\n"
    "v_mul_f32 v164, s60, v164\n"
    "v_mul_f32 v165, s60, v165\n"
    "v_mfma_f32_16x16x32_bf16 v[168:171], v[80:83], a[56:59], v[168:171]\n"
    "buffer_load_dwordx4 v17, s[44:47], s56 offen lds\n"
    "s_add_u32 m0, m0, 1024\n"
    "s_add_u32 s56, s56, 16384\n"
    "v_mfma_f32_16x16x32_bf16 v[172:175], v[84:87], a[56:59], v[172:175]\n"
    "v_mul_f32 v166, s60, v166\n"
    "v_mul_f32 v167, s60, v167\n"
    "v_mfma_f32_16x16x32_bf16 v[168:171], v[88:91], a[60:63], v[168:171]\n"
    "buffer_load_dwordx4 v17, s[44:47], s56 offen lds\n"
    "s_add_u32 m0, m0, 1024\n"
    "s_add_u32 s56, s56, 16384\n"
    "v_mfma_f32_16x16x32_bf16 v[172:175], v[92:95], a[60:63], v[172:175]\n"
    "v_cvt_pk_bf16_f32 v224, v160, v161\n"
    "v_cvt_pk_bf16_f32 v225, v162, v163\n"
    "v_mfma_f32_16x16x32_bf16 v[176:179], v[32:35], a[64:67], 0\n"
    "v_cvt_pk_bf16_f32 v226, v164, v165\n"
    "v_cvt_pk_bf16_f32 v227, v166, v167\n"
    "v_mfma_f32_16x16x32_bf16 v[180:183], v[36:39], a[64:67], 0\n"
    "s_add_u32 s58, s59, 0\n"
    "buffer_store_dwordx4 v[224:227], v0, s[40:43], s58 offen sc1\n"
    "v_mfma_f32_16x16x32_bf16 v[176:179], v[40:43], a[68:71], v[176:179]\n"
    "v_mfma_f32_16x16x32_bf16 v[180:183], v[44:47], a[68:71], v[180:183]\n"
    "v_mfma_f32_16x16x32_bf16 v[176:179], v[48:51], a[72:75], v[176:179]\n"
    "v_mfma_f32_16x16x32_bf16 v[180:183], v[52:55], a[72:75], v[180:183]\n"
    "v_mfma_f32_16x16x32_bf16 v[176:179], v[56:59], a[76:79], v[176:179]\n"
    "v_mul_f32 v168, s60, v168\n"
    "v_mul_f32 v169, s60, v169\n"
    "v_mfma_f32_16x16x32_bf16 v[180:183], v[60:63], a[76:79], v[180:183]\n"
    "v_mul_f32 v170, s60, v170\n"
    "v_mul_f32 v171, s60, v171\n"
    "v_mfma_f32_16x16x32_bf16 v[176:179], v[64:67], a[80:83], v[176:179]\n"
    "v_mul_f32 v172, s60, v172\n"
    "v_mul_f32 v173, s60, v173\n"
    "v_mfma_f32_16x16x32_bf16 v[180:183], v[68:71], a[80:83], v[180:183]\n"
    "v_mul_f32 v174, s60, v174\n"
    "v_mul_f32 v175, s60, v175\n"
    "v_mfma_f32_16x16x32_bf16 v[176:179], v[72:75], a[84:87], v[176:179]\n"
    "v_cvt_pk_bf16_f32 v228, v168, v169\n"
    "v_cvt_pk_bf16_f32 v229, v170, v171\n"
    "v_mfma_f32_16x16x32_bf16 v[180:183], v[76:79], a[84:87], v[180:183]\n"
    "v_cvt_pk_bf16_f32 v230, v172, v173\n"
    "v_cvt_pk_bf16_f32 v231, v174, v175\n"
    "v_mfma_f32_16x16x32_bf16 v[176:179], v[80:83], a[88:91], v[176:179]\n"
    "s_add_u32 s58, s59, 14336\n"
    "buffer_store_dwordx4 v[228:231], v0, s[40:43], s58 offen sc1\n"
    "v_mfma_f32_16x16x32_bf16 v[180:183], v[84:87], a[88:91], v[180:183]\n"
    "v_mfma_f32_16x16x32_bf16 v[176:179], v[88:91], a[92:95], v[176:179]\n"
    "v_mfma_f32_16x16x32_bf16 v[180:183], v[92:95], a[92:95], v[180:183]\n"
    "v_mfma_f32_16x16x32_bf16 v[184:187], v[32:35], a[96:99], 0\n"
    "v_mfma_f32_16x16x32_bf16 v[188:191], v[36:39], a[96:99], 0\n"
    "v_mfma_f32_16x16x32_bf16 v[184:187], v[40:43], a[100:103], v[184:187]\n"
    "v_mfma_f32_16x16x32_bf16 v[188:191], v[44:47], a[100:103], v[188:191]\n"
    "v_mfma_f32_16x16x32_bf16 v[184:187], v[48:51], a[104:107], v[184:187]\n"
    "v_mfma_f32_16x16x32_bf16 v[188:191], v[52:55], a[104:107], v[188:191]\n"
    "v_mfma_f32_16x16x32_bf16 v[184:187], v[56:59], a[108:111], v[184:187]\n"
    "v_mul_f32 v176, s60, v176\n"
    "v_mul_f32 v177, s60, v177\n"
    "v_mfma_f32_16x16x32_bf16 v[188:191], v[60:63], a[108:111], v[188:191]\n"
    "v_mul_f32 v178, s60, v178\n"
    "v_mul_f32 v179, s60, v179\n"
    "s_waitcnt vmcnt(2)\n"
    "v_mfma_f32_16x16x32_bf16 v[184:187], v[64:67], a[112:115], v[184:187]\n"
    "ds_read_b128 v[96:99], v2 offset:0\n"
    "ds_read_b128 v[100:103], v2 offset:1024\n"
    "v_mfma_f32_16x16x32_bf16 v[188:191], v[68:71], a[112:115], v[188:191]\n"
    "ds_read_b128 v[104:107], v3 offset:0\n"
    "ds_read_b128 v[108:111], v3 offset:1024\n"
    "v_mfma_f32_16x16x32_bf16 v[184:187], v[72:75], a[116:119], v[184:187]\n"
    "ds_read_b128 v[112:115], v4 offset:0\n"
    "ds_read_b128 v[116:119], v4 offset:1024\n"
    "v_mfma_f32_16x16x32_bf16 v[188:191], v[76:79], a[116:119], v[188:191]\n"
    "ds_read_b128 v[120:123], v5 offset:0\n"
    "ds_read_b128 v[124:127], v5 offset:1024\n"
    "v_mfma_f32_16x16x32_bf16 v[184:187], v[80:83], a[120:123], v[184:187]\n"
    "ds_read_b128 v[128:131], v2 offset:256\n"
    "ds_read_b128 v[132:135], v2 offset:1280\n"
    "v_mfma_f32_16x16x32_bf16 v[188:191], v[84:87], a[120:123], v[188:191]\n"
    "ds_read_b128 v[136:139], v3 offset:256\n"
    "ds_read_b128 v[140:143], v3 offset:1280\n"
    "v_mfma_f32_16x16x32_bf16 v[184:187], v[88:91], a[124:127], v[184:187]\n"
    "ds_read_b128 v[144:147], v4 offset:256\n"
    "ds_read_b128 v[148:151], v4 offset:1280\n"
    "v_mfma_f32_16x16x32_bf16 v[188:191], v[92:95], a[124:127], v[188:191]\n"
    "ds_read_b128 v[152:155], v5 offset:256\n"
    "ds_read_b128 v[156:159], v5 offset:1280\n"
    "v_mul_f32 v180, s60, v180\n"
    "v_mul_f32 v181, s60, v181\n"
    "v_mul_f32 v182, s60, v182\n"
    "v_mul_f32 v183, s60, v183\n"
    "v_cvt_pk_bf16_f32 v232, v176, v177\n"
    "v_cvt_pk_bf16_f32 v233, v178, v179\n"
    "v_cvt_pk_bf16_f32 v234, v180, v181\n"
    "v_cvt_pk_bf16_f32 v235, v182, v183\n"
    "s_add_u32 s58, s59, 28672\n"
    "buffer_store_dwordx4 v[232:235], v0, s[40:43], s58 offen sc1\n"
    "v_mul_f32 v184, s60, v184\n"
    "v_mul_f32 v185, s60, v185\n"
    "v_mul_f32 v186, s60, v186\n"
    "v_mul_f32 v187, s60, v187\n"
    "v_mul_f32 v188, s60, v188\n"
    "v_mul_f32 v189, s60, v189\n"
    "v_mul_f32 v190, s60, v190\n"
    "v_mul_f32 v191, s60, v191\n"
    "v_cvt_pk_bf16_f32 v236, v184, v185\n"
    "v_cvt_pk_bf16_f32 v237, v186, v187\n"
    "v_cvt_pk_bf16_f32 v238, v188, v189\n"
    "v_cvt_pk_bf16_f32 v239, v190, v191\n"
    "s_add_u32 s58, s59, 43008\n"
    "buffer_store_dwordx4 v[236:239], v0, s[40:43], s58 offen sc1\n"
    "s_waitcnt lgkmcnt(0)\n"
    "s_cmp_eq_u32 s53, 0\n"
    "s_cbranch_scc1 LK0_%=_end\n"
    "s_lshl_b32 s59, s52, 10\n"
    "s_add_u32 s59, s59, %10\n"
    "s_add_u32 s52, s52, 4\n"
    "s_cmp_ge_u32 s52, 14\n"
    "s_cselect_b32 s57, 14, 0\n"
    "s_sub_u32 s52, s52, s57\n"
    "s_sub_u32 s53, s53, 1\n"
    "s_lshl_b32 s56, s52, 18\n"
    "s_lshl_b32 s57, %8, 9\n"
    "s_add_u32 s56, s56, s57\n"
    "s_mov_b32 m0, s54\n"
    "s_cmp_eq_u32 s53, 0\n"
    "s_cselect_b32 s56, 0x70000000, s56\n"
    "v_mfma_f32_16x16x32_bf16 v[160:163], v[96:99], a[0:3], 0\n"
    "buffer_load_dwordx4 v16, s[44:47], s56 offen lds\n"
    "s_add_u32 m0, m0, 1024\n"
    "s_add_u32 s56, s56, 16384\n"
    "v_mfma_f32_16x16x32_bf16 v[164:167], v[100:103], a[0:3], 0\n"
    "v_mfma_f32_16x16x32_bf16 v[160:163], v[104:107], a[4:7], v[160:163]\n"
    "buffer_load_dwordx4 v16, s[44:47], s56 offen lds\n"
    "s_add_u32 m0, m0, 1024\n"
    "s_add_u32 s56, s56, 16384\n"
    "v_mfma_f32_16x16x32_bf16 v[164:167], v[108:111], a[4:7], v[164:167]\n"
    "v_mfma_f32_16x16x32_bf16 v[160:163], v[112:115], a[8:11], v[160:163]\n"
    "buffer_load_dwordx4 v16, s[44:47], s56 offen lds\n"
    "s_add_u32 m0, m0, 1024\n"
    "s_add_u32 s56, s56, 16384\n"
    "v_mfma_f32_16x16x32_bf16 v[164:167], v[116:119], a[8:11], v[164:167]\n"
    "v_mfma_f32_16x16x32_bf16 v[160:163], v[120:123], a[12:15], v[160:163]\n"
    "buffer_load_dwordx4 v16, s[44:47], s56 offen lds\n"
    "s_add_u32 m0, m0, 1024\n"
    "s_add_u32 s56, s56, 16384\n"
    "v_mfma_f32_16x16x32_bf16 v[164:167], v[124:127], a[12:15], v[164:167]\n"
    "v_mfma_f32_16x16x32_bf16 v[160:163], v[128:131], a[16:19], v[160:163]\n"
    "buffer_load_dwordx4 v17, s[44:47], s56 offen lds\n"
    "s_add_u32 m0, m0, 1024\n"
    "s_add_u32 s56, s56, 16384\n"
    "v_mfma_f32_16x16x32_bf16 v[164:167], v[132:135], a[16:19], v[164:167]\n"
    "v_mfma_f32_16x16x32_bf16 v[160:163], v[136:139], a[20:23], v[160:163]\n"
    "buffer_load_dwordx4 v17, s[44:47], s56 offen lds\n"
    "s_add_u32 m0, m0, 1024\n"
    "s_add_u32 s56, s56, 16384\n"
    "v_mfma_f32_16x16x32_bf16 v[164:167], v[140:143], a[20:23], v[164:167]\n"
    "v_mfma_f32_16x16x32_bf16 v[160:163], v[144:147], a[24:27], v[160:163]\n"
    "buffer_load_dwordx4 v17, s[44:47], s56 offen lds\n"
    "s_add_u32 m0, m0, 1024\n"
    "s_add_u32 s56, s56, 16384\n"
    "v_mfma_f32_16x16x32_bf16 v[164:167], v[148:151], a[24:27], v[164:167]\n"
    "v_mfma_f32_16x16x32_bf16 v[160:163], v[152:155], a[28:31], v[160:163]\n"
    "buffer_load_dwordx4 v17, s[44:47], s56 offen lds\n"
    "s_add_u32 m0, m0, 1024\n"
    "s_add_u32 s56, s56, 16384\n"
    "v_mfma_f32_16x16x32_bf16 v[164:167], v[156:159], a[28:31], v[164:167]\n"
    "v_mfma_f32_16x16x32_bf16 v[168:171], v[96:99], a[32:35], 0\n"
    "buffer_load_dwordx4 v16, s[44:47], s56 offen lds\n"
    "s_add_u32 m0, m0, 1024\n"
    "s_add_u32 s56, s56, 16384\n"
    "v_mfma_f32_16x16x32_bf16 v[172:175], v[100:103], a[32:35], 0\n"
    "v_mfma_f32_16x16x32_bf16 v[168:171], v[104:107], a[36:39], v[168:171]\n"
    "buffer_load_dwordx4 v16, s[44:47], s56 offen lds\n"
    "s_add_u32 m0, m0, 1024\n"
    "s_add_u32 s56, s56, 16384\n"
    "v_mfma_f32_16x16x32_bf16 v[172:175], v[108:111], a[36:39], v[172:175]\n"
    "v_mfma_f32_16x16x32_bf16 v[168:171], v[112:115], a[40:43], v[168:171]\n"
    "buffer_load_dwordx4 v16, s[44:47], s56 offen lds\n"
    "s_add_u32 m0, m0, 1024\n"
    "s_add_u32 s56, s56, 16384\n"
    "v_mfma_f32_16x16x32_bf16 v[172:175], v[116:119], a[40:43], v[172:175]\n"
    "v_mfma_f32_16x16x32_bf16 v[168:171], v[120:123], a[44:47], v[168:171]\n"
    "buffer_load_dwordx4 v16, s[44:47], s56 offen lds\n"
    "s_add_u32 m0, m0, 1024\n"
    "s_add_u32 s56, s56, 16384\n"
    "v_mfma_f32_16x16x32_bf16 v[172:175], v[124:127], a[44:47], v[172:175]\n"
    "v_mul_f32 v160, s60, v160\n"
    "v_mul_f32 v161, s60, v161\n"
    "v_mfma_f32_16x16x32_bf16 v[168:171], v[128:131], a[48:51], v[168:171]\n"
    "buffer_load_dwordx4 v17, s[44:47], s56 offen lds\n"
    "s_add_u32 m0, m0, 1024\n"
    "s_add_u32 s56, s56, 16384\n"
    "v_mfma_f32_16x16x32_bf16 v[172:175], v[132:135], a[48:51], v[172:175]\n"
    "v_mul_f32 v162, s60, v162\n"
    "v_mul_f32 v163, s60, v163\n"
    "v_mfma_f32_16x16x32_bf16 v[168:171], v[136:139], a[52:55], v[168:171]\n"
    "buffer_load_dwordx4 v17, s[44:47], s56 offen lds\n"
    "s_add_u32 m0, m0, 1024\n"
    "s_add_u32 s56, s56, 16384\n"
    "v_mfma_f32_16x16x32_bf16 v[172:175], v[140:143], a[52:55], v[172:175]\n"
    "v_mul_f32 v164, s60, v164\n"
    "v_mul_f32 v165, s60, v165\n"
    "v_mfma_f32_16x16x32_bf16 v[168:171], v[144:147], a[56:59], v[168:171]\n"
    "buffer_load_dwordx4 v17, s[44:47], s56 offen lds\n"
    "s_add_u32 m0, m0, 1024\n"
    "s_add_u32 s56, s56, 16384\n"
    "v_mfma_f32_16x16x32_bf16 v[172:175], v[148:151], a[56:59], v[172:175]\n"
    "v_mul_f32 v166, s60, v166\n"
    "v_mul_f32 v167, s60, v167\n"
    "v_mfma_f32_16x16x32_bf16 v[168:171], v[152:155], a[60:63], v[168:171]\n"
    "buffer_load_dwordx4 v17, s[44:47], s56 offen lds\n"
    "s_add_u32 m0, m0, 1024\n"
    "s_add_u32 s56, s56, 16384\n"
    "v_mfma_f32_16x16x32_bf16 v[172:175], v[156:159], a[60:63], v[172:175]\n"
    "v_cvt_pk_bf16_f32 v224, v160, v161\n"
    "v_cvt_pk_bf16_f32 v225, v162, v163\n"
    "v_mfma_f32_16x16x32_bf16 v[176:179], v[96:99], a[64:67], 0\n"
    "v_cvt_pk_bf16_f32 v226, v164, v165\n"
    "v_cvt_pk_bf16_f32 v227, v166, v167\n"
    "v_mfma_f32_16x16x32_bf16 v[180:183], v[100:103], a[64:67], 0\n"
    "s_add_u32 s58, s59, 0\n"
    "buffer_store_dwordx4 v[224:227], v0, s[40:43], s58 offen sc1\n"
    "v_mfma_f32_16x16x32_bf16 v[176:179], v[104:107], a[68:71], v[176:179]\n"
    "v_mfma_f32_16x16x32_bf16 v[180:183], v[108:111], a[68:71], v[180:183]\n"
    "v_mfma_f32_16x16x32_bf16 v[176:179], v[112:115], a[72:75], v[176:179]\n"
    "v_mfma_f32_16x16x32_bf16 v[180:183], v[116:119], a[72:75], v[180:183]\n"
    "v_mfma_f32_16x16x32_bf16 v[176:179], v[120:123], a[76:79], v[176:179]\n"
    "v_mul_f32 v168, s60, v168\n"
    "v_mul_f32 v169, s60, v169\n"
    "v_mfma_f32_16x16x32_bf16 v[180:183], v[124:127], a[76:79], v[180:183]\n"
    "v_mul_f32 v170, s60, v170\n"
    "v_mul_f32 v171, s60, v171\n"
    "v_mfma_f32_16x16x32_bf16 v[176:179], v[128:131], a[80:83], v[176:179]\n"
    "v_mul_f32 v172, s60, v172\n"
    "v_mul_f32 v173, s60, v173\n"
    "v_mfma_f32_16x16x32_bf16 v[180:183], v[132:135], a[80:83], v[180:183]\n"
    "v_mul_f32 v174, s60, v174\n"
    "v_mul_f32 v175, s60, v175\n"
    "v_mfma_f32_16x16x32_bf16 v[176:179], v[136:139], a[84:87], v[176:179]\n"
    "v_cvt_pk_bf16_f32 v228, v168, v169\n"
    "v_cvt_pk_bf16_f32 v229, v170, v171\n"
    "v_mfma_f32_16x16x32_bf16 v[180:183], v[140:143], a[84:87], v[180:183]\n"
    "v_cvt_pk_bf16_f32 v230, v172, v173\n"
    "v_cvt_pk_bf16_f32 v231, v174, v175\n"
    "v_mfma_f32_16x16x32_bf16 v[176:179], v[144:147], a[88:91], v[176:179]\n"
    "s_add_u32 s58, s59, 14336\n"
    "buffer_store_dwordx4 v[228:231], v0, s[40:43], s58 offen sc1\n"
    "v_mfma_f32_16x16x32_bf16 v[180:183], v[148:151], a[88:91], v[180:183]\n"
    "v_mfma_f32_16x16x32_bf16 v[176:179], v[152:155], a[92:95], v[176:179]\n"
    "v_mfma_f32_16x16x32_bf16 v[180:183], v[156:159], a[92:95], v[180:183]\n"
    "v_mfma_f32_16x16x32_bf16 v[184:187], v[96:99], a[96:99], 0\n"
    "v_mfma_f32_16x16x32_bf16 v[188:191], v[100:103], a[96:99], 0\n"
    "v_mfma_f32_16x16x32_bf16 v[184:187], v[104:107], a[100:103], v[184:187]\n"
    "v_mfma_f32_16x16x32_bf16 v[188:191], v[108:111], a[100:103], v[188:191]\n"
    "v_mfma_f32_16x16x32_bf16 v[184:187], v[112:115], a[104:107], v[184:187]\n"
    "v_mfma_f32_16x16x32_bf16 v[188:191], v[116:119], a[104:107], v[188:191]\n"
    "v_mfma_f32_16x16x32_bf16 v[184:187], v[120:123], a[108:111], v[184:187]\n"
    "v_mul_f32 v176, s60, v176\n"
    "v_mul_f32 v177, s60, v177\n"
    "v_mfma_f32_16x16x32_bf16 v[188:191], v[124:127], a[108:111], v[188:191]\n"
    "v_mul_f32 v178, s60, v178\n"
    "v_mul_f32 v179, s60, v179\n"
    "s_waitcnt vmcnt(2)\n"
    "v_mfma_f32_16x16x32_bf16 v[184:187], v[128:131], a[112:115], v[184:187]\n"
    "ds_read_b128 v[32:35], v2 offset:0\n"
    "ds_read_b128 v[36:39], v2 offset:1024\n"
    "v_mfma_f32_16x16x32_bf16 v[188:191], v[132:135], a[112:115], v[188:191]\n"
    "ds_read_b128 v[40:43], v3 offset:0\n"
    "ds_read_b128 v[44:47], v3 offset:1024\n"
    "v_mfma_f32_16x16x32_bf16 v[184:187], v[136:139], a[116:119], v[184:187]\n"
    "ds_read_b128 v[48:51], v4 offset:0\n"
    "ds_read_b128 v[52:55], v4 offset:1024\n"
    "v_mfma_f32_16x16x32_bf16 v[188:191], v[140:143], a[116:119], v[188:191]\n"
    "ds_read_b128 v[56:59], v5 offset:0\n"
    "ds_read_b128 v[60:63], v5 offset:1024\n"
    "v_mfma_f32_16x16x32_bf16 v[184:187], v[144:147], a[120:123], v[184:187]\n"
    "ds_read_b128 v[64:67], v2 offset:256\n"
    "ds_read_b128 v[68:71], v2 offset:1280\n"
    "v_mfma_f32_16x16x32_bf16 v[188:191], v[148:151], a[120:123], v[188:191]\n"
    "ds_read_b128 v[72:75], v3 offset:256\n"
    "ds_read_b128 v[76:79], v3 offset:1280\n"
    "v_mfma_f32_16x16x32_bf16 v[184:187], v[152:155], a[124:127], v[184:187]\n"
    "ds_read_b128 v[80:83], v4 offset:256\n"
    "ds_read_b128 v[84:87], v4 offset:1280\n"
    "v_mfma_f32_16x16x32_bf16 v[188:191], v[156:159], a[124:127], v[188:191]\n"
    "ds_read_b128 v[88:91], v5 offset:256\n"
    "ds_read_b128 v[92:95], v5 offset:1280\n"
    "v_mul_f32 v180, s60, v180\n"
    "v_mul_f32 v181, s60, v181\n"
    "v_mul_f32 v182, s60, v182\n"
    "v_mul_f32 v183, s60, v183\n"
    "v_cvt_pk_bf16_f32 v232, v176, v177\n"
    "v_cvt_pk_bf16_f32 v233, v178, v179\n"
    "v_cvt_pk_bf16_f32 v234, v180, v181\n"
    "v_cvt_pk_bf16_f32 v235, v182, v183\n"
    "s_add_u32 s58, s59, 28672\n"
    "buffer_store_dwordx4 v[232:235], v0, s[40:43], s58 offen sc1\n"
    "v_mul_f32 v184, s60, v184\n"
    "v_mul_f32 v185, s60, v185\n"
    "v_mul_f32 v186, s60, v186\n"
    "v_mul_f32 v187, s60, v187\n"
    "v_mul_f32 v188, s60, v188\n"
    "v_mul_f32 v189, s60, v189\n"
    "v_mul_f32 v190, s60, v190\n"
    "v_mul_f32 v191, s60, v191\n"
    "v_cvt_pk_bf16_f32 v236, v184, v185\n"
    "v_cvt_pk_bf16_f32 v237, v186, v187\n"
    "v_cvt_pk_bf16_f32 v238, v188, v189\n"
    "v_cvt_pk_bf16_f32 v239, v190, v191\n"
    "s_add_u32 s58, s59, 43008\n"
    "buffer_store_dwordx4 v[236:239], v0, s[40:43], s58 offen sc1\n"
    "s_waitcnt lgkmcnt(0)\n"
    "s_cmp_lg_u32 s53, 0\n"
    "s_cbranch_scc1 LK0_%=_loop\n"
    "LK0_%=_end:\n"
    "s_endpgm\n"
            :: "s"(wlo), "s"(whi), "s"(p.ws_bytes), "s"(w), "s"((u32)(size_t)p.wk), "s"((u32)((size_t)p.wk >> 32)), "s"((u32)(size_t)p.wq), "s"((u32)((size_t)p.wq >> 32)),
               "s"(h), "s"(part), "s"(img), "s"(SCORE_SCALE), "s"(WS_DBG + 200000 + (768 + b) * 4)
            : CHAIN_CLOBBERS);
        } else {
            asm volatile(
    "s_mov_b32 s40, %0\n"
    "s_and_b32 s41, %1, 0xffff\n"
    "s_mov_b32 s42, %2\n"
    "s_mov_b32 s43, 0x00020000\n"
    "s_mov_b32 s44, %4\n"
    "s_and_b32 s45, %5, 0xffff\n"
    "s_mov_b32 s46, 3670016\n"
    "s_mov_b32 s47, 0x00020000\n"
    "s_mov_b32 s48, %6\n"
    "s_and_b32 s49, %7, 0xffff\n"
    "s_mov_b32 s50, 3670016\n"
    "s_mov_b32 s51, 0x00020000\n"
    "v_mbcnt_lo_u32_b32 v10, -1, 0\n"
    "v_mbcnt_hi_u32_b32 v10, -1, v10\n"
    "v_lshlrev_b32 v0, 4, v10\n"
    "v_lshrrev_b32 v2, 5, v10\n"
    "v_and_b32 v3, 31, v10\n"
    "v_lshlrev_b32 v4, 2, v2\n"
    "v_xor_b32 v3, v3, v4\n"
    "v_xor_b32 v4, 0, v3\n"
    "v_lshlrev_b32 v4, 4, v4\n"
    "v_lshl_add_u32 v16, v2, 13, v4\n"
    "v_xor_b32 v4, 8, v3\n"
    "v_lshlrev_b32 v4, 4, v4\n"
    "v_lshl_add_u32 v17, v2, 13, v4\n"
    "s_mul_i32 s57, %3, 8448\n"
    "s_mov_b32 m0, s57\n"
    "v_lshrrev_b32 v2, 3, v10\n"
    "v_lshlrev_b32 v3, 1, v2\n"
    "v_and_b32 v3, 4, v3\n"
    "v_xor_b32 v3, v3, v10\n"
    "v_and_b32 v3, 7, v3\n"
    "v_lshlrev_b32 v3, 4, v3\n"
    "v_mul_u32_u24 v2, 896, v2\n"
    "v_add_u32 v24, v2, v3\n"
    "s_lshl_b32 s56, %8, 2\n"
    "s_add_u32 s56, s56, %3\n"
    "s_mul_i32 s56, s56, 57344\n"
    "s_lshl_b32 s57, %9, 7\n"
    "s_add_u32 s56, s56, s57\n"
    "buffer_load_dwordx4 v24, s[48:51], s56 offen lds\n"
    "s_add_u32 m0, m0, 1056\n"
    "s_add_u32 s56, s56, 7168\n"
    "buffer_load_dwordx4 v24, s[48:51], s56 offen lds\n"
    "s_add_u32 m0, m0, 1056\n"
    "s_add_u32 s56, s56, 7168\n"
    "buffer_load_dwordx4 v24, s[48:51], s56 offen lds\n"
    "s_add_u32 m0, m0, 1056\n"
    "s_add_u32 s56, s56, 7168\n"
    "buffer_load_dwordx4 v24, s[48:51], s56 offen lds\n"
    "s_add_u32 m0, m0, 1056\n"
    "s_add_u32 s56, s56, 7168\n"
    "buffer_load_dwordx4 v24, s[48:51], s56 offen lds\n"
    "s_add_u32 m0, m0, 1056\n"
    "s_add_u32 s56, s56, 7168\n"
    "buffer_load_dwordx4 v24, s[48:51], s56 offen lds\n"
    "s_add_u32 m0, m0, 1056\n"
    "s_add_u32 s56, s56, 7168\n"
    "buffer_load_dwordx4 v24, s[48:51], s56 offen lds\n"
    "s_add_u32 m0, m0, 1056\n"
    "s_add_u32 s56, s56, 7168\n"
    "buffer_load_dwordx4 v24, s[48:51], s56 offen lds\n"
    "s_add_u32 m0, m0, 1056\n"
    "s_add_u32 s56, s56, 7168\n"
    "s_and_b32 s58, %3, 1\n"
    "s_lshl_b32 s52, %9, 1\n"
    "s_add_u32 s56, s52, s58\n"
    "s_lshl_b32 s56, s56, 18\n"
    "s_lshr_b32 s55, %3, 1\n"
    "s_lshl_b32 s57, s55, 17\n"
    "s_add_u32 s56, s56, s57\n"
    "s_lshl_b32 s57, %8, 9\n"
    "s_add_u32 s56, s56, s57\n"
    "s_lshl_b32 s58, s58, 14\n"
    "s_lshl_b32 s57, s55, 13\n"
    "s_add_u32 s57, s57, s58\n"
    "s_add_u32 m0, s57, 34816\n"
    "s_nop 0\n"
    "buffer_load_dwordx4 v16, s[44:47], s56 offen lds\n"
    "s_add_u32 m0, m0, 1024\n"
    "s_add_u32 s56, s56, 16384\n"
    "buffer_load_dwordx4 v16, s[44:47], s56 offen lds\n"
    "s_add_u32 m0, m0, 1024\n"
    "s_add_u32 s56, s56, 16384\n"
    "buffer_load_dwordx4 v16, s[44:47], s56 offen lds\n"
    "s_add_u32 m0, m0, 1024\n"
    "s_add_u32 s56, s56, 16384\n"
    "buffer_load_dwordx4 v16, s[44:47], s56 offen lds\n"
    "s_add_u32 m0, m0, 1024\n"
    "s_add_u32 s56, s56, 16384\n"
    "buffer_load_dwordx4 v17, s[44:47], s56 offen lds\n"
    "s_add_u32 m0, m0, 1024\n"
    "s_add_u32 s56, s56, 16384\n"
    "buffer_load_dwordx4 v17, s[44:47], s56 offen lds\n"
    "s_add_u32 m0, m0, 1024\n"
    "s_add_u32 s56, s56, 16384\n"
    "buffer_load_dwordx4 v17, s[44:47], s56 offen lds\n"
    "s_add_u32 m0, m0, 1024\n"
    "s_add_u32 s56, s56, 16384\n"
    "buffer_load_dwordx4 v17, s[44:47], s56 offen lds\n"
    "s_add_u32 m0, m0, 1024\n"
    "s_add_u32 s56, s56, 16384\n"
    "s_add_u32 s52, s52, %3\n"
    "s_cmp_ge_u32 s52, 14\n"
    "s_cselect_b32 s57, 14, 0\n"
    "s_sub_u32 s52, s52, s57\n"
    "s_mov_b32 s55, s52\n"
    "s_lshl_b32 s54, %3, 14\n"
    "s_add_u32 s54, s54, 34816\n"
    "v_and_b32 v11, 15, v10\n"
    "v_lshrrev_b32 v12, 4, v10\n"
    "v_and_b32 v13, 8, v11\n"
    "v_lshlrev_b32 v13, 1, v13\n"
    "v_and_b32 v14, 2, v11\n"
    "v_lshl_add_u32 v13, v14, 2, v13\n"
    "v_and_b32 v14, 5, v11\n"
    "v_add_u32 v13, v13, v14\n"
    "v_and_b32 v14, 3, v11\n"
    "v_lshl_add_u32 v14, v14, 2, v12\n"
    "v_xor_b32 v15, 0, v14\n"
    "v_lshlrev_b32 v15, 4, v15\n"
    "v_lshl_add_u32 v2, v13, 9, v15\n"
    "v_xor_b32 v15, 4, v14\n"
    "v_lshlrev_b32 v15, 4, v15\n"
    "v_lshl_add_u32 v3, v13, 9, v15\n"
    "v_xor_b32 v15, 8, v14\n"
    "v_lshlrev_b32 v15, 4, v15\n"
    "v_lshl_add_u32 v4, v13, 9, v15\n"
    "v_xor_b32 v15, 12, v14\n"
    "v_lshlrev_b32 v15, 4, v15\n"
    "v_lshl_add_u32 v5, v13, 9, v15\n"
    "v_mul_u32_u24 v6, 1056, v12\n"
    "v_lshrrev_b32 v14, 2, v11\n"
    "v_lshl_add_u32 v6, v14, 7, v6\n"
    "v_and_b32 v15, 3, v11\n"
    "v_lshl_add_u32 v6, v15, 3, v6\n"
    "v_and_b32 v14, 2, v14\n"
    "v_lshlrev_b32 v14, 5, v14\n"
    "v_xor_b32 v7, 0, v14\n"
    "v_add_u32 v24, v7, v6\n"
    "v_xor_b32 v7, 64, v14\n"
    "v_add_u32 v25, v7, v6\n"
    "s_mov_b32 s60, %11\n"
    "s_cmp_lt_u32 %3, 2\n"
    "s_cselect_b32 s53, 4, 3\n"
    "s_waitcnt vmcnt(0)\n"
    "s_barrier\n"
    "s_cbranch_scc1 LK1_%=_have\n"
    "s_lshl_b32 s56, s52, 18\n"
    "s_lshl_b32 s57, %8, 9\n"
    "s_add_u32 s56, s56, s57\n"
    "s_mov_b32 m0, s54\n"
    "s_cmp_eq_u32 s53, 0\n"
    "s_cselect_b32 s56, 0x70000000, s56\n"
    "buffer_load_dwordx4 v16, s[44:47], s56 offen lds\n"
    "s_add_u32 m0, m0, 1024\n"
    "s_add_u32 s56, s56, 16384\n"
    "buffer_load_dwordx4 v16, s[44:47], s56 offen lds\n"
    "s_add_u32 m0, m0, 1024\n"
    "s_add_u32 s56, s56, 16384\n"
    "buffer_load_dwordx4 v16, s[44:47], s56 offen lds\n"
    "s_add_u32 m0, m0, 1024\n"
    "s_add_u32 s56, s56, 16384\n"
    "buffer_load_dwordx4 v16, s[44:47], s56 offen lds\n"
    "s_add_u32 m0, m0, 1024\n"
    "s_add_u32 s56, s56, 16384\n"
    "buffer_load_dwordx4 v17, s[44:47], s56 offen lds\n"
    "s_add_u32 m0, m0, 1024\n"
    "s_add_u32 s56, s56, 16384\n"
    "buffer_load_dwordx4 v17, s[44:47], s56 offen lds\n"
    "s_add_u32 m0, m0, 1024\n"
    "s_add_u32 s56, s56, 16384\n"
    "buffer_load_dwordx4 v17, s[44:47], s56 offen lds\n"
    "s_add_u32 m0, m0, 1024\n"
    "s_add_u32 s56, s56, 16384\n"
    "buffer_load_dwordx4 v17, s[44:47], s56 offen lds\n"
    "s_add_u32 m0, m0, 1024\n"
    "s_add_u32 s56, s56, 16384\n"
    "buffer_load_dwordx4 v16, s[44:47], s56 offen lds\n"
    "s_add_u32 m0, m0, 1024\n"
    "s_add_u32 s56, s56, 16384\n"
    "buffer_load_dwordx4 v16, s[44:47], s56 offen lds\n"
    "s_add_u32 m0, m0, 1024\n"
    "s_add_u32 s56, s56, 16384\n"
    "buffer_load_dwordx4 v16, s[44:47], s56 offen lds\n"
    "s_add_u32 m0, m0, 1024\n"
    "s_add_u32 s56, s56, 16384\n"
    "buffer_load_dwordx4 v16, s[44:47], s56 offen lds\n"
    "s_add_u32 m0, m0, 1024\n"
    "s_add_u32 s56, s56, 16384\n"
    "buffer_load_dwordx4 v17, s[44:47], s56 offen lds\n"
    "s_add_u32 m0, m0, 1024\n"
    "s_add_u32 s56, s56, 16384\n"
    "buffer_load_dwordx4 v17, s[44:47], s56 offen lds\n"
    "s_add_u32 m0, m0, 1024\n"
    "s_add_u32 s56, s56, 16384\n"
    "buffer_load_dwordx4 v17, s[44:47], s56 offen lds\n"
    "s_add_u32 m0, m0, 1024\n"
    "s_add_u32 s56, s56, 16384\n"
    "buffer_load_dwordx4 v17, s[44:47], s56 offen lds\n"
    "s_add_u32 m0, m0, 1024\n"
    "s_add_u32 s56, s56, 16384\n"
    "LK1_%=_have:\n"
    "ds_read_b64_tr_b16 a[0:1], v24 offset:0\n"
    "ds_read_b64_tr_b16 a[2:3], v24 offset:512\n"
    "ds_read_b64_tr_b16 a[4:5], v24 offset:4224\n"
    "ds_read_b64_tr_b16 a[6:7], v24 offset:4736\n"
    "ds_read_b64_tr_b16 a[8:9], v24 offset:8448\n"
    "ds_read_b64_tr_b16 a[10:11], v24 offset:8960\n"
    "ds_read_b64_tr_b16 a[12:13], v24 offset:12672\n"
    "ds_read_b64_tr_b16 a[14:15], v24 offset:13184\n"
    "ds_read_b64_tr_b16 a[16:17], v24 offset:16896\n"
    "ds_read_b64_tr_b16 a[18:19], v24 offset:17408\n"
    "ds_read_b64_tr_b16 a[20:21], v24 offset:21120\n"
    "ds_read_b64_tr_b16 a[22:23], v24 offset:21632\n"
    "ds_read_b64_tr_b16 a[24:25], v24 offset:25344\n"
    "ds_read_b64_tr_b16 a[26:27], v24 offset:25856\n"
    "ds_read_b64_tr_b16 a[28:29], v24 offset:29568\n"
    "ds_read_b64_tr_b16 a[30:31], v24 offset:30080\n"
    "ds_read_b64_tr_b16 a[32:33], v24 offset:32\n"
    "ds_read_b64_tr_b16 a[34:35], v24 offset:544\n"
    "ds_read_b64_tr_b16 a[36:37], v24 offset:4256\n"
    "ds_read_b64_tr_b16 a[38:39], v24 offset:4768\n"
    "ds_read_b64_tr_b16 a[40:41], v24 offset:8480\n"
    "ds_read_b64_tr_b16 a[42:43], v24 offset:8992\n"
    "ds_read_b64_tr_b16 a[44:45], v24 offset:12704\n"
    "ds_read_b64_tr_b16 a[46:47], v24 offset:13216\n"
    "ds_read_b64_tr_b16 a[48:49], v24 offset:16928\n"
    "ds_read_b64_tr_b16 a[50:51], v24 offset:17440\n"
    "ds_read_b64_tr_b16 a[52:53], v24 offset:21152\n"
    "ds_read_b64_tr_b16 a[54:55], v24 offset:21664\n"
    "ds_read_b64_tr_b16 a[56:57], v24 offset:25376\n"
    "ds_read_b64_tr_b16 a[58:59], v24 offset:25888\n"
    "ds_read_b64_tr_b16 a[60:61], v24 offset:29600\n"
    "ds_read_b64_tr_b16 a[62:63], v24 offset:30112\n"
    "ds_read_b64_tr_b16 a[64:65], v25 offset:0\n"
    "ds_read_b64_tr_b16 a[66:67], v25 offset:512\n"
    "ds_read_b64_tr_b16 a[68:69], v25 offset:4224\n"
    "ds_read_b64_tr_b16 a[70:71], v25 offset:4736\n"
    "ds_read_b64_tr_b16 a[72:73], v25 offset:8448\n"
    "ds_read_b64_tr_b16 a[74:75], v25 offset:8960\n"
    "ds_read_b64_tr_b16 a[76:77], v25 offset:12672\n"
    "ds_read_b64_tr_b16 a[78:79], v25 offset:13184\n"
    "ds_read_b64_tr_b16 a[80:81], v25 offset:16896\n"
    "ds_read_b64_tr_b16 a[82:83], v25 offset:17408\n"
    "ds_read_b64_tr_b16 a[84:85], v25 offset:21120\n"
    "ds_read_b64_tr_b16 a[86:87], v25 offset:21632\n"
    "ds_read_b64_tr_b16 a[88:89], v25 offset:25344\n"
    "ds_read_b64_tr_b16 a[90:91], v25 offset:25856\n"
    "ds_read_b64_tr_b16 a[92:93], v25 offset:29568\n"
    "ds_read_b64_tr_b16 a[94:95], v25 offset:30080\n"
    "ds_read_b64_tr_b16 a[96:97], v25 offset:32\n"
    "ds_read_b64_tr_b16 a[98:99], v25 offset:544\n"
    "ds_read_b64_tr_b16 a[100:101], v25 offset:4256\n"
    "ds_read_b64_tr_b16 a[102:103], v25 offset:4768\n"
    "ds_read_b64_tr_b16 a[104:105], v25 offset:8480\n"
    "ds_read_b64_tr_b16 a[106:107], v25 offset:8992\n"
    "ds_read_b64_tr_b16 a[108:109], v25 offset:12704\n"
    "ds_read_b64_tr_b16 a[110:111], v25 offset:13216\n"
    "ds_read_b64_tr_b16 a[112:113], v25 offset:16928\n"
    "ds_read_b64_tr_b16 a[114:115], v25 offset:17440\n"
    "ds_read_b64_tr_b16 a[116:117], v25 offset:21152\n"
    "ds_read_b64_tr_b16 a[118:119], v25 offset:21664\n"
    "ds_read_b64_tr_b16 a[120:121], v25 offset:25376\n"
    "ds_read_b64_tr_b16 a[122:123], v25 offset:25888\n"
    "ds_read_b64_tr_b16 a[124:125], v25 offset:29600\n"
    "ds_read_b64_tr_b16 a[126:127], v25 offset:30112\n"
    "v_add_u32 v2, s54, v2\n"
    "v_add_u32 v3, s54, v3\n"
    "v_add_u32 v4, s54, v4\n"
    "v_add_u32 v5, s54, v5\n"
    "s_waitcnt vmcnt(0)\n"
    "ds_read_b128 v[32:35], v2 offset:0\n"
    "ds_read_b128 v[36:39], v2 offset:1024\n"
    "ds_read_b128 v[40:43], v3 offset:0\n"
    "ds_read_b128 v[44:47], v3 offset:1024\n"
    "ds_read_b128 v[48:51], v4 offset:0\n"
    "ds_read_b128 v[52:55], v4 offset:1024\n"
    "ds_read_b128 v[56:59], v5 offset:0\n"
    "ds_read_b128 v[60:63], v5 offset:1024\n"
    "ds_read_b128 v[64:67], v2 offset:256\n"
    "ds_read_b128 v[68:71], v2 offset:1280\n"
    "ds_read_b128 v[72:75], v3 offset:256\n"
    "ds_read_b128 v[76:79], v3 offset:1280\n"
    "ds_read_b128 v[80:83], v4 offset:256\n"
    "ds_read_b128 v[84:87], v4 offset:1280\n"
    "ds_read_b128 v[88:91], v5 offset:256\n"
    "ds_read_b128 v[92:95], v5 offset:1280\n"
    "s_waitcnt lgkmcnt(0)\n"
    "LK1_%=_loop:\n"
    "s_lshl_b32 s59, s52, 10\n"
    "s_add_u32 s59, s59, %10\n"
    "s_add_u32 s52, s52, 4\n"
    "s_cmp_ge_u32 s52, 14\n"
    "s_cselect_b32 s57, 14, 0\n"
    "s_sub_u32 s52, s52, s57\n"
    "s_sub_u32 s53, s53, 1\n"
    "s_lshl_b32 s56, s52, 18\n"
    "s_lshl_b32 s57, %8, 9\n"
    "s_add_u32 s56, s56, s57\n"
    "s_mov_b32 m0, s54\n"
    "s_cmp_eq_u32 s53, 0\n"
    "s_cselect_b32 s56, 0x70000000, s56\n"
    "v_mfma_f32_16x16x32_bf16 v[160:163], v[32:35], a[0:3], 0\n"
    "buffer_load_dwordx4 v16, s[44:47], s56 offen lds\n"
    "s_add_u32 m0, m0, 1024\n"
    "s_add_u32 s56, s56, 16384\n"
    "v_mfma_f32_16x16x32_bf16 v[164:167], v[36:39], a[0:3], 0\n"
    "v_mfma_f32_16x16x32_bf16 v[160:163], v[40:43], a[4:7], v[160:163]\n"
    "buffer_load_dwordx4 v16, s[44:47], s56 offen lds\n"
    "s_add_u32 m0, m0, 1024\n"
    "s_add_u32 s56, s56, 16384\n"
    "v_mfma_f32_16x16x32_bf16 v[164:167], v[44:47], a[4:7], v[164:167]\n"
    "v_mfma_f32_16x16x32_bf16 v[160:163], v[48:51], a[8:11], v[160:163]\n"
    "buffer_load_dwordx4 v16, s[44:47], s56 offen lds\n"
    "s_add_u32 m0, m0, 1024\n"
    "s_add_u32 s56, s56, 16384\n"
    "v_mfma_f32_16x16x32_bf16 v[164:167], v[52:55], a[8:11], v[164:167]\n"
    "v_mfma_f32_16x16x32_bf16 v[160:163], v[56:59], a[12:15], v[160:163]\n"
    "buffer_load_dwordx4 v16, s[44:47], s56 offen lds\n"
    "s_add_u32 m0, m0, 1024\n"
    "s_add_u32 s56, s56, 16384\n"
    "v_mfma_f32_16x16x32_bf16 v[164:167], v[60:63], a[12:15], v[164:167]\n"
    "v_mfma_f32_16x16x32_bf16 v[160:163], v[64:67], a[16:19], v[160:163]\n"
    "buffer_load_dwordx4 v17, s[44:47], s56 offen lds\n"
    "s_add_u32 m0, m0, 1024\n"
    "s_add_u32 s56, s56, 16384\n"
    "v_mfma_f32_16x16x32_bf16 v[164:167], v[68:71], a[16:19], v[164:167]\n"
    "v_mfma_f32_16x16x32_bf16 v[160:163], v[72:75], a[20:23], v[160:163]\n"
    "buffer_load_dwordx4 v17, s[44:47], s56 offen lds\n"
    "s_add_u32 m0, m0, 1024\n"
    "s_add_u32 s56, s56, 16384\n"
    "v_mfma_f32_16x16x32_bf16 v[164:167], v[76:79], a[20:23], v[164:167]\n"
    "v_mfma_f32_16x16x32_bf16 v[160:163], v[80:83], a[24:27], v[160:163]\n"
    "buffer_load_dwordx4 v17, s[44:47], s56 offen lds\n"
    "s_add_u32 m0, m0, 1024\n"
    "s_add_u32 s56, s56, 16384\n"
    "v_mfma_f32_16x16x32_bf16 v[164:167], v[84:87], a[24:27], v[164:167]\n"
    "v_mfma_f32_16x16x32_bf16 v[160:163], v[88:91], a[28:31], v[160:163]\n"
    "buffer_load_dwordx4 v17, s[44:47], s56 offen lds\n"
    "s_add_u32 m0, m0, 1024\n"
    "s_add_u32 s56, s56, 16384\n"
    "v_mfma_f32_16x16x32_bf16 v[164:167], v[92:95], a[28:31], v[164:167]\n"
    "v_mfma_f32_16x16x32_bf16 v[168:171], v[32:35], a[32:35], 0\n"
    "buffer_load_dwordx4 v16, s[44:47], s56 offen lds\n"
    "s_add_u32 m0, m0, 1024\n"
    "s_add_u32 s56, s56, 16384\n"
    "v_mfma_f32_16x16x32_bf16 v[172:175], v[36:39], a[32:35], 0\n"
    "v_mfma_f32_16x16x32_bf16 v[168:171], v[40:43], a[36:39], v[168:171]\n"
    "buffer_load_dwordx4 v16, s[44:47], s56 offen lds\n"
    "s_add_u32 m0, m0, 1024\n"
    "s_add_u32 s56, s56, 16384\n"
    "v_mfma_f32_16x16x32_bf16 v[172:175], v[44:47], a[36:39], v[172:175]\n"
    "v_mfma_f32_16x16x32_bf16 v[168:171], v[48:51], a[40:43], v[168:171]\n"
    "buffer_load_dwordx4 v16, s[44:47], s56 offen lds\n"
    "s_add_u32 m0, m0, 1024\n"
    "s_add_u32 s56, s56, 16384\n"
    "v_mfma_f32_16x16x32_bf16 v[172:175], v[52:55], a[40:43], v[172:175]\n"
    "v_mfma_f32_16x16x32_bf16 v[168:171], v[56:59], a[44:47], v[168:171]\n"
    "buffer_load_dwordx4 v16, s[44:47], s56 offen lds\n"
    "s_add_u32 m0, m0, 1024\n"
    "s_add_u32 s56, s56, 16384\n"
    "v_mfma_f32_16x16x32_bf16 v[172:175], v[60:63], a[44:47], v[172:175]\n"
    "v_cvt_pk_bf16_f32 v224, v160, v161\n"
    "v_cvt_pk_bf16_f32 v225, v162, v163\n"
    "v_mfma_f32_16x16x32_bf16 v[168:171], v[64:67], a[48:51], v[168:171]\n"
    "buffer_load_dwordx4 v17, s[44:47], s56 offen lds\n"
    "s_add_u32 m0, m0, 1024\n"
    "s_add_u32 s56, s56, 16384\n"
    "v_mfma_f32_16x16x32_bf16 v[172:175], v[68:71], a[48:51], v[172:175]\n"
    "v_cvt_pk_bf16_f32 v226, v164, v165\n"
    "v_cvt_pk_bf16_f32 v227, v166, v167\n"
    "v_mfma_f32_16x16x32_bf16 v[168:171], v[72:75], a[52:55], v[168:171]\n"
    "buffer_load_dwordx4 v17, s[44:47], s56 offen lds\n"
    "s_add_u32 m0, m0, 1024\n"
    "s_add_u32 s56, s56, 16384\n"
    "v_mfma_f32_16x16x32_bf16 v[172:175], v[76:79], a[52:55], v[172:175]\n"
    "s_add_u32 s58, s59, 0\n"
    "buffer_store_dwordx4 v[224:227], v0, s[40:43], s58 offen sc1\n"
    "v_mfma_f32_16x16x32_bf16 v[168:171], v[80:83], a[56:59], v[168:171]\n"
    "buffer_load_dwordx4 v17, s[44:47], s56 offen lds\n"
    "s_add_u32 m0, m0, 1024\n"
    "s_add_u32 s56, s56, 16384\n"
    "v_mfma_f32_16x16x32_bf16 v[172:175], v[84:87], a[56:59], v[172:175]\n"
    "v_mfma_f32_16x16x32_bf16 v[168:171], v[88:91], a[60:63], v[168:171]\n"
    "buffer_load_dwordx4 v17, s[44:47], s56 offen lds\n"
    "s_add_u32 m0, m0, 1024\n"
    "s_add_u32 s56, s56, 16384\n"
    "v_mfma_f32_16x16x32_bf16 v[172:175], v[92:95], a[60:63], v[172:175]\n"
    "v_mfma_f32_16x16x32_bf16 v[176:179], v[32:35], a[64:67], 0\n"
    "v_mfma_f32_16x16x32_bf16 v[180:183], v[36:39], a[64:67], 0\n"
    "v_mfma_f32_16x16x32_bf16 v[176:179], v[40:43], a[68:71], v[176:179]\n"
    "v_mfma_f32_16x16x32_bf16 v[180:183], v[44:47], a[68:71], v[180:183]\n"
    "v_mfma_f32_16x16x32_bf16 v[176:179], v[48:51], a[72:75], v[176:179]\n"
    "v_mfma_f32_16x16x32_bf16 v[180:183], v[52:55], a[72:75], v[180:183]\n"
    "v_mfma_f32_16x16x32_bf16 v[176:179], v[56:59], a[76:79], v[176:179]\n"
    "v_cvt_pk_bf16_f32 v228, v168, v169\n"
    "v_cvt_pk_bf16_f32 v229, v170, v171\n"
    "v_mfma_f32_16x16x32_bf16 v[180:183], v[60:63], a[76:79], v[180:183]\n"
    "v_cvt_pk_bf16_f32 v230, v172, v173\n"
    "v_cvt_pk_bf16_f32 v231, v174, v175\n"
    "v_mfma_f32_16x16x32_bf16 v[176:179], v[64:67], a[80:83], v[176:179]\n"
    "s_add_u32 s58, s59, 14336\n"
    "buffer_store_dwordx4 v[228:231], v0, s[40:43], s58 offen sc1\n"
    "v_mfma_f32_16x16x32_bf16 v[180:183], v[68:71], a[80:83], v[180:183]\n"
    "v_mfma_f32_16x16x32_bf16 v[176:179], v[72:75], a[84:87], v[176:179]\n"
    "v_mfma_f32_16x16x32_bf16 v[180:183], v[76:79], a[84:87], v[180:183]\n"
    "v_mfma_f32_16x16x32_bf16 v[176:179], v[80:83], a[88:91], v[176:179]\n"
    "v_mfma_f32_16x16x32_bf16 v[180:183], v[84:87], a[88:91], v[180:183]\n"
    "v_mfma_f32_16x16x32_bf16 v[176:179], v[88:91], a[92:95], v[176:179]\n"
    "v_mfma_f32_16x16x32_bf16 v[180:183], v[92:95], a[92:95], v[180:183]\n"
    "v_mfma_f32_16x16x32_bf16 v[184:187], v[32:35], a[96:99], 0\n"
    "v_mfma_f32_16x16x32_bf16 v[188:191], v[36:39], a[96:99], 0\n"
    "v_mfma_f32_16x16x32_bf16 v[184:187], v[40:43], a[100:103], v[184:187]\n"
    "v_mfma_f32_16x16x32_bf16 v[188:191], v[44:47], a[100:103], v[188:191]\n"
    "v_mfma_f32_16x16x32_bf16 v[184:187], v[48:51], a[104:107], v[184:187]\n"
    "v_mfma_f32_16x16x32_bf16 v[188:191], v[52:55], a[104:107], v[188:191]\n"
    "v_mfma_f32_16x16x32_bf16 v[184:187], v[56:59], a[108:111], v[184:187]\n"
    "v_cvt_pk_bf16_f32 v232, v176, v177\n"
    "v_cvt_pk_bf16_f32 v233, v178, v179\n"
    "v_mfma_f32_16x16x32_bf16 v[188:191], v[60:63], a[108:111], v[188:191]\n"
    "v_cvt_pk_bf16_f32 v234, v180, v181\n"
    "v_cvt_pk_bf16_f32 v235, v182, v183\n"
    "s_waitcnt vmcnt(1)\n"
    "v_mfma_f32_16x16x32_bf16 v[184:187], v[64:67], a[112:115], v[184:187]\n"
    "ds_read_b128 v[96:99], v2 offset:0\n"
    "ds_read_b128 v[100:103], v2 offset:1024\n"
    "v_mfma_f32_16x16x32_bf16 v[188:191], v[68:71], a[112:115], v[188:191]\n"
    "ds_read_b128 v[104:107], v3 offset:0\n"
    "ds_read_b128 v[108:111], v3 offset:1024\n"
    "v_mfma_f32_16x16x32_bf16 v[184:187], v[72:75], a[116:119], v[184:187]\n"
    "ds_read_b128 v[112:115], v4 offset:0\n"
    "ds_read_b128 v[116:119], v4 offset:1024\n"
    "v_mfma_f32_16x16x32_bf16 v[188:191], v[76:79], a[116:119], v[188:191]\n"
    "ds_read_b128 v[120:123], v5 offset:0\n"
    "ds_read_b128 v[124:127], v5 offset:1024\n"
    "v_mfma_f32_16x16x32_bf16 v[184:187], v[80:83], a[120:123], v[184:187]\n"
    "ds_read_b128 v[128:131], v2 offset:256\n"
    "ds_read_b128 v[132:135], v2 offset:1280\n"
    "v_mfma_f32_16x16x32_bf16 v[188:191], v[84:87], a[120:123], v[188:191]\n"
    "ds_read_b128 v[136:139], v3 offset:256\n"
    "ds_read_b128 v[140:143], v3 offset:1280\n"
    "v_mfma_f32_16x16x32_bf16 v[184:187], v[88:91], a[124:127], v[184:187]\n"
    "ds_read_b128 v[144:147], v4 offset:256\n"
    "ds_read_b128 v[148:151], v4 offset:1280\n"
    "v_mfma_f32_16x16x32_bf16 v[188:191], v[92:95], a[124:127], v[188:191]\n"
    "ds_read_b128 v[152:155], v5 offset:256\n"
    "ds_read_b128 v[156:159], v5 offset:1280\n"
    "s_add_u32 s58, s59, 28672\n"
    "buffer_store_dwordx4 v[232:235], v0, s[40:43], s58 offen sc1\n"
    "s_nop 3\n"
    "v_cvt_pk_bf16_f32 v236, v184, v185\n"
    "v_cvt_pk_bf16_f32 v237, v186, v187\n"
    "v_cvt_pk_bf16_f32 v238, v188, v189\n"
    "v_cvt_pk_bf16_f32 v239, v190, v191\n"
    "s_add_u32 s58, s59, 43008\n"
    "buffer_store_dwordx4 v[236:239], v0, s[40:43], s58 offen sc1\n"
    "s_waitcnt lgkmcnt(0)\n"
    "s_cmp_eq_u32 s53, 0\n"
    "s_cbranch_scc1 LK1_%=_end\n"
    "s_lshl_b32 s59, s52, 10\n"
    "s_add_u32 s59, s59, %10\n"
    "s_add_u32 s52, s52, 4\n"
    "s_cmp_ge_u32 s52, 14\n"
    "s_cselect_b32 s57, 14, 0\n"
    "s_sub_u32 s52, s52, s57\n"
    "s_sub_u32 s53, s53, 1\n"
    "s_lshl_b32 s56, s52, 18\n"
    "s_lshl_b32 s57, %8, 9\n"
    "s_add_u32 s56, s56, s57\n"
    "s_mov_b32 m0, s54\n"
    "s_cmp_eq_u32 s53, 0\n"
    "s_cselect_b32 s56, 0x70000000, s56\n"
    "v_mfma_f32_16x16x32_bf16 v[160:163], v[96:99], a[0:3], 0\n"
    "buffer_load_dwordx4 v16, s[44:47], s56 offen lds\n"
    "s_add_u32 m0, m0, 1024\n"
    "s_add_u32 s56, s56, 16384\n"
    "v_mfma_f32_16x16x32_bf16 v[164:167], v[100:103], a[0:3], 0\n"
    "v_mfma_f32_16x16x32_bf16 v[160:163], v[104:107], a[4:7], v[160:163]\n"
    "buffer_load_dwordx4 v16, s[44:47], s56 offen lds\n"
    "s_add_u32 m0, m0, 1024\n"
    "s_add_u32 s56, s56, 16384\n"
    "v_mfma_f32_16x16x32_bf16 v[164:167], v[108:111], a[4:7], v[164:167]\n"
    "v_mfma_f32_16x16x32_bf16 v[160:163], v[112:115], a[8:11], v[160:163]\n"
    "buffer_load_dwordx4 v16, s[44:47], s56 offen lds\n"
    "s_add_u32 m0, m0, 1024\n"
    "s_add_u32 s56, s56, 16384\n"
    "v_mfma_f32_16x16x32_bf16 v[164:167], v[116:119], a[8:11], v[164:167]\n"
    "v_mfma_f32_16x16x32_bf16 v[160:163], v[120:123], a[12:15], v[160:163]\n"
    "buffer_load_dwordx4 v16, s[44:47], s56 offen lds\n"
    "s_add_u32 m0, m0, 1024\n"
    "s_add_u32 s56, s56, 16384\n"
    "v_mfma_f32_16x16x32_bf16 v[164:167], v[124:127], a[12:15], v[164:167]\n"
    "v_mfma_f32_16x16x32_bf16 v[160:163], v[128:131], a[16:19], v[160:163]\n"
    "buffer_load_dwordx4 v17, s[44:47], s56 offen lds\n"
    "s_add_u32 m0, m0, 1024\n"
    "s_add_u32 s56, s56, 16384\n"
    "v_mfma_f32_16x16x32_bf16 v[164:167], v[132:135], a[16:19], v[164:167]\n"
    "v_mfma_f32_16x16x32_bf16 v[160:163], v[136:139], a[20:23], v[160:163]\n"
    "buffer_load_dwordx4 v17, s[44:47], s56 offen lds\n"
    "s_add_u32 m0, m0, 1024\n"
    "s_add_u32 s56, s56, 16384\n"
    "v_mfma_f32_16x16x32_bf16 v[164:167], v[140:143], a[20:23], v[164:167]\n"
    "v_mfma_f32_16x16x32_bf16 v[160:163], v[144:147], a[24:27], v[160:163]\n"
    "buffer_load_dwordx4 v17, s[44:47], s56 offen lds\n"
    "s_add_u32 m0, m0, 1024\n"
    "s_add_u32 s56, s56, 16384\n"
    "v_mfma_f32_16x16x32_bf16 v[164:167], v[148:151], a[24:27], v[164:167]\n"
    "v_mfma_f32_16x16x32_bf16 v[160:163], v[152:155], a[28:31], v[160:163]\n"
    "buffer_load_dwordx4 v17, s[44:47], s56 offen lds\n"
    "s_add_u32 m0, m0, 1024\n"
    "s_add_u32 s56, s56, 16384\n"
    "v_mfma_f32_16x16x32_bf16 v[164:167], v[156:159], a[28:31], v[164:167]\n"
    "v_mfma_f32_16x16x32_bf16 v[168:171], v[96:99], a[32:35], 0\n"
    "buffer_load_dwordx4 v16, s[44:47], s56 offen lds\n"
    "s_add_u32 m0, m0, 1024\n"
    "s_add_u32 s56, s56, 16384\n"
    "v_mfma_f32_16x16x32_bf16 v[172:175], v[100:103], a[32:35], 0\n"
    "v_mfma_f32_16x16x32_bf16 v[168:171], v[104:107], a[36:39], v[168:171]\n"
    "buffer_load_dwordx4 v16, s[44:47], s56 offen lds\n"
    "s_add_u32 m0, m0, 1024\n"
    "s_add_u32 s56, s56, 16384\n"
    "v_mfma_f32_16x16x32_bf16 v[172:175], v[108:111], a[36:39], v[172:175]\n"
    "v_mfma_f32_16x16x32_bf16 v[168:171], v[112:115], a[40:43], v[168:171]\n"
    "buffer_load_dwordx4 v16, s[44:47], s56 offen lds\n"
    "s_add_u32 m0, m0, 1024\n"
    "s_add_u32 s56, s56, 16384\n"
    "v_mfma_f32_16x16x32_bf16 v[172:175], v[116:119], a[40:43], v[172:175]\n"
    "v_mfma_f32_16x16x32_bf16 v[168:171], v[120:123], a[44:47], v[168:171]\n"
    "buffer_load_dwordx4 v16, s[44:47], s56 offen lds\n"
    "s_add_u32 m0, m0, 1024\n"
    "s_add_u32 s56, s56, 16384\n"
    "v_mfma_f32_16x16x32_bf16 v[172:175], v[124:127], a[44:47], v[172:175]\n"
    "v_cvt_pk_bf16_f32 v224, v160, v161\n"
    "v_cvt_pk_bf16_f32 v225, v162, v163\n"
    "v_mfma_f32_16x16x32_bf16 v[168:171], v[128:131], a[48:51], v[168:171]\n"
    "buffer_load_dwordx4 v17, s[44:47], s56 offen lds\n"
    "s_add_u32 m0, m0, 1024\n"
    "s_add_u32 s56, s56, 16384\n"
    "v_mfma_f32_16x16x32_bf16 v[172:175], v[132:135], a[48:51], v[172:175]\n"
    "v_cvt_pk_bf16_f32 v226, v164, v165\n"
    "v_cvt_pk_bf16_f32 v227, v166, v167\n"
    "v_mfma_f32_16x16x32_bf16 v[168:171], v[136:139], a[52:55], v[168:171]\n"
    "buffer_load_dwordx4 v17, s[44:47], s56 offen lds\n"
    "s_add_u32 m0, m0, 1024\n"
    "s_add_u32 s56, s56, 16384\n"
    "v_mfma_f32_16x16x32_bf16 v[172:175], v[140:143], a[52:55], v[172:175]\n"
    "s_add_u32 s58, s59, 0\n"
    "buffer_store_dwordx4 v[224:227], v0, s[40:43], s58 offen sc1\n"
    "v_mfma_f32_16x16x32_bf16 v[168:171], v[144:147], a[56:59], v[168:171]\n"
    "buffer_load_dwordx4 v17, s[44:47], s56 offen lds\n"
    "s_add_u32 m0, m0, 1024\n"
    "s_add_u32 s56, s56, 16384\n"
    "v_mfma_f32_16x16x32_bf16 v[172:175], v[148:151], a[56:59], v[172:175]\n"
    "v_mfma_f32_16x16x32_bf16 v[168:171], v[152:155], a[60:63], v[168:171]\n"
    "buffer_load_dwordx4 v17, s[44:47], s56 offen lds\n"
    "s_add_u32 m0, m0, 1024\n"
    "s_add_u32 s56, s56, 16384\n"
    "v_mfma_f32_16x16x32_bf16 v[172:175], v[156:159], a[60:63], v[172:175]\n"
    "v_mfma_f32_16x16x32_bf16 v[176:179], v[96:99], a[64:67], 0\n"
    "v_mfma_f32_16x16x32_bf16 v[180:183], v[100:103], a[64:67], 0\n"
    "v_mfma_f32_16x16x32_bf16 v[176:179], v[104:107], a[68:71], v[176:179]\n"
    "v_mfma_f32_16x16x32_bf16 v[180:183], v[108:111], a[68:71], v[180:183]\n"
    "v_mfma_f32_16x16x32_bf16 v[176:179], v[112:115], a[72:75], v[176:179]\n"
    "v_mfma_f32_16x16x32_bf16 v[180:183], v[116:119], a[72:75], v[180:183]\n"
    "v_mfma_f32_16x16x32_bf16 v[176:179], v[120:123], a[76:79], v[176:179]\n"
    "v_cvt_pk_bf16_f32 v228, v168, v169\n"
    "v_cvt_pk_bf16_f32 v229, v170, v171\n"
    "v_mfma_f32_16x16x32_bf16 v[180:183], v[124:127], a[76:79], v[180:183]\n"
    "v_cvt_pk_bf16_f32 v230, v172, v173\n"
    "v_cvt_pk_bf16_f32 v231, v174, v175\n"
    "v_mfma_f32_16x16x32_bf16 v[176:179], v[128:131], a[80:83], v[176:179]\n"
    "s_add_u32 s58, s59, 14336\n"
    "buffer_store_dwordx4 v[228:231], v0, s[40:43], s58 offen sc1\n"
    "v_mfma_f32_16x16x32_bf16 v[180:183], v[132:135], a[80:83], v[180:183]\n"
    "v_mfma_f32_16x16x32_bf16 v[176:179], v[136:139], a[84:87], v[176:179]\n"
    "v_mfma_f32_16x16x32_bf16 v[180:183], v[140:143], a[84:87], v[180:183]\n"
    "v_mfma_f32_16x16x32_bf16 v[176:179], v[144:147], a[88:91], v[176:179]\n"
    "v_mfma_f32_16x16x32_bf16 v[180:183], v[148:151], a[88:91], v[180:183]\n"
    "v_mfma_f32_16x16x32_bf16 v[176:179], v[152:155], a[92:95], v[176:179]\n"
    "v_mfma_f32_16x16x32_bf16 v[180:183], v[156:159], a[92:95], v[180:183]\n"
    "v_mfma_f32_16x16x32_bf16 v[184:187], v[96:99], a[96:99], 0\n"
    "v_mfma_f32_16x16x32_bf16 v[188:191], v[100:103], a[96:99], 0\n"
    "v_mfma_f32_16x16x32_bf16 v[184:187], v[104:107], a[100:103], v[184:187]\n"
    "v_mfma_f32_16x16x32_bf16 v[188:191], v[108:111], a[100:103], v[188:191]\n"
    "v_mfma_f32_16x16x32_bf16 v[184:187], v[112:115], a[104:107], v[184:187]\n"
    "v_mfma_f32_16x16x32_bf16 v[188:191], v[116:119], a[104:107], v[188:191]\n"
    "v_mfma_f32_16x16x32_bf16 v[184:187], v[120:123], a[108:111], v[184:187]\n"
    "v_cvt_pk_bf16_f32 v232, v176, v177\n"
    "v_cvt_pk_bf16_f32 v233, v178, v179\n"
    "v_mfma_f32_16x16x32_bf16 v[188:191], v[124:127], a[108:111], v[188:191]\n"
    "v_cvt_pk_bf16_f32 v234, v180, v181\n"
    "v_cvt_pk_bf16_f32 v235, v182, v183\n"
    "s_waitcnt vmcnt(1)\n"
    "v_mfma_f32_16x16x32_bf16 v[184:187], v[128:131], a[112:115], v[184:187]\n"
    "ds_read_b128 v[32:35], v2 offset:0\n"
    "ds_read_b128 v[36:39], v2 offset:1024\n"
    "v_mfma_f32_16x16x32_bf16 v[188:191], v[132:135], a[112:115], v[188:191]\n"
    "ds_read_b128 v[40:43], v3 offset:0\n"
    "ds_read_b128 v[44:47], v3 offset:1024\n"
    "v_mfma_f32_16x16x32_bf16 v[184:187], v[136:139], a[116:119], v[184:187]\n"
    "ds_read_b128 v[48:51], v4 offset:0\n"
    "ds_read_b128 v[52:55], v4 offset:1024\n"
    "v_mfma_f32_16x16x32_bf16 v[188:191], v[140:143], a[116:119], v[188:191]\n"
    "ds_read_b128 v[56:59], v5 offset:0\n"
    "ds_read_b128 v[60:63], v5 offset:1024\n"
    "v_mfma_f32_16x16x32_bf16 v[184:187], v[144:147], a[120:123], v[184:187]\n"
    "ds_read_b128 v[64:67], v2 offset:256\n"
    "ds_read_b128 v[68:71], v2 offset:1280\n"
    "v_mfma_f32_16x16x32_bf16 v[188:191], v[148:151], a[120:123], v[188:191]\n"
    "ds_read_b128 v[72:75], v3 offset:256\n"
    "ds_read_b128 v[76:79], v3 offset:1280\n"
    "v_mfma_f32_16x16x32_bf16 v[184:187], v[152:155], a[124:127], v[184:187]\n"
    "ds_read_b128 v[80:83], v4 offset:256\n"
    "ds_read_b128 v[84:87], v4 offset:1280\n"
    "v_mfma_f32_16x16x32_bf16 v[188:191], v[156:159], a[124:127], v[188:191]\n"
    "ds_read_b128 v[88:91], v5 offset:256\n"
    "ds_read_b128 v[92:95], v5 offset:1280\n"
    "s_add_u32 s58, s59, 28672\n"
    "buffer_store_dwordx4 v[232:235], v0, s[40:43], s58 offen sc1\n"
    "s_nop 3\n"
    "v_cvt_pk_bf16_f32 v236, v184, v185\n"
    "v_cvt_pk_bf16_f32 v237, v186, v187\n"
    "v_cvt_pk_bf16_f32 v238, v188, v189\n"
    "v_cvt_pk_bf16_f32 v239, v190, v191\n"
    "s_add_u32 s58, s59, 43008\n"
    "buffer_store_dwordx4 v[236:239], v0, s[40:43], s58 offen sc1\n"
    "s_waitcnt lgkmcnt(0)\n"
    "s_cmp_lg_u32 s53, 0\n"
    "s_cbranch_scc1 LK1_%=_loop\n"
    "LK1_%=_end:\n"
    "s_endpgm\n"
            :: "s"(wlo), "s"(whi), "s"(p.ws_bytes), "s"(w), "s"((u32)(size_t)p.wv), "s"((u32)((size_t)p.wv >> 32)), "s"((u32)(size_t)p.wo), "s"((u32)((size_t)p.wo >> 32)),
               "s"(h), "s"(part), "s"(img), "s"(SCORE_SCALE), "s"(WS_DBG + 200000 + (768 + b) * 4)
            : CHAIN_CLOBBERS);
        }
        __builtin_unreachable();
    }
    const int lane = lane_id(), x = b & 7;
    const rsrc_t ws = mk_rsrc(p.ws, p.ws_bytes);
    const u32 ring = WS_RING + ((0u - (u32)(size_t)p.ws) & 4095u);
    const u32x4 ones = {~0u, ~0u, ~0u, ~0u};
    if (x == 0 || x == 2) {
        const int side = x == 2, slot = ((b - 224) >> 3) * 4 + w, i0 = (side * 16 + slot) * 64 + lane;
        for (int i = i0; i < NPAGE * 256; i += 2048) st16c(ones, ws, (u32)(i * 16), ring);
        if (i0 < NFLAG) __builtin_amdgcn_raw_buffer_store_b32(0u, ws, (u32)(i0 * 128), WS_FLAGS, 16);
        if (i0 >= 1024 && i0 < 1029) __builtin_amdgcn_raw_buffer_store_b32(~0u, ws, (u32)(WS_LAT + 2048 + (i0 - 1024) * 4), 0, 16);
        if (i0 >= 512 && i0 < 960) {
            const int c = i0 - 512, kk = c >> 6, l = c & 63;
            u32x4 v = ld16(mk_rsrc(p.mem0, 8 * 896), (u32)(((l & 1) + 2 * ((l >> 2) & 3)) * 896 + (64 * kk + 16 * (l >> 4) + 8 * ((l >> 1) & 1)) * 2), 0);
            v.x = min(v.x, 0xfffffffeu); v.y = min(v.y, 0xfffffffeu); v.z = min(v.z, 0xfffffffeu); v.w = min(v.w, 0xfffffffeu);
            st16c(v, ws, (u32)(c * 16), WS_M0);
        }
        constexpr int PP = NPAGE / 16;
        const u32 pg0 = __builtin_amdgcn_readfirstlane(ring + slot * PP * 4096 + 2048 + side * 128);
        u32 best = ~0u;
        for (int rep = 0; rep < PROBE_REPS; ++rep) {
            st16c(ones, ws, lane < 8 * PP ? (u32)((lane >> 3) * 4096 + (lane & 7) * 16) : 0x7fffff00u, pg0);
            drain();
            for (int i = 0; i < PP; ++i) {
                unsigned long long t0, t1;
                asm volatile("s_memtime %0\n\ts_waitcnt lgkmcnt(0)" : "=s"(t0) :: "memory");
                const u32x4 v = ld16c(ws, (u32)((lane & 7) * 16), pg0 + i * 4096);
                asm volatile("s_waitcnt vmcnt(0)\n\ts_memtime %0\n\ts_waitcnt lgkmcnt(0)" : "=s"(t1) : "v"(v) : "memory");
                if (lane == i) best = min(best, (u32)(t1 - t0));
            }
        }
        if (lane < PP) __builtin_amdgcn_raw_buffer_store_b32(best, ws, (u32)(WS_LAT + (side * 128 + slot * PP + lane) * 4), 0, 16);
    } else {
        const int u0 = (((b - 224) >> 3) * 6 + x - (x > 0) - (x > 2)) * 4 + w, r = lane >> 3, c = lane & 7;
        u32x4 xv[3][7];
#pragma unroll
        for (int k = 0; k < 3; ++k)
#pragma unroll
            for (int i = 0; i < 7; ++i) xv[k][i] = ld16(mk_rsrc(p.x, 2048 * 896), (u32)(r * 896 + c * 16 + i * 128), (u32)((u0 + 96 * k) * 8 * 896));
        KST(0)
        LAS unsigned char* tile = sm + w * 56 * 144;
#pragma unroll
        for (int k = 0; k < 3; ++k) {
            const int u = u0 + 96 * k;
            if (u >= 256) break;
            typedef float f32x2 __attribute__((ext_vector_type(2)));
            f32x2 f[28], s2 = {0.f, 0.f}, q2 = {0.f, 0.f};
#pragma unroll
            for (int i = 0; i < 28; ++i) { const u32 d = xv[k][i >> 2][i & 3]; f[i] = f32x2{lo(d), hi(d)}; s2 += f[i]; }
            float s = s2.x + s2.y;
            s += dpp<0xB1>(s); s += dpp<0x4E>(s); s += dpp<0x141>(s);
            const float mu = s * (1.0f / 448.0f);
#pragma unroll
            for (int i = 0; i < 28; ++i) { f[i] -= mu; q2 += f[i] * f[i]; }
            float q = q2.x + q2.y;
            q += dpp<0xB1>(q); q += dpp<0x4E>(q); q += dpp<0x141>(q);
            const float rs = __builtin_amdgcn_rsqf(q * (1.0f / 448.0f) + 1e-5f);
            LAS unsigned char* dst = tile + (c & 6) * 144 + r * 16 + (c & 1) * 4;
#pragma unroll
            for (int i = 0; i < 28; ++i) {
                const f32x2 y = f[i] * rs;
                *(LAS u32*)(dst + ((i >> 2) * 8 + ((i >> 1) & 1)) * 144 + (i & 1) * 8) = pk(y.x, y.y);
            }
#pragma unroll
            for (int i = 0; i < 7; ++i) st16c(*(const LAS u32x4*)(tile + (8 * i + r) * 144 + c * 16), ws,
                                              (u32)((8 * i + r) * 256 + c * 16), (u32)(WS_XP + (u >> 1) * 14336 + (u & 1) * 128));
            KST(1 + (k > 0))
        }
    }
}

__device__ __forceinline__ void role_scores(const Params& p, int idx, int lane, int w) {
    const rsrc_t ws = mk_rsrc(p.ws, p.ws_bytes);
    const int h = (idx & 7) * 2 + (w >> 1), qt = w & 1, ph = idx >> 3, n = lane & 15, q = lane >> 4;
    u32 seen1 = 0, seen2 = 0;
    bool ok = true;
    for (int s = ph; s < 64; s += 3) {
        ok = ok && wait_flags(ws, WS_FLAGS + FL_F1K + (h >> 2) * 7 * 128, 7, (u32)(s + 1), seen1, lane);
        u32x4 xf[14], kf[28];
        f32x4 sx[2] = {}, d0 = {}, d1 = {};
#pragma unroll
        for (int ub = 0; ub < 14; ++ub) xf[ub] = ld16(ws, (u32)(lane * 16 + ub * 1024), (u32)(WS_XP + (2 * s + qt) * 14336));
#pragma unroll
        for (int i = 0; i < 28; ++i) kf[i] = ld16c(ws, (u32)(lane * 16 + i * 1024) | (i >= 14 && !qt ? 0x7ff00000u : 0u), (u32)(WS_KX + (s * 16 + h) * 28672));
        __builtin_amdgcn_sched_barrier(0);
#pragma unroll
        for (int ub = 0; ub < 14; ++ub) { sx[0] = mfma(kf[ub], xf[ub], sx[0]); sx[1] = mfma(kf[14 + ub], xf[ub], sx[1]); }
        const int tq = 16 * qt + n;
        float mx = -1e30f;
#pragma unroll
        for (int kt = 0; kt < 2; ++kt)
#pragma unroll
            for (int r = 0; r < 4; ++r) { if (16 * kt + 4 * q + r > tq) sx[kt][r] = -__builtin_inff(); mx = fmaxf(mx, sx[kt][r]); }
        u32x4 b0[7], b1[7];
#pragma unroll
        for (int kk = 0; kk < 7; ++kk) {
            b0[kk] = u32x4{xf[2 * kk].x, xf[2 * kk].z, xf[2 * kk + 1].x, xf[2 * kk + 1].z};
            b1[kk] = u32x4{xf[2 * kk].y, xf[2 * kk].w, xf[2 * kk + 1].y, xf[2 * kk + 1].w};
            asm volatile("" : "+v"(b0[kk]), "+v"(b1[kk]));
        }
        ok = ok && wait_flags(ws, WS_FLAGS + FL_FS + h * 128, 1, (u32)(s + 1), seen2, lane);
        TL(0, s, h == 5 && qt == 0)
#pragma unroll
        for (int kk = 0; kk < 7; ++kk) kf[kk] = ld16c(ws, (u32)(lane * 16 + kk * 1024), (u32)(WS_KM + (s * 16 + h) * 7168));
        __builtin_amdgcn_sched_barrier(0);
#pragma unroll
        for (int kk = 0; kk < 7; ++kk) { d0 = mfma(kf[kk], b0[kk], d0); d1 = mfma(kf[kk], b1[kk], d1); }
        float m0 = d0[0] + d1[2], m1 = d0[1] + d1[3];
        mx = xmax(fmaxf(mx, fmaxf(m0, m1)));
        m0 = __builtin_amdgcn_exp2f(m0 - mx); m1 = __builtin_amdgcn_exp2f(m1 - mx);
        float z = m0 + m1;
#pragma unroll
        for (int r = 0; r < 4; ++r) {
            sx[0][r] = __builtin_amdgcn_exp2f(sx[0][r] - mx); sx[1][r] = __builtin_amdgcn_exp2f(sx[1][r] - mx);
            z += sx[0][r] + sx[1][r];
        }
        const float rz = __builtin_amdgcn_rcpf(xsum(z));
        const u32 pb = (u32)(WS_PX + (2 * s + qt) * 20480);
        st16c(pk8(sx[0] * rz, sx[1] * rz), ws, (u32)(lane * 16), pb + h * 1024);
        __builtin_amdgcn_raw_buffer_store_b32(pk(m0 * rz, m1 * rz), ws, (u32)(16384 + (h >> 2) * 1024 + ((2 * ((h >> 1) & 1) + (q >> 1)) * 16 + n) * 16 + ((h & 1) + 2 * (q & 1)) * 4), pb, 16);
        drain();
        __builtin_amdgcn_s_barrier();
        if (w == 0) put_flag(ws, WS_FLAGS + FL_FP + idx * 128, (u32)(s + 1), lane);
        TL(1, s, h == 5 && qt == 0)
    }
}

__device__ __forceinline__ void role_values(const Params& p, int idx, int lane, int w) {
    const rsrc_t ws = mk_rsrc(p.ws, p.ws_bytes), rx = mk_rsrc(p.x, 2048 * 896);
    const int ph = idx & 3, tp = (idx >> 2) * 2 + (w >> 1), qt = w & 1, n = lane & 15, q = lane >> 4;
    u32 seen1 = 0, seen2 = 0;
    bool ok = true;
    for (int s = ph; s < 64; s += 4) {
        ok = ok && wait_flags(ws, WS_FLAGS + FL_F1V + (tp >> 1) * 512, 4, (u32)(s + 1), seen1, lane);
        u32x4 vf[2][20], pf[20];
        u32x2 xr[2];
#pragma unroll
        for (int j = 0; j < 2; ++j) {
#pragma unroll
            for (int h = 0; h < 16; ++h) vf[j][h] = ld16c(ws, (u32)(lane * 16 + (j * 16 + h) * 1024), (u32)(WS_VX + (s * 28 + 2 * tp) * 16384));
            xr[j] = __builtin_amdgcn_raw_buffer_load_b64(rx, (u32)((32 * s + 16 * qt + n) * 896 + (32 * tp + 16 * j + 4 * q) * 2), 0, 0);
        }
#pragma unroll
        for (int h = 0; h < 16; ++h) asm volatile("" : "+a"(vf[0][h]), "+a"(vf[1][h]));
        ok = ok && wait_flags(ws, WS_FLAGS + FL_FV + tp * 256, 2, (u32)(s + 1), seen2, lane);
        TL(2, s, tp == 5 && qt == 0)
#pragma unroll
        for (int j = 0; j < 2; ++j)
#pragma unroll
            for (int a = 0; a < 4; ++a) vf[j][16 + a] = ld16c(ws, (u32)(lane * 16 + (j * 4 + a) * 1024), (u32)(WS_VM + (s * 28 + 2 * tp) * 4096));
        u32 seen3 = 0;
        ok = ok && wait_flags(ws, WS_FLAGS + FL_FP + (s - 3 * ((s * 43) >> 7)) * 8 * 128, 8, (u32)(s + 1), seen3, lane);
        TL(3, s, tp == 5 && qt == 0)
#pragma unroll
        for (int i = 0; i < 20; ++i) pf[i] = ld16c(ws, (u32)(lane * 16 + i * 1024), (u32)(WS_PX + (2 * s + qt) * 20480));
        f32x4 c[2] = {};
        __builtin_amdgcn_sched_barrier(0);
#pragma unroll
        for (int i = 0; i < 20; ++i) { c[0] = mfma(vf[0][i], pf[i], c[0]); c[1] = mfma(vf[1][i], pf[i], c[1]); }
        const u32x4 o = pk8(c[0], c[1]);
        const u32x4 hv = {pk(lo(xr[0].x) + lo(o.x), hi(xr[0].x) + hi(o.x)), pk(lo(xr[0].y) + lo(o.y), hi(xr[0].y) + hi(o.y)),
                          pk(lo(xr[1].x) + lo(o.z), hi(xr[1].x) + hi(o.z)), pk(lo(xr[1].y) + lo(o.w), hi(xr[1].y) + hi(o.w))};
        st16c(hv, ws, (u32)(lane * 16), (u32)(WS_H + ((2 * s + qt) * 14 + tp) * 1024));
        drain();
        __builtin_amdgcn_s_barrier();
        if (w == 0) put_flag(ws, WS_FLAGS + FL_FH + (ph * 7 + (idx >> 2)) * 128, (u32)(s + 1), lane);
        TL(4, s, tp == 5 && qt == 0)
    }
}

__device__ __forceinline__ void role_up(const Params& p, LAS unsigned char* sm, int idx, int lane, int w) {
    const rsrc_t ws = mk_rsrc(p.ws, p.ws_bytes), rw = mk_rsrc(p.w1, 448 * 1792 * 2);
    const int qt = idx & 1, par = (idx >> 1) & 1, fg = idx >> 2, fp = fg * 4 + w, n = lane & 15, q = lane >> 4;
    u32x4 wf[2][14];
    f32x4 cs[2], bias[2];
    const u32x4 ones = {0x3f803f80u, 0x3f803f80u, 0x3f803f80u, 0x3f803f80u};
#pragma unroll 4
    for (int i = 0; i < 28; ++i) dma16(rw, sm + (w * 28 + i) * 1024, (u32)((lane >> 4) * 3584 + (lane & 15) * 16), (u32)((w * 28 + i) * 4 * 3584 + fg * 256));
    drain();
    __builtin_amdgcn_s_barrier();
    asm volatile("" ::: "memory");
#pragma unroll
    for (int j = 0; j < 2; ++j) {
        cs[j] = f32x4{0, 0, 0, 0};
#pragma unroll
        for (int ub = 0; ub < 14; ++ub) {
            wf[j][ub] = tr_frag(sm + (32 * ub + 4 * q + (n >> 2)) * 256 + (2 * w + j) * 32 + (n & 3) * 8, 16 * 256);
            cs[j] = mfma(wf[j][ub], ones, cs[j]);
        }
        const u32x2 bb = *(const u32x2*)(p.b1 + 32 * fp + 16 * j + 4 * q);
        bias[j] = f32x4{lo(bb.x), hi(bb.x), lo(bb.y), hi(bb.y)};
    }
    const u32 gflag = WS_FLAGS + FL_FG + ((par * 2 + qt) * 14 + fg) * 128;
    __syncthreads();
    bool ok = true;
    for (int s = par; s < 64; s += 2) {
        u32 seen = 0;
        if (w == 0) ok = ok && wait_flags(ws, WS_FLAGS + FL_FH + (s & 3) * 7 * 128, 7, (u32)(s + 1), seen, lane);
        __builtin_amdgcn_s_barrier();
        asm volatile("" ::: "memory");
        TL(5, s, fp == 5 && qt == 0)
#pragma unroll
        for (int i = 0; i < 4; ++i) if (i < 3 || w < 2)
            __builtin_amdgcn_raw_ptr_buffer_load_lds(ws, sm + (4 * i + w) * 1024, 16, (u32)(lane * 16), (u32)(WS_H + (2 * s + qt) * 14336 + (4 * i + w) * 1024), 0, 16);
        drain();
        __builtin_amdgcn_s_barrier();
        asm volatile("" ::: "memory");
        u32x4 hf[14];
#pragma unroll
        for (int ub = 0; ub < 14; ++ub) hf[ub] = *(const LAS u32x4*)(sm + ub * 1024 + lane * 16);
        f32x4 acc[2] = {}, s1 = {}, s2 = {};
#pragma unroll
        for (int ub = 0; ub < 14; ++ub) {
            s1 = mfma(ones, hf[ub], s1); s2 = mfma(hf[ub], hf[ub], s2);
            acc[0] = mfma(wf[0][ub], hf[ub], acc[0]); acc[1] = mfma(wf[1][ub], hf[ub], acc[1]);
        }
        const float dg = (n & 2) ? ((n & 1) ? s2[3] : s2[2]) : ((n & 1) ? s2[1] : s2[0]);
        const float mu = s1[0] * (1.0f / 448.0f), var = fmaxf(xsum(q == (n >> 2) ? dg : 0.f) * (1.0f / 448.0f) - mu * mu, 0.f);
        const float r1 = __builtin_amdgcn_rsqf(var + 1e-5f), rho = r1 * __builtin_amdgcn_rsqf(var * r1 * r1 + 1e-5f);
#pragma unroll
        for (int j = 0; j < 2; ++j)
#pragma unroll
            for (int r = 0; r < 4; ++r) {
                const float z = rho * (acc[j][r] - mu * cs[j][r]) + bias[j][r];
                const float u = z * (2.3022081985f + 0.1029432397f * z * z);
                acc[j][r] = z * __builtin_amdgcn_rcpf(1.0f + __builtin_amdgcn_exp2f(-u));
            }
        st16c(pk8(acc[0], acc[1]), ws, (u32)(lane * 16), (u32)(WS_G + ((2 * s + qt) * 56 + fp) * 1024));
        TL(6, s, fp == 5 && qt == 0)
        drain();
        __builtin_amdgcn_s_barrier();
        if (w == 0) put_flag(ws, gflag, (u32)(s + 1), lane);
    }
}

__device__ __forceinline__ void role_down(const Params& p, LAS unsigned char* sm, int idx, int lane, int w) {
    const rsrc_t ws = mk_rsrc(p.ws, p.ws_bytes), rw = mk_rsrc(p.w2, 448 * 1792 * 2), ro = mk_rsrc(p.out, 2048 * 896);
    const int qt = idx & 1, par = (idx >> 1) & 1, cg = idx >> 2, n = lane & 15, q = lane >> 4;
    u32x4 wf[4][14];
#pragma unroll
    for (int half = 0; half < 2; ++half) {
        LAS unsigned char* buf = sm + w * 28672;
#pragma unroll 4
        for (int i = 0; i < 28; ++i) dma16(rw, buf + i * 1024, (u32)((lane >> 3) * 896 + (lane & 7) * 16), (u32)((448 * w + 224 * half + 8 * i) * 896 + cg * 128));
        drain();
#pragma unroll
        for (int j = 0; j < 4; ++j)
#pragma unroll
            for (int i = 0; i < 7; ++i) wf[j][7 * half + i] = tr_frag(buf + (32 * i + 4 * q + (n >> 2)) * 128 + j * 32 + (n & 3) * 8, 16 * 128);
        asm volatile("s_waitcnt lgkmcnt(0)" ::: "memory");
    }
    __builtin_amdgcn_s_barrier();
    const int c0 = 64 * cg + 16 * w + 4 * q;
    const u32x2 bb = *(const u32x2*)(p.b2 + c0);
    u32 seen = 0;
    bool ok = true;
    for (int s = par; s < 64; s += 2) {
        ok = ok && wait_flags(ws, WS_FLAGS + FL_FG + ((par * 2 + qt) * 14 + ((w * 14) >> 2)) * 128, 4, (u32)(s + 1), seen, lane);
        TL(7, s, cg == 3 && qt == 0 && w == 0)
        u32x4 gf[14];
#pragma unroll
        for (int i = 0; i < 14; ++i) gf[i] = ld16c(ws, (u32)(lane * 16), (u32)(WS_G + ((2 * s + qt) * 56 + 14 * w + i) * 1024));
        const u32 off = (u32)((32 * s + 16 * qt + n) * 896 + c0 * 2);
        const u32x2 hr = __builtin_amdgcn_raw_buffer_load_b64(ws, (u32)(lane * 16 + (w & 1) * 8), (u32)(WS_H + ((2 * s + qt) * 14 + 2 * cg + (w >> 1)) * 1024), 16);
        f32x4 acc[4] = {};
        __builtin_amdgcn_sched_barrier(0);
#pragma unroll
        for (int i = 0; i < 14; ++i)
#pragma unroll
            for (int j = 0; j < 4; ++j) { asm volatile("" : "+a"(wf[j][i])); acc[j] = mfma(wf[j][i], gf[i], acc[j]); }
        LAS unsigned char* red = sm + (s & 2) * 8192;
#pragma unroll
        for (int j = 0; j < 4; ++j) *(LAS f32x4*)(red + (j * 4 + w) * 1024 + lane * 16) = acc[j];
        __syncthreads();
        f32x4 o = *(LAS f32x4*)(red + (w * 4) * 1024 + lane * 16);
#pragma unroll
        for (int k = 1; k < 4; ++k) o += *(LAS f32x4*)(red + (w * 4 + k) * 1024 + lane * 16);
        const u32x2 ov = {pk(lo(hr.x) + lo(bb.x) + o[0], hi(hr.x) + hi(bb.x) + o[1]), pk(lo(hr.y) + lo(bb.y) + o[2], hi(hr.y) + hi(bb.y) + o[3])};
        __builtin_amdgcn_raw_buffer_store_b64(ov, ro, off, 0, 0);
        TL(8, s, cg == 3 && qt == 0 && w == 0)
    }
}

template <bool ISV>
__device__ __forceinline__ void role_warm(const Params& p, u32 tick, int id, int lane, int w) {
    const rsrc_t ws = mk_rsrc(p.ws, p.ws_bytes);
    constexpr int N = ISV ? 28 : 7;
    u32 seen = 0;
    for (int s = 1; s < 64; ++s) {
        if (!(ISV ? wait_flags(ws, WS_FLAGS + FL_F1V, 28, (u32)(s + 1), seen, lane) : wait_flags(ws, WS_FLAGS + FL_F1K + (id >> 2) * 7 * 128, 7, (u32)(s + 1), seen, lane))) return;
        for (u32 spin = 0;; ++spin) {
            asm volatile("" ::: "memory");
            if ((int)__builtin_amdgcn_readfirstlane(__builtin_amdgcn_raw_buffer_load_b32(ws, 2048, tick, 16)) >= s - WARM_AHEAD) break;
            if (spin > SPIN_CAP) return;
            __builtin_amdgcn_s_sleep(2);
        }
        if (ISV) __builtin_amdgcn_s_sleep(WARM_SLEEPV);
        else { __builtin_amdgcn_s_sleep(WARM_SLEEP); if (id & 1) __builtin_amdgcn_s_sleep(WARM_STAG); if (w & 1) __builtin_amdgcn_s_sleep(WARM_WSTEP); if (w & 2) __builtin_amdgcn_s_sleep(2 * WARM_WSTEP); }
        u32x4 v[N];
        const u32 base = ISV ? (u32)(WS_VX + (s * 28 + id * 7) * 16384) : (u32)(WS_KX + (s * 16 + id) * 28672);
#pragma unroll
        for (int i = 0; i < N; ++i) v[i] = ld16c(ws, (u32)(lane * 16 + i * 1024), base + w * N * 1024);
#pragma unroll
        for (int i = 0; i < N; ++i) asm volatile("" :: "v"(v[i]));
    }
}

__device__ __forceinline__ Params args() {
    const __attribute__((address_space(4))) Params* q = (const __attribute__((address_space(4))) Params*)__builtin_amdgcn_kernarg_segment_ptr();
    asm volatile("" : "+s"(q));
    Params p;
    p.x = q->x; p.mem0 = q->mem0; p.wq = q->wq; p.wk = q->wk; p.wv = q->wv; p.wo = q->wo; p.w1 = q->w1; p.b1 = q->b1; p.w2 = q->w2; p.b2 = q->b2;
    p.out = q->out; p.ws = q->ws; p.ws_bytes = q->ws_bytes; p.ws0 = q->ws0; p.ws1 = q->ws1; p.wm = q->wm; p.wv_t = q->wv_t; p.pad = 0;
    return p;
}

__global__ void __launch_bounds__(256, 1) main_kernel(Params) {
    extern __shared__ __attribute__((aligned(16))) unsigned char smem[];
    LAS unsigned char* sm = (LAS unsigned char*)smem;
    const int b = blockIdx.x, x = b & 7, r = b >> 3;
    const int w = __builtin_amdgcn_readfirstlane(threadIdx.x >> 6);
    const bool isS = x == 0 && r < 16, isV = x == 2 && r < 28;
    if (x == 0 || x == 2) {
        if (!isS && !isV) {
            const Params p = args();
            const u32 ring = WS_RING + ((0u - (u32)(size_t)p.ws) & 4095u);
            const int lane = lane_id();
            const rsrc_t ws = mk_rsrc(p.ws, p.ws_bytes);
            u32 key[2], rank[2] = {0, 0};
#pragma unroll
            for (int k = 0; k < 2; ++k) {
                const int c = lane + 64 * k;
                const u32 l0 = __builtin_amdgcn_raw_buffer_load_b32(ws, (u32)(WS_LAT + c * 4), 0, 0), l1 = __builtin_amdgcn_raw_buffer_load_b32(ws, (u32)(WS_LAT + 512 + c * 4), 0, 0);
                key[k] = c < NPAGE ? ((min(l0, 0xffffu) + min(l1, 0xffffu)) << 7) | (u32)c : ~0u;
            }
#pragma unroll 4
            for (int j = 0; j < 64; ++j) {
                const u32 a = __builtin_amdgcn_readlane(key[0], j), c = __builtin_amdgcn_readlane(key[1], j);
                rank[0] += (a < key[0]) + (c < key[0]); rank[1] += (a < key[1]) + (c < key[1]);
            }
            auto page = [&](int j) {
                const unsigned long long m0 = __builtin_amdgcn_ballot_w64(rank[0] == (u32)j), m1 = __builtin_amdgcn_ballot_w64(rank[1] == (u32)j);
                return (u32)(m0 ? __builtin_ctzll(m0) : 64 + __builtin_ctzll(m1)) & 127u;
            };
            if (x == 2 && r == 28 && w == 0) {
                u32 word = 0;
                for (int j = 0; j < 20; ++j) { const u32 g = page(j); if (lane == (j >> 2)) word |= g << (8 * (j & 3)); }
                if (lane < 5) __builtin_amdgcn_raw_buffer_store_b32(word, ws, (u32)(WS_LAT + 2048 + lane * 4), 0, 16);
            }
            if (x == 0) role_warm<false>(p, ring + 4096u * page(16), r - 16, lane, w);
            return;
        }
        const Params p = args();
        const u32 wlo = (u32)(size_t)p.ws, whi = (u32)((size_t)p.ws >> 32), ring = WS_RING + ((0u - wlo) & 4095u);
        if (isS) {
            asm volatile(
    "s_mov_b32 s40, %0\n"
    "s_and_b32 s41, %1, 0xffff\n"
    "s_mov_b32 s42, %6\n"
    "s_mov_b32 s43, 0x00020000\n"
    "s_mov_b32 s72, %2\n"
    "s_mov_b32 s73, %4\n"
    "s_mov_b32 s74, %5\n"
    "s_mov_b32 s44, 0\n"
    "s_mov_b32 s75, 0\n"
    "s_mov_b32 s69, 0x10000\n"
    "s_mov_b32 s79, 0x11000\n"
    "v_mbcnt_lo_u32_b32 v10, -1, 0\n"
    "v_mbcnt_hi_u32_b32 v10, -1, v10\n"
    "v_lshlrev_b32 v0, 4, v10\n"
    "v_and_b32 v11, 2, v10\n"
    "v_cmp_eq_u32 vcc, 0, v11\n"
    "v_mov_b32 v12, 0xeeee\n"
    "v_mov_b32 v13, 0x4444\n"
    "v_cndmask_b32 v1, v12, v13, vcc\n"
    "s_cmp_eq_u32 %3, 1\n"
    "s_cbranch_scc1 LS1_%=_start\n"
    "s_cmp_eq_u32 %3, 2\n"
    "s_cbranch_scc1 LS2_%=_start\n"
    "s_cmp_eq_u32 %3, 3\n"
    "s_cbranch_scc1 LS3_%=_start\n"
    "LS0_%=_start:\n"
    "v_and_b32 v11, 15, v10\n"
    "v_lshrrev_b32 v12, 4, v10\n"
    "v_and_b32 v2, 4, v11\n"
    "v_lshlrev_b32 v3, 5, v2\n"
    "v_and_b32 v2, 1, v11\n"
    "v_lshl_add_u32 v3, v2, 5, v3\n"
    "v_and_b32 v2, 8, v11\n"
    "v_lshlrev_b32 v13, 4, v2\n"
    "v_xor_b32 v3, v3, v13\n"
    "v_lshl_add_u32 v3, v2, 5, v3\n"
    "v_and_b32 v2, 2, v11\n"
    "v_lshl_add_u32 v3, v2, 1, v3\n"
    "v_lshl_add_u32 v3, v12, 3, v3\n"
    "v_add_u32 v3, 0x1d000, v3\n"
    "v_mov_b32 v5, v0\n"
    "v_and_b32 v2, 8, v11\n"
    "v_lshlrev_b32 v6, 7, v2\n"
    "v_and_b32 v2, 2, v11\n"
    "v_lshl_add_u32 v6, v2, 8, v6\n"
    "v_lshl_add_u32 v6, v12, 7, v6\n"
    "v_and_b32 v2, 4, v11\n"
    "v_lshl_add_u32 v6, v2, 2, v6\n"
    "v_and_b32 v2, 1, v11\n"
    "v_lshl_add_u32 v6, v2, 2, v6\n"
    "v_add_u32 v6, 0x1ec00, v6\n"
    "v_lshlrev_b32 v7, 3, v11\n"
    "v_lshrrev_b32 v2, 1, v12\n"
    "v_xor_b32 v13, v2, v12\n"
    "v_and_b32 v13, 1, v13\n"
    "v_lshl_add_u32 v7, v13, 7, v7\n"
    "v_lshl_add_u32 v7, v2, 8, v7\n"
    "v_add_u32 v7, 0x1d000, v7\n"
    "v_add_u32 v8, 0x200, v7\n"
    "s_lshl_b32 s49, s72, 7\n"
    "s_add_u32 s49, s49, 0x1c00\n"
    "v_mov_b32 v9, 0x7fffff00\n"
    "v_mov_b32 v2, s49\n"
    "v_cmp_eq_u32 vcc, 0, v10\n"
    "s_nop 1\n"
    "v_cndmask_b32 v9, v9, v2, vcc\n"
    "v_mov_b32 v4, 0x1f440\n"
    "v_mov_b32 v15, 0\n"
    "ds_write_b32 v4, v15\n"
    "ds_write_b32 v4, v15 offset:12\n"
    "s_lshr_b32 s49, s72, 2\n"
    "s_mul_i32 s49, s49, 896\n"
    "s_add_u32 s49, s49, 0x0\n"
    "v_min_u32 v170, 6, v10\n"
    "v_lshlrev_b32 v170, 7, v170\n"
    "v_add_u32 v170, s49, v170\n"
    "s_mul_i32 s51, s72, 401408\n"
    "s_add_u32 s51, s51, 0xd3000\n"
    "s_add_u32 s49, s51, 344064\n"
    "s_mov_b32 m0, 0xf000\n"
    "s_nop 0\n"
    "buffer_load_dwordx4 v0, s[40:43], s49 offen offset:0 lds\n"
    "buffer_load_dwordx4 v0, s[40:43], s49 offen offset:1024 lds\n"
    "buffer_load_dwordx4 v0, s[40:43], s49 offen offset:2048 lds\n"
    "buffer_load_dwordx4 v0, s[40:43], s49 offen offset:3072 lds\n"
    "s_mov_b32 m0, 0x10000\n"
    "s_add_u32 s49, s49, 4096\n"
    "buffer_load_dwordx4 v0, s[40:43], s49 offen offset:0 lds\n"
    "buffer_load_dwordx4 v0, s[40:43], s49 offen offset:1024 lds\n"
    "buffer_load_dwordx4 v0, s[40:43], s49 offen offset:2048 lds\n"
    "buffer_load_dwordx4 v0, s[40:43], s49 offen offset:3072 lds\n"
    "s_mov_b32 m0, 0x11000\n"
    "s_add_u32 s49, s49, 4096\n"
    "buffer_load_dwordx4 v0, s[40:43], s49 offen offset:0 lds\n"
    "buffer_load_dwordx4 v0, s[40:43], s49 offen offset:1024 lds\n"
    "buffer_load_dwordx4 v0, s[40:43], s49 offen offset:2048 lds\n"
    "buffer_load_dwordx4 v0, s[40:43], s49 offen offset:3072 lds\n"
    "s_mov_b32 m0, 0x12000\n"
    "s_add_u32 s49, s49, 4096\n"
    "buffer_load_dwordx4 v0, s[40:43], s49 offen offset:0 lds\n"
    "buffer_load_dwordx4 v0, s[40:43], s49 offen offset:1024 lds\n"
    "s_add_u32 s49, s51, 0\n"
    "buffer_load_dwordx4 a[0:3], v0, s[40:43], s49 offen offset:0\n"
    "buffer_load_dwordx4 a[4:7], v0, s[40:43], s49 offen offset:1024\n"
    "buffer_load_dwordx4 a[8:11], v0, s[40:43], s49 offen offset:2048\n"
    "buffer_load_dwordx4 a[12:15], v0, s[40:43], s49 offen offset:3072\n"
    "s_add_u32 s49, s49, 4096\n"
    "buffer_load_dwordx4 a[16:19], v0, s[40:43], s49 offen offset:0\n"
    "buffer_load_dwordx4 a[20:23], v0, s[40:43], s49 offen offset:1024\n"
    "buffer_load_dwordx4 a[24:27], v0, s[40:43], s49 offen offset:2048\n"
    "buffer_load_dwordx4 a[28:31], v0, s[40:43], s49 offen offset:3072\n"
    "s_add_u32 s49, s49, 4096\n"
    "buffer_load_dwordx4 a[32:35], v0, s[40:43], s49 offen offset:0\n"
    "buffer_load_dwordx4 a[36:39], v0, s[40:43], s49 offen offset:1024\n"
    "buffer_load_dwordx4 a[40:43], v0, s[40:43], s49 offen offset:2048\n"
    "buffer_load_dwordx4 a[44:47], v0, s[40:43], s49 offen offset:3072\n"
    "s_add_u32 s49, s49, 4096\n"
    "buffer_load_dwordx4 a[48:51], v0, s[40:43], s49 offen offset:0\n"
    "buffer_load_dwordx4 a[52:55], v0, s[40:43], s49 offen offset:1024\n"
    "buffer_load_dwordx4 a[56:59], v0, s[40:43], s49 offen offset:2048\n"
    "buffer_load_dwordx4 a[60:63], v0, s[40:43], s49 offen offset:3072\n"
    "s_add_u32 s49, s49, 4096\n"
    "buffer_load_dwordx4 a[64:67], v0, s[40:43], s49 offen offset:0\n"
    "buffer_load_dwordx4 a[68:71], v0, s[40:43], s49 offen offset:1024\n"
    "buffer_load_dwordx4 a[72:75], v0, s[40:43], s49 offen offset:2048\n"
    "buffer_load_dwordx4 a[76:79], v0, s[40:43], s49 offen offset:3072\n"
    "s_add_u32 s49, s49, 4096\n"
    "buffer_load_dwordx4 a[80:83], v0, s[40:43], s49 offen offset:0\n"
    "buffer_load_dwordx4 a[84:87], v0, s[40:43], s49 offen offset:1024\n"
    "buffer_load_dwordx4 a[88:91], v0, s[40:43], s49 offen offset:2048\n"
    "buffer_load_dwordx4 a[92:95], v0, s[40:43], s49 offen offset:3072\n"
    "s_add_u32 s49, s49, 4096\n"
    "buffer_load_dwordx4 a[96:99], v0, s[40:43], s49 offen offset:0\n"
    "buffer_load_dwordx4 a[100:103], v0, s[40:43], s49 offen offset:1024\n"
    "buffer_load_dwordx4 a[104:107], v0, s[40:43], s49 offen offset:2048\n"
    "buffer_load_dwordx4 a[108:111], v0, s[40:43], s49 offen offset:3072\n"
    "s_add_u32 s49, s49, 4096\n"
    "buffer_load_dwordx4 a[112:115], v0, s[40:43], s49 offen offset:0\n"
    "buffer_load_dwordx4 a[116:119], v0, s[40:43], s49 offen offset:1024\n"
    "buffer_load_dwordx4 a[120:123], v0, s[40:43], s49 offen offset:2048\n"
    "buffer_load_dwordx4 a[124:127], v0, s[40:43], s49 offen offset:3072\n"
    "s_add_u32 s49, s49, 4096\n"
    "buffer_load_dwordx4 a[128:131], v0, s[40:43], s49 offen offset:0\n"
    "buffer_load_dwordx4 a[132:135], v0, s[40:43], s49 offen offset:1024\n"
    "buffer_load_dwordx4 a[136:139], v0, s[40:43], s49 offen offset:2048\n"
    "buffer_load_dwordx4 a[140:143], v0, s[40:43], s49 offen offset:3072\n"
    "s_add_u32 s49, s49, 4096\n"
    "buffer_load_dwordx4 a[144:147], v0, s[40:43], s49 offen offset:0\n"
    "buffer_load_dwordx4 a[148:151], v0, s[40:43], s49 offen offset:1024\n"
    "buffer_load_dwordx4 a[152:155], v0, s[40:43], s49 offen offset:2048\n"
    "buffer_load_dwordx4 a[156:159], v0, s[40:43], s49 offen offset:3072\n"
    "s_add_u32 s49, s49, 4096\n"
    "s_waitcnt vmcnt(40)\n"
    "buffer_load_dwordx4 a[160:163], v0, s[40:43], s49 offen offset:0\n"
    "buffer_load_dwordx4 a[164:167], v0, s[40:43], s49 offen offset:1024\n"
    "buffer_load_dwordx4 a[168:171], v0, s[40:43], s49 offen offset:2048\n"
    "buffer_load_dwordx4 a[172:175], v0, s[40:43], s49 offen offset:3072\n"
    "s_add_u32 s49, s49, 4096\n"
    "buffer_load_dwordx4 a[176:179], v0, s[40:43], s49 offen offset:0\n"
    "buffer_load_dwordx4 a[180:183], v0, s[40:43], s49 offen offset:1024\n"
    "buffer_load_dwordx4 a[184:187], v0, s[40:43], s49 offen offset:2048\n"
    "buffer_load_dwordx4 a[188:191], v0, s[40:43], s49 offen offset:3072\n"
    "s_add_u32 s49, s49, 4096\n"
    "s_waitcnt vmcnt(40)\n"
    "buffer_load_dwordx4 a[192:195], v0, s[40:43], s49 offen offset:0\n"
    "buffer_load_dwordx4 a[196:199], v0, s[40:43], s49 offen offset:1024\n"
    "buffer_load_dwordx4 a[200:203], v0, s[40:43], s49 offen offset:2048\n"
    "buffer_load_dwordx4 a[204:207], v0, s[40:43], s49 offen offset:3072\n"
    "s_add_u32 s49, s49, 4096\n"
    "buffer_load_dwordx4 a[208:211], v0, s[40:43], s49 offen offset:0\n"
    "buffer_load_dwordx4 a[212:215], v0, s[40:43], s49 offen offset:1024\n"
    "buffer_load_dwordx4 a[216:219], v0, s[40:43], s49 offen offset:2048\n"
    "buffer_load_dwordx4 a[220:223], v0, s[40:43], s49 offen offset:3072\n"
    "s_add_u32 s49, s49, 4096\n"
    "s_waitcnt vmcnt(40)\n"
    "buffer_load_dwordx4 a[224:227], v0, s[40:43], s49 offen offset:0\n"
    "buffer_load_dwordx4 a[228:231], v0, s[40:43], s49 offen offset:1024\n"
    "buffer_load_dwordx4 a[232:235], v0, s[40:43], s49 offen offset:2048\n"
    "buffer_load_dwordx4 a[236:239], v0, s[40:43], s49 offen offset:3072\n"
    "s_add_u32 s49, s49, 4096\n"
    "buffer_load_dwordx4 a[240:243], v0, s[40:43], s49 offen offset:0\n"
    "buffer_load_dwordx4 a[244:247], v0, s[40:43], s49 offen offset:1024\n"
    "buffer_load_dwordx4 a[248:251], v0, s[40:43], s49 offen offset:2048\n"
    "buffer_load_dwordx4 a[252:255], v0, s[40:43], s49 offen offset:3072\n"
    "s_add_u32 s49, s49, 4096\n"
    "s_waitcnt vmcnt(40)\n"
    "buffer_load_dwordx4 v[174:177], v0, s[40:43], s49 offen offset:0\n"
    "buffer_load_dwordx4 v[178:181], v0, s[40:43], s49 offen offset:1024\n"
    "buffer_load_dwordx4 v[182:185], v0, s[40:43], s49 offen offset:2048\n"
    "buffer_load_dwordx4 v[186:189], v0, s[40:43], s49 offen offset:3072\n"
    "s_add_u32 s49, s49, 4096\n"
    "buffer_load_dwordx4 v[190:193], v0, s[40:43], s49 offen offset:0\n"
    "buffer_load_dwordx4 v[194:197], v0, s[40:43], s49 offen offset:1024\n"
    "buffer_load_dwordx4 v[198:201], v0, s[40:43], s49 offen offset:2048\n"
    "buffer_load_dwordx4 v[202:205], v0, s[40:43], s49 offen offset:3072\n"
    "s_add_u32 s49, s49, 4096\n"
    "s_waitcnt vmcnt(40)\n"
    "buffer_load_dwordx4 v[206:209], v0, s[40:43], s49 offen offset:0\n"
    "buffer_load_dwordx4 v[210:213], v0, s[40:43], s49 offen offset:1024\n"
    "buffer_load_dwordx4 v[214:217], v0, s[40:43], s49 offen offset:2048\n"
    "buffer_load_dwordx4 v[218:221], v0, s[40:43], s49 offen offset:3072\n"
    "s_add_u32 s49, s49, 4096\n"
    "buffer_load_dwordx4 v[222:225], v0, s[40:43], s49 offen offset:0\n"
    "buffer_load_dwordx4 v[226:229], v0, s[40:43], s49 offen offset:1024\n"
    "buffer_load_dwordx4 v[230:233], v0, s[40:43], s49 offen offset:2048\n"
    "buffer_load_dwordx4 v[234:237], v0, s[40:43], s49 offen offset:3072\n"
    "s_add_u32 s49, s49, 4096\n"
    "s_waitcnt vmcnt(40)\n"
    "buffer_load_dwordx4 v[238:241], v0, s[40:43], s49 offen offset:0\n"
    "buffer_load_dwordx4 v[242:245], v0, s[40:43], s49 offen offset:1024\n"
    "buffer_load_dwordx4 v[246:249], v0, s[40:43], s49 offen offset:2048\n"
    "buffer_load_dwordx4 v[250:253], v0, s[40:43], s49 offen offset:3072\n"
    "v_min_u32 v168, 4, v10\n"
    "v_lshlrev_b32 v168, 2, v168\n"
    "v_add_u32 v168, 0xf800, v168\n"
    "s_mov_b32 s50, 0\n"
    "LS0_%=_pt:\n"
    "buffer_load_dword v169, v168, s[40:43], 0 offen sc1\n"
    "s_add_u32 s50, s50, 1\n"
    "s_cmp_gt_u32 s50, 30000\n"
    "s_cbranch_scc1 LS_exit_%=\n"
    "s_waitcnt vmcnt(0)\n"
    "v_cmp_eq_u32 vcc, -1, v169\n"
    "s_cbranch_vccnz LS0_%=_pt\n"
    "v_readlane_b32 s49, v169, 0\n"
    "s_nop 1\n"
    "s_bfe_u32 s39, s49, 0x70000\n"
    "s_lshl_b32 s39, s39, 12\n"
    "s_add_u32 s39, s39, %7\n"
    "s_bfe_u32 s54, s49, 0x70008\n"
    "s_lshl_b32 s54, s54, 12\n"
    "s_add_u32 s54, s54, %7\n"
    "s_bfe_u32 s55, s49, 0x70010\n"
    "s_lshl_b32 s55, s55, 12\n"
    "s_add_u32 s55, s55, %7\n"
    "s_bfe_u32 s56, s49, 0x70018\n"
    "s_lshl_b32 s56, s56, 12\n"
    "s_add_u32 s56, s56, %7\n"
    "v_readlane_b32 s49, v169, 1\n"
    "s_nop 1\n"
    "s_bfe_u32 s57, s49, 0x70000\n"
    "s_lshl_b32 s57, s57, 12\n"
    "s_add_u32 s57, s57, %7\n"
    "s_bfe_u32 s58, s49, 0x70008\n"
    "s_lshl_b32 s58, s58, 12\n"
    "s_add_u32 s58, s58, %7\n"
    "s_bfe_u32 s59, s49, 0x70010\n"
    "s_lshl_b32 s59, s59, 12\n"
    "s_add_u32 s59, s59, %7\n"
    "s_bfe_u32 s60, s49, 0x70018\n"
    "s_lshl_b32 s60, s60, 12\n"
    "s_add_u32 s60, s60, %7\n"
    "v_readlane_b32 s49, v169, 2\n"
    "s_nop 1\n"
    "s_bfe_u32 s61, s49, 0x70000\n"
    "s_lshl_b32 s61, s61, 12\n"
    "s_add_u32 s61, s61, %7\n"
    "s_bfe_u32 s62, s49, 0x70008\n"
    "s_lshl_b32 s62, s62, 12\n"
    "s_add_u32 s62, s62, %7\n"
    "s_bfe_u32 s63, s49, 0x70010\n"
    "s_lshl_b32 s63, s63, 12\n"
    "s_add_u32 s63, s63, %7\n"
    "s_bfe_u32 s64, s49, 0x70018\n"
    "s_lshl_b32 s64, s64, 12\n"
    "s_add_u32 s64, s64, %7\n"
    "v_readlane_b32 s49, v169, 3\n"
    "s_nop 1\n"
    "s_bfe_u32 s65, s49, 0x70000\n"
    "s_lshl_b32 s65, s65, 12\n"
    "s_add_u32 s65, s65, %7\n"
    "s_bfe_u32 s66, s49, 0x70008\n"
    "s_lshl_b32 s66, s66, 12\n"
    "s_add_u32 s66, s66, %7\n"
    "s_bfe_u32 s67, s49, 0x70010\n"
    "s_lshl_b32 s67, s67, 12\n"
    "s_add_u32 s67, s67, %7\n"
    "s_bfe_u32 s68, s49, 0x70018\n"
    "s_lshl_b32 s68, s68, 12\n"
    "s_add_u32 s68, s68, %7\n"
    "v_readlane_b32 s49, v169, 4\n"
    "s_nop 1\n"
    "s_bfe_u32 s77, s49, 0x70000\n"
    "s_lshl_b32 s77, s77, 12\n"
    "s_add_u32 s77, s77, %7\n"
    "s_bfe_u32 s78, s49, 0x70008\n"
    "s_lshl_b32 s78, s78, 12\n"
    "s_add_u32 s78, s78, %7\n"
    "s_bfe_u32 s48, s49, 0x70010\n"
    "s_lshl_b32 s48, s48, 12\n"
    "s_add_u32 s48, s48, %7\n"
    "s_mov_b32 s96, 1\n"
    "s_mov_b32 s52, 1\n"
    "s_mov_b32 s50, 0\n"
    "LS0_%=_pf0_poll:\n"
    "buffer_load_dword v171, v170, s[40:43], 0 offen sc1\n"
    "s_add_u32 s50, s50, 1\n"
    "s_cmp_gt_u32 s50, 30000\n"
    "s_cbranch_scc1 LS_exit_%=\n"
    "s_waitcnt vmcnt(0)\n"
    "v_cmp_gt_u32 vcc, s52, v171\n"
    "s_cbranch_vccnz LS0_%=_pf0_poll\n"
    "LS0_%=_pf0_ok:\n"
    "s_sub_u32 s49, s96, 1\n"
    "s_and_b32 s99, s49, 1\n"
    "s_lshl_b32 s99, s99, 15\n"
    "s_add_u32 s99, s99, 0\n"
    "s_min_u32 s49, s49, 63\n"
    "s_lshl_b32 s49, s49, 4\n"
    "s_add_u32 s49, s49, s72\n"
    "s_mul_i32 s49, s49, 28672\n"
    "s_add_u32 s97, s49, 0xed3000\n"
    "s_add_u32 m0, s99, 0\n"
    "s_add_u32 s98, s97, 0\n"
    "buffer_load_dwordx4 v0, s[40:43], s98 offen offset:0 sc1 lds\n"
    "buffer_load_dwordx4 v0, s[40:43], s98 offen offset:1024 sc1 lds\n"
    "buffer_load_dwordx4 v0, s[40:43], s98 offen offset:2048 sc1 lds\n"
    "buffer_load_dwordx4 v0, s[40:43], s98 offen offset:3072 sc1 lds\n"
    "s_add_u32 m0, s99, 4096\n"
    "s_add_u32 s98, s97, 4096\n"
    "buffer_load_dwordx4 v0, s[40:43], s98 offen offset:0 sc1 lds\n"
    "buffer_load_dwordx4 v0, s[40:43], s98 offen offset:1024 sc1 lds\n"
    "s_waitcnt vmcnt(0)\n"
    ".p2align 6\n"
    "s_nop 0\n"
    "LS0_%=_top:\n"
    "v_mov_b32 v14, s44\n"
    "s_mov_b32 s50, 0\n"
    "s_add_u32 s49, s44, 1\n"
    "v_mov_b32 v15, s49\n"
    "v_mov_b32 v44, 0\n"
    "v_mov_b32 v45, 0\n"
    "v_mov_b32 v46, 0\n"
    "v_mov_b32 v47, 0\n"
    "v_mov_b32 v48, 0\n"
    "v_mov_b32 v49, 0\n"
    "v_mov_b32 v50, 0\n"
    "v_mov_b32 v51, 0\n"
    "v_mov_b32 v52, 0\n"
    "v_mov_b32 v53, 0\n"
    "v_mov_b32 v54, 0\n"
    "v_mov_b32 v55, 0\n"
    "v_mov_b32 v56, 0\n"
    "v_mov_b32 v57, 0\n"
    "v_mov_b32 v58, 0\n"
    "v_mov_b32 v59, 0\n"
    "v_mov_b32 v60, 0\n"
    "v_mov_b32 v61, 0\n"
    "v_mov_b32 v62, 0\n"
    "v_mov_b32 v63, 0\n"
    "v_mov_b32 v64, 0\n"
    "v_mov_b32 v65, 0\n"
    "v_mov_b32 v66, 0\n"
    "v_mov_b32 v67, 0\n"
    "v_mov_b32 v68, 0\n"
    "v_mov_b32 v69, 0\n"
    "v_mov_b32 v70, 0\n"
    "v_mov_b32 v71, 0\n"
    "v_mov_b32 v72, 0\n"
    "v_mov_b32 v73, 0\n"
    "v_mov_b32 v74, 0\n"
    "v_mov_b32 v75, 0\n"
    "s_waitcnt lgkmcnt(0)\n"
    "s_barrier\n"
    "LS0_%=_issue:\n"
    "buffer_load_dwordx4 v[16:19], v0, s[40:43], s69 offen offset:0 sc1\n"
    "ds_read_b128 v[76:79], v5 offset:0\n"
    "ds_read_b128 v[80:83], v5 offset:1024\n"
    "s_nop 8\n"
    "buffer_load_dwordx4 v[20:23], v0, s[40:43], s69 offen offset:1024 sc1\n"
    "ds_read_b128 v[84:87], v5 offset:14336\n"
    "ds_read_b128 v[88:91], v5 offset:15360\n"
    "s_nop 8\n"
    "buffer_load_dwordx4 v[24:27], v0, s[40:43], s69 offen offset:2048 sc1\n"
    "ds_read_b128 v[92:95], v5 offset:2048\n"
    "ds_read_b128 v[96:99], v5 offset:3072\n"
    "s_nop 8\n"
    "buffer_load_dwordx4 v[28:31], v0, s[40:43], s69 offen offset:3072 sc1\n"
    "ds_read_b128 v[100:103], v5 offset:16384\n"
    "ds_read_b128 v[104:107], v5 offset:17408\n"
    "s_nop 8\n"
    "buffer_load_dwordx4 v[32:35], v0, s[40:43], s79 offen offset:0 sc1\n"
    "ds_read_b128 v[108:111], v5 offset:4096\n"
    "ds_read_b128 v[112:115], v5 offset:5120\n"
    "s_nop 8\n"
    "buffer_load_dwordx4 v[36:39], v0, s[40:43], s79 offen offset:1024 sc1\n"
    "ds_read_b128 v[116:119], v5 offset:18432\n"
    "ds_read_b128 v[120:123], v5 offset:19456\n"
    "s_nop 8\n"
    "buffer_load_dwordx4 v[40:43], v0, s[40:43], s79 offen offset:2048 sc1\n"
    "LS0_%=_compute:\n"
    "s_waitcnt vmcnt(6)\n"
    "v_smfmac_f32_16x16x64_bf16 v[44:47], v[16:19], a[0:7], v1\n"
    "v_smfmac_f32_16x16x64_bf16 v[48:51], v[16:19], a[56:63], v1\n"
    "buffer_store_dword v14, v9, s[40:43], 0 offen sc1\n"
    "v_smfmac_f32_16x16x64_bf16 v[52:55], v[16:19], a[112:119], v1\n"
    "v_smfmac_f32_16x16x64_bf16 v[56:59], v[16:19], a[168:175], v1\n"
    "v_smfmac_f32_16x16x64_bf16 v[60:63], v[16:19], a[224:231], v1\n"
    "v_smfmac_f32_16x16x64_bf16 v[64:67], v[16:19], v[198:205], v1\n"
    "s_waitcnt lgkmcnt(10)\n"
    "v_smfmac_f32_16x16x64_bf16 v[68:71], v[16:19], v[76:83], v1\n"
    "ds_read_b128 v[76:79], v5 offset:6144\n"
    "ds_read_b128 v[80:83], v5 offset:7168\n"
    "s_waitcnt lgkmcnt(10)\n"
    "v_smfmac_f32_16x16x64_bf16 v[72:75], v[16:19], v[84:91], v1\n"
    "ds_read_b128 v[84:87], v5 offset:8192\n"
    "ds_read_b128 v[88:91], v5 offset:9216\n"
    "s_waitcnt vmcnt(6)\n"
    "v_smfmac_f32_16x16x64_bf16 v[44:47], v[20:23], a[8:15], v1\n"
    "v_smfmac_f32_16x16x64_bf16 v[48:51], v[20:23], a[64:71], v1\n"
    "v_smfmac_f32_16x16x64_bf16 v[52:55], v[20:23], a[120:127], v1\n"
    "v_smfmac_f32_16x16x64_bf16 v[56:59], v[20:23], a[176:183], v1\n"
    "v_smfmac_f32_16x16x64_bf16 v[60:63], v[20:23], a[232:239], v1\n"
    "v_smfmac_f32_16x16x64_bf16 v[64:67], v[20:23], v[206:213], v1\n"
    "s_waitcnt lgkmcnt(10)\n"
    "v_smfmac_f32_16x16x64_bf16 v[68:71], v[20:23], v[92:99], v1\n"
    "ds_read_b128 v[92:95], v5 offset:10240\n"
    "ds_read_b128 v[96:99], v5 offset:11264\n"
    "s_waitcnt lgkmcnt(10)\n"
    "v_smfmac_f32_16x16x64_bf16 v[72:75], v[20:23], v[100:107], v1\n"
    "ds_read_b128 v[100:103], v5 offset:12288\n"
    "ds_read_b128 v[104:107], v5 offset:13312\n"
    "s_waitcnt vmcnt(5)\n"
    "v_smfmac_f32_16x16x64_bf16 v[44:47], v[24:27], a[16:23], v1\n"
    "v_smfmac_f32_16x16x64_bf16 v[48:51], v[24:27], a[72:79], v1\n"
    "v_smfmac_f32_16x16x64_bf16 v[52:55], v[24:27], a[128:135], v1\n"
    "v_smfmac_f32_16x16x64_bf16 v[56:59], v[24:27], a[184:191], v1\n"
    "v_smfmac_f32_16x16x64_bf16 v[60:63], v[24:27], a[240:247], v1\n"
    "v_smfmac_f32_16x16x64_bf16 v[64:67], v[24:27], v[214:221], v1\n"
    "s_waitcnt lgkmcnt(10)\n"
    "v_smfmac_f32_16x16x64_bf16 v[68:71], v[24:27], v[108:115], v1\n"
    "ds_read_b128 v[108:111], v5 offset:20480\n"
    "ds_read_b128 v[112:115], v5 offset:21504\n"
    "s_waitcnt lgkmcnt(10)\n"
    "v_smfmac_f32_16x16x64_bf16 v[72:75], v[24:27], v[116:123], v1\n"
    "ds_read_b128 v[116:119], v5 offset:22528\n"
    "ds_read_b128 v[120:123], v5 offset:23552\n"
    "s_waitcnt vmcnt(4)\n"
    "s_waitcnt lgkmcnt(10)\n"
    "v_smfmac_f32_16x16x64_bf16 v[68:71], v[28:31], v[76:83], v1\n"
    "ds_read_b128 v[76:79], v5 offset:24576\n"
    "ds_read_b128 v[80:83], v5 offset:25600\n"
    "s_waitcnt vmcnt(3)\n"
    "s_waitcnt lgkmcnt(10)\n"
    "v_smfmac_f32_16x16x64_bf16 v[68:71], v[32:35], v[84:91], v1\n"
    "ds_read_b128 v[84:87], v5 offset:26624\n"
    "ds_read_b128 v[88:91], v5 offset:27648\n"
    "s_waitcnt vmcnt(2)\n"
    "s_waitcnt lgkmcnt(10)\n"
    "v_smfmac_f32_16x16x64_bf16 v[68:71], v[36:39], v[92:99], v1\n"
    "s_waitcnt vmcnt(1)\n"
    "s_waitcnt lgkmcnt(8)\n"
    "v_smfmac_f32_16x16x64_bf16 v[68:71], v[40:43], v[100:107], v1\n"
    "s_waitcnt lgkmcnt(6)\n"
    "v_smfmac_f32_16x16x64_bf16 v[72:75], v[28:31], v[108:115], v1\n"
    "s_waitcnt lgkmcnt(4)\n"
    "v_smfmac_f32_16x16x64_bf16 v[72:75], v[32:35], v[116:123], v1\n"
    "v_add_f32 v68, v68, v70\n"
    "s_waitcnt lgkmcnt(2)\n"
    "v_smfmac_f32_16x16x64_bf16 v[72:75], v[36:39], v[76:83], v1\n"
    "v_add_f32 v69, v69, v71\n"
    "s_waitcnt lgkmcnt(0)\n"
    "v_smfmac_f32_16x16x64_bf16 v[72:75], v[40:43], v[84:91], v1\n"
    "v_cmp_u_f32 vcc, v68, v69\n"
    "v_smfmac_f32_16x16x64_bf16 v[44:47], v[28:31], a[24:31], v1\n"
    "s_cbranch_vccnz LS0_%=_nan\n"
    "LS0_%=_good:\n"
    "ds_write_b32 v4, v15\n"
    "v_smfmac_f32_16x16x64_bf16 v[44:47], v[32:35], a[32:39], v1\n"
    "ds_write_b32 v6, v68 offset:0\n"
    "ds_write_b32 v6, v69 offset:32\n"
    "v_smfmac_f32_16x16x64_bf16 v[44:47], v[36:39], a[40:47], v1\n"
    "v_add_f32 v72, v72, v74\n"
    "v_smfmac_f32_16x16x64_bf16 v[44:47], v[40:43], a[48:55], v1\n"
    "v_add_f32 v73, v73, v75\n"
    "v_smfmac_f32_16x16x64_bf16 v[48:51], v[28:31], a[80:87], v1\n"
    "ds_write_b32 v6, v72 offset:8\n"
    "ds_write_b32 v6, v73 offset:40\n"
    "ds_write_b32 v4, v15 offset:12\n"
    "v_smfmac_f32_16x16x64_bf16 v[48:51], v[32:35], a[88:95], v1\n"
    "v_add_f32 v44, v44, v46\n"
    "v_smfmac_f32_16x16x64_bf16 v[48:51], v[36:39], a[96:103], v1\n"
    "v_add_f32 v45, v45, v47\n"
    "v_smfmac_f32_16x16x64_bf16 v[48:51], v[40:43], a[104:111], v1\n"
    "v_cvt_pk_bf16_f32 v44, v44, v45\n"
    "v_smfmac_f32_16x16x64_bf16 v[52:55], v[28:31], a[136:143], v1\n"
    "ds_write_b32 v3, v44 offset:0\n"
    "v_smfmac_f32_16x16x64_bf16 v[52:55], v[32:35], a[144:151], v1\n"
    "v_add_f32 v48, v48, v50\n"
    "v_smfmac_f32_16x16x64_bf16 v[52:55], v[36:39], a[152:159], v1\n"
    "v_add_f32 v49, v49, v51\n"
    "v_smfmac_f32_16x16x64_bf16 v[52:55], v[40:43], a[160:167], v1\n"
    "v_cvt_pk_bf16_f32 v48, v48, v49\n"
    "v_smfmac_f32_16x16x64_bf16 v[56:59], v[28:31], a[192:199], v1\n"
    "ds_write_b32 v3, v48 offset:64\n"
    "v_smfmac_f32_16x16x64_bf16 v[56:59], v[32:35], a[200:207], v1\n"
    "v_add_f32 v52, v52, v54\n"
    "v_smfmac_f32_16x16x64_bf16 v[56:59], v[36:39], a[208:215], v1\n"
    "v_add_f32 v53, v53, v55\n"
    "v_smfmac_f32_16x16x64_bf16 v[56:59], v[40:43], a[216:223], v1\n"
    "v_cvt_pk_bf16_f32 v52, v52, v53\n"
    "v_smfmac_f32_16x16x64_bf16 v[60:63], v[28:31], a[248:255], v1\n"
    "ds_write_b32 v3, v52 offset:512\n"
    "v_smfmac_f32_16x16x64_bf16 v[60:63], v[32:35], v[174:181], v1\n"
    "v_add_f32 v56, v56, v58\n"
    "v_smfmac_f32_16x16x64_bf16 v[60:63], v[36:39], v[182:189], v1\n"
    "v_add_f32 v57, v57, v59\n"
    "v_smfmac_f32_16x16x64_bf16 v[60:63], v[40:43], v[190:197], v1\n"
    "v_cvt_pk_bf16_f32 v56, v56, v57\n"
    "v_smfmac_f32_16x16x64_bf16 v[64:67], v[28:31], v[222:229], v1\n"
    "ds_write_b32 v3, v56 offset:576\n"
    "v_smfmac_f32_16x16x64_bf16 v[64:67], v[32:35], v[230:237], v1\n"
    "v_add_f32 v60, v60, v62\n"
    "v_smfmac_f32_16x16x64_bf16 v[64:67], v[36:39], v[238:245], v1\n"
    "v_add_f32 v61, v61, v63\n"
    "v_smfmac_f32_16x16x64_bf16 v[64:67], v[40:43], v[246:253], v1\n"
    "v_cvt_pk_bf16_f32 v60, v60, v61\n"
    "s_nop 6\n"
    "v_pk_add_f32 v[64:65], v[64:65], v[66:67]\n"
    "v_cvt_pk_bf16_f32 v64, v64, v65\n"
    "ds_write_b32 v3, v60 offset:1024\n"
    "ds_write_b32 v3, v64 offset:1088\n"
    "s_waitcnt lgkmcnt(0)\n"
    "s_barrier\n"
    "s_nop 7\n"
    "s_nop 7\n"
    "s_nop 7\n"
    "ds_read_b64_tr_b16 v[76:77], v7 offset:0\n"
    "ds_read_b64_tr_b16 v[78:79], v8 offset:0\n"
    "ds_read_b64_tr_b16 v[80:81], v7 offset:1024\n"
    "ds_read_b64_tr_b16 v[82:83], v8 offset:1024\n"
    "ds_read_b64_tr_b16 v[84:85], v7 offset:2048\n"
    "ds_read_b64_tr_b16 v[86:87], v8 offset:2048\n"
    "ds_read_b64_tr_b16 v[88:89], v7 offset:3072\n"
    "ds_read_b64_tr_b16 v[90:91], v8 offset:3072\n"
    "ds_read_b64_tr_b16 v[92:93], v7 offset:4096\n"
    "ds_read_b64_tr_b16 v[94:95], v8 offset:4096\n"
    "ds_read_b64_tr_b16 v[96:97], v7 offset:5120\n"
    "ds_read_b64_tr_b16 v[98:99], v8 offset:5120\n"
    "ds_read_b64_tr_b16 v[100:101], v7 offset:6144\n"
    "ds_read_b64_tr_b16 v[102:103], v8 offset:6144\n"
    "s_lshl_b32 s49, s44, 4\n"
    "s_add_u32 s49, s49, s72\n"
    "s_mul_i32 s49, s49, 7168\n"
    "s_add_u32 s49, s49, 0x46d3000\n"
    "s_add_u32 s53, s49, 4096\n"
    "s_waitcnt lgkmcnt(12)\n"
    "buffer_store_dwordx4 v[76:79], v0, s[40:43], s49 offen offset:0 sc1\n"
    "s_waitcnt lgkmcnt(10)\n"
    "buffer_store_dwordx4 v[80:83], v0, s[40:43], s49 offen offset:1024 sc1\n"
    "s_waitcnt lgkmcnt(8)\n"
    "buffer_store_dwordx4 v[84:87], v0, s[40:43], s49 offen offset:2048 sc1\n"
    "s_waitcnt lgkmcnt(6)\n"
    "buffer_store_dwordx4 v[88:91], v0, s[40:43], s49 offen offset:3072 sc1\n"
    "s_waitcnt lgkmcnt(4)\n"
    "buffer_store_dwordx4 v[92:95], v0, s[40:43], s53 offen offset:0 sc1\n"
    "s_waitcnt lgkmcnt(2)\n"
    "buffer_store_dwordx4 v[96:99], v0, s[40:43], s53 offen offset:1024 sc1\n"
    "s_waitcnt lgkmcnt(0)\n"
    "buffer_store_dwordx4 v[100:103], v0, s[40:43], s53 offen offset:2048 sc1\n"
    "s_cmp_lg_u32 s44, 63\n"
    "s_cbranch_scc1 LS0_%=_more\n"
    "s_waitcnt vmcnt(0)\n"
    "buffer_store_dword v15, v9, s[40:43], 0 offen sc1\n"
    "LS0_%=_more:\n"
    "v_xor_b32 v5, 0x8000, v5\n"
    "s_barrier\n"
    "s_add_u32 s52, s96, 1\n"
    "s_add_u32 s48, s44, 2\n"
    "s_cmp_lt_u32 s96, s48\n"
    "s_cbranch_scc1 LS0_%=_late\n"
    "LS0_%=_early:\n"
    "v_cmp_gt_u32 vcc, s52, v171\n"
    "s_cbranch_vccnz LS0_%=_none\n"
    "s_add_u32 s96, s96, 1\n"
    "s_sub_u32 s49, s96, 1\n"
    "s_and_b32 s99, s49, 1\n"
    "s_lshl_b32 s99, s99, 15\n"
    "s_add_u32 s99, s99, 0\n"
    "s_min_u32 s49, s49, 63\n"
    "s_lshl_b32 s49, s49, 4\n"
    "s_add_u32 s49, s49, s72\n"
    "s_mul_i32 s49, s49, 28672\n"
    "s_add_u32 s97, s49, 0xed3000\n"
    "s_add_u32 m0, s99, 0\n"
    "s_add_u32 s98, s97, 0\n"
    "buffer_load_dwordx4 v0, s[40:43], s98 offen offset:0 sc1 lds\n"
    "buffer_load_dwordx4 v0, s[40:43], s98 offen offset:1024 sc1 lds\n"
    "buffer_load_dwordx4 v0, s[40:43], s98 offen offset:2048 sc1 lds\n"
    "buffer_load_dwordx4 v0, s[40:43], s98 offen offset:3072 sc1 lds\n"
    "s_add_u32 m0, s99, 4096\n"
    "s_add_u32 s98, s97, 4096\n"
    "buffer_load_dwordx4 v0, s[40:43], s98 offen offset:0 sc1 lds\n"
    "buffer_load_dwordx4 v0, s[40:43], s98 offen offset:1024 sc1 lds\n"
    "LS0_%=_none:\n"
    "buffer_load_dword v171, v170, s[40:43], 0 offen sc1\n"
    "s_cmp_eq_u32 s44, 63\n"
    "s_cbranch_scc1 LS_exit_%=\n"
    "s_add_u32 s44, s44, 1\n"
    "s_mov_b32 s49, s54\n"
    "s_mov_b32 s53, s55\n"
    "s_mov_b32 s54, s56\n"
    "s_mov_b32 s55, s57\n"
    "s_mov_b32 s56, s58\n"
    "s_mov_b32 s57, s59\n"
    "s_mov_b32 s58, s49\n"
    "s_mov_b32 s59, s53\n"
    "s_mov_b32 s69, s54\n"
    "s_mov_b32 s79, s55\n"
    "s_mov_b32 s49, s60\n"
    "s_mov_b32 s60, s63\n"
    "s_mov_b32 s63, s66\n"
    "s_mov_b32 s66, s49\n"
    "s_mov_b32 s49, s61\n"
    "s_mov_b32 s61, s64\n"
    "s_mov_b32 s64, s67\n"
    "s_mov_b32 s67, s49\n"
    "s_mov_b32 s49, s62\n"
    "s_mov_b32 s62, s65\n"
    "s_mov_b32 s65, s68\n"
    "s_mov_b32 s68, s49\n"
    "s_branch LS0_%=_top\n"
    "LS0_%=_late:\n"
    "s_min_u32 s52, s52, 64\n"
    "v_cmp_gt_u32 vcc, s52, v171\n"
    "s_cbranch_vccz LS0_%=_f_ok\n"
    "s_mov_b32 s50, 0\n"
    "LS0_%=_f_poll:\n"
    "buffer_load_dword v171, v170, s[40:43], 0 offen sc1\n"
    "s_add_u32 s50, s50, 1\n"
    "s_cmp_gt_u32 s50, 30000\n"
    "s_cbranch_scc1 LS_exit_%=\n"
    "s_waitcnt vmcnt(0)\n"
    "v_cmp_gt_u32 vcc, s52, v171\n"
    "s_cbranch_vccnz LS0_%=_f_poll\n"
    "LS0_%=_f_ok:\n"
    "s_add_u32 s96, s96, 1\n"
    "s_sub_u32 s49, s96, 1\n"
    "s_and_b32 s99, s49, 1\n"
    "s_lshl_b32 s99, s99, 15\n"
    "s_add_u32 s99, s99, 0\n"
    "s_min_u32 s49, s49, 63\n"
    "s_lshl_b32 s49, s49, 4\n"
    "s_add_u32 s49, s49, s72\n"
    "s_mul_i32 s49, s49, 28672\n"
    "s_add_u32 s97, s49, 0xed3000\n"
    "s_add_u32 m0, s99, 0\n"
    "s_add_u32 s98, s97, 0\n"
    "buffer_load_dwordx4 v0, s[40:43], s98 offen offset:0 sc1 lds\n"
    "buffer_load_dwordx4 v0, s[40:43], s98 offen offset:1024 sc1 lds\n"
    "buffer_load_dwordx4 v0, s[40:43], s98 offen offset:2048 sc1 lds\n"
    "buffer_load_dwordx4 v0, s[40:43], s98 offen offset:3072 sc1 lds\n"
    "s_add_u32 m0, s99, 4096\n"
    "s_add_u32 s98, s97, 4096\n"
    "buffer_load_dwordx4 v0, s[40:43], s98 offen offset:0 sc1 lds\n"
    "buffer_load_dwordx4 v0, s[40:43], s98 offen offset:1024 sc1 lds\n"
    "s_waitcnt vmcnt(0)\n"
    "s_add_u32 s52, s96, 1\n"
    "s_branch LS0_%=_early\n"
    "LS0_%=_nan:\n"
    "v_mov_b32 v2, 0\n"
    "v_max3_u32 v2, v16, v17, v2\n"
    "v_max3_u32 v2, v18, v19, v2\n"
    "v_max3_u32 v2, v20, v21, v2\n"
    "v_max3_u32 v2, v22, v23, v2\n"
    "v_max3_u32 v2, v24, v25, v2\n"
    "v_max3_u32 v2, v26, v27, v2\n"
    "v_max3_u32 v2, v28, v29, v2\n"
    "v_max3_u32 v2, v30, v31, v2\n"
    "v_max3_u32 v2, v32, v33, v2\n"
    "v_max3_u32 v2, v34, v35, v2\n"
    "v_max3_u32 v2, v36, v37, v2\n"
    "v_max3_u32 v2, v38, v39, v2\n"
    "v_max3_u32 v2, v40, v41, v2\n"
    "v_max3_u32 v2, v42, v43, v2\n"
    "v_cmp_eq_u32 vcc, -1, v2\n"
    "s_cbranch_vccz LS0_%=_good\n"
    "LS0_%=_again:\n"
    "s_nop 7\n"
    "buffer_load_dwordx4 v[16:19], v0, s[40:43], s69 offen offset:0 sc1\n"
    "buffer_load_dwordx4 v[20:23], v0, s[40:43], s69 offen offset:1024 sc1\n"
    "buffer_load_dwordx4 v[24:27], v0, s[40:43], s69 offen offset:2048 sc1\n"
    "buffer_load_dwordx4 v[28:31], v0, s[40:43], s69 offen offset:3072 sc1\n"
    "buffer_load_dwordx4 v[32:35], v0, s[40:43], s79 offen offset:0 sc1\n"
    "buffer_load_dwordx4 v[36:39], v0, s[40:43], s79 offen offset:1024 sc1\n"
    "buffer_load_dwordx4 v[40:43], v0, s[40:43], s79 offen offset:2048 sc1\n"
    "s_add_u32 s50, s50, 1\n"
    "s_add_u32 s75, s75, 1\n"
    "s_cmp_gt_u32 s50, 30000\n"
    "s_cbranch_scc1 LS_exit_%=\n"
    "s_waitcnt lgkmcnt(0)\n"
    "ds_read_b128 v[76:79], v5 offset:0\n"
    "ds_read_b128 v[80:83], v5 offset:1024\n"
    "ds_read_b128 v[84:87], v5 offset:14336\n"
    "ds_read_b128 v[88:91], v5 offset:15360\n"
    "ds_read_b128 v[92:95], v5 offset:2048\n"
    "ds_read_b128 v[96:99], v5 offset:3072\n"
    "ds_read_b128 v[100:103], v5 offset:16384\n"
    "ds_read_b128 v[104:107], v5 offset:17408\n"
    "ds_read_b128 v[108:111], v5 offset:4096\n"
    "ds_read_b128 v[112:115], v5 offset:5120\n"
    "ds_read_b128 v[116:119], v5 offset:18432\n"
    "ds_read_b128 v[120:123], v5 offset:19456\n"
    "v_mov_b32 v44, 0\n"
    "v_mov_b32 v45, 0\n"
    "v_mov_b32 v46, 0\n"
    "v_mov_b32 v47, 0\n"
    "v_mov_b32 v48, 0\n"
    "v_mov_b32 v49, 0\n"
    "v_mov_b32 v50, 0\n"
    "v_mov_b32 v51, 0\n"
    "v_mov_b32 v52, 0\n"
    "v_mov_b32 v53, 0\n"
    "v_mov_b32 v54, 0\n"
    "v_mov_b32 v55, 0\n"
    "v_mov_b32 v56, 0\n"
    "v_mov_b32 v57, 0\n"
    "v_mov_b32 v58, 0\n"
    "v_mov_b32 v59, 0\n"
    "v_mov_b32 v60, 0\n"
    "v_mov_b32 v61, 0\n"
    "v_mov_b32 v62, 0\n"
    "v_mov_b32 v63, 0\n"
    "v_mov_b32 v64, 0\n"
    "v_mov_b32 v65, 0\n"
    "v_mov_b32 v66, 0\n"
    "v_mov_b32 v67, 0\n"
    "v_mov_b32 v68, 0\n"
    "v_mov_b32 v69, 0\n"
    "v_mov_b32 v70, 0\n"
    "v_mov_b32 v71, 0\n"
    "v_mov_b32 v72, 0\n"
    "v_mov_b32 v73, 0\n"
    "v_mov_b32 v74, 0\n"
    "v_mov_b32 v75, 0\n"
    "s_waitcnt vmcnt(0)\n"
    "v_mov_b32 v2, 0\n"
    "v_max3_u32 v2, v16, v17, v2\n"
    "v_max3_u32 v2, v18, v19, v2\n"
    "v_max3_u32 v2, v20, v21, v2\n"
    "v_max3_u32 v2, v22, v23, v2\n"
    "v_max3_u32 v2, v24, v25, v2\n"
    "v_max3_u32 v2, v26, v27, v2\n"
    "v_max3_u32 v2, v28, v29, v2\n"
    "v_max3_u32 v2, v30, v31, v2\n"
    "v_max3_u32 v2, v32, v33, v2\n"
    "v_max3_u32 v2, v34, v35, v2\n"
    "v_max3_u32 v2, v36, v37, v2\n"
    "v_max3_u32 v2, v38, v39, v2\n"
    "v_max3_u32 v2, v40, v41, v2\n"
    "v_max3_u32 v2, v42, v43, v2\n"
    "v_cmp_eq_u32 vcc, -1, v2\n"
    "s_cbranch_vccnz LS0_%=_again\n"
    "s_branch LS0_%=_compute\n"
    "LS1_%=_start:\n"
    "v_and_b32 v11, 15, v10\n"
    "v_lshrrev_b32 v12, 4, v10\n"
    "v_and_b32 v2, 4, v11\n"
    "v_lshlrev_b32 v3, 5, v2\n"
    "v_and_b32 v2, 1, v11\n"
    "v_lshl_add_u32 v3, v2, 5, v3\n"
    "v_and_b32 v2, 8, v11\n"
    "v_lshlrev_b32 v13, 4, v2\n"
    "v_xor_b32 v3, v3, v13\n"
    "v_lshl_add_u32 v3, v2, 5, v3\n"
    "v_and_b32 v2, 2, v11\n"
    "v_lshl_add_u32 v3, v2, 1, v3\n"
    "v_lshl_add_u32 v3, v12, 3, v3\n"
    "v_add_u32 v3, 0x1d000, v3\n"
    "v_and_b32 v2, 2, v10\n"
    "v_cmp_eq_u32 s[70:71], 0, v2\n"
    "v_lshlrev_b32 v6, 5, v10\n"
    "v_add_u32 v6, 0x1ec00, v6\n"
    "v_mov_b32 v2, 0x1f400\n"
    "v_cndmask_b32 v6, v2, v6, s[70:71]\n"
    "v_and_b32 v13, 3, v10\n"
    "v_bfe_u32 v14, v10, 2, 2\n"
    "v_lshlrev_b32 v7, 5, v14\n"
    "v_lshl_add_u32 v7, v13, 3, v7\n"
    "v_and_b32 v2, 1, v12\n"
    "v_lshlrev_b32 v2, 7, v2\n"
    "v_xor_b32 v7, v7, v2\n"
    "v_lshl_add_u32 v7, v12, 8, v7\n"
    "v_xor_b32 v8, 0x80, v7\n"
    "v_add_u32 v7, 0x1d000, v7\n"
    "v_add_u32 v8, 0x1d000, v8\n"
    "v_mov_b32 v2, 0x1f400\n"
    "v_lshl_add_u32 v2, v11, 2, v2\n"
    "v_mov_b32 v151, 0xf149f2ca\n"
    "ds_write_b32 v2, v151\n"
    "v_mov_b32 v146, -1\n"
    "v_mov_b32 v147, -1\n"
    "v_mov_b32 v148, -1\n"
    "v_mov_b32 v149, -1\n"
    "v_mov_b32 v145, 0x71800000\n"
    "v_min_u32 v4, 3, v10\n"
    "v_lshlrev_b32 v4, 2, v4\n"
    "v_add_u32 v4, 0x1f440, v4\n"
    "v_min_u32 v172, 2, v10\n"
    "v_lshlrev_b32 v172, 2, v172\n"
    "v_add_u32 v172, 0x1f440, v172\n"
    "v_lshrrev_b32 v2, 1, v12\n"
    "v_lshlrev_b32 v13, 8, v2\n"
    "v_and_b32 v2, 1, v12\n"
    "v_lshl_add_u32 v13, v2, 5, v13\n"
    "v_lshl_add_u32 v13, v11, 4, v13\n"
    "v_lshrrev_b32 v2, 2, v11\n"
    "v_lshlrev_b32 v14, 5, v2\n"
    "v_and_b32 v2, 1, v11\n"
    "v_lshl_add_u32 v14, v2, 4, v14\n"
    "v_lshl_add_u32 v14, v12, 2, v14\n"
    "s_lshl_b32 s49, s72, 7\n"
    "v_mov_b32 v9, 0x7fffff00\n"
    "v_mov_b32 v2, s49\n"
    "v_cmp_eq_u32 vcc, 0, v10\n"
    "s_nop 1\n"
    "v_cndmask_b32 v9, v9, v2, vcc\n"
    "s_cmp_eq_u32 s72, 15\n"
    "s_cselect_b32 s49, 28, 0\n"
    "v_mov_b32 v5, s49\n"
    "v_mov_b32 v2, 0x7fffff00\n"
    "v_cndmask_b32 v13, v2, v13, s[70:71]\n"
    "v_cndmask_b32 v14, v2, v14, s[70:71]\n"
    "s_lshr_b32 s49, s72, 2\n"
    "s_mul_i32 s49, s49, 896\n"
    "s_add_u32 s49, s49, 0x0\n"
    "v_min_u32 v170, 6, v10\n"
    "v_lshlrev_b32 v170, 7, v170\n"
    "v_add_u32 v170, s49, v170\n"
    "s_mul_i32 s51, s72, 401408\n"
    "s_add_u32 s51, s51, 0xd3000\n"
    "s_add_u32 s49, s51, 358400\n"
    "s_mov_b32 m0, 0x12800\n"
    "s_nop 0\n"
    "buffer_load_dwordx4 v0, s[40:43], s49 offen offset:0 lds\n"
    "buffer_load_dwordx4 v0, s[40:43], s49 offen offset:1024 lds\n"
    "buffer_load_dwordx4 v0, s[40:43], s49 offen offset:2048 lds\n"
    "buffer_load_dwordx4 v0, s[40:43], s49 offen offset:3072 lds\n"
    "s_mov_b32 m0, 0x13800\n"
    "s_add_u32 s49, s49, 4096\n"
    "buffer_load_dwordx4 v0, s[40:43], s49 offen offset:0 lds\n"
    "buffer_load_dwordx4 v0, s[40:43], s49 offen offset:1024 lds\n"
    "buffer_load_dwordx4 v0, s[40:43], s49 offen offset:2048 lds\n"
    "buffer_load_dwordx4 v0, s[40:43], s49 offen offset:3072 lds\n"
    "s_mov_b32 m0, 0x14800\n"
    "s_add_u32 s49, s49, 4096\n"
    "buffer_load_dwordx4 v0, s[40:43], s49 offen offset:0 lds\n"
    "buffer_load_dwordx4 v0, s[40:43], s49 offen offset:1024 lds\n"
    "buffer_load_dwordx4 v0, s[40:43], s49 offen offset:2048 lds\n"
    "buffer_load_dwordx4 v0, s[40:43], s49 offen offset:3072 lds\n"
    "s_mov_b32 m0, 0x15800\n"
    "s_add_u32 s49, s49, 4096\n"
    "buffer_load_dwordx4 v0, s[40:43], s49 offen offset:0 lds\n"
    "buffer_load_dwordx4 v0, s[40:43], s49 offen offset:1024 lds\n"
    "s_add_u32 s49, s51, 86016\n"
    "buffer_load_dwordx4 a[0:3], v0, s[40:43], s49 offen offset:0\n"
    "buffer_load_dwordx4 a[4:7], v0, s[40:43], s49 offen offset:1024\n"
    "buffer_load_dwordx4 a[8:11], v0, s[40:43], s49 offen offset:2048\n"
    "buffer_load_dwordx4 a[12:15], v0, s[40:43], s49 offen offset:3072\n"
    "s_add_u32 s49, s49, 4096\n"
    "buffer_load_dwordx4 a[16:19], v0, s[40:43], s49 offen offset:0\n"
    "buffer_load_dwordx4 a[20:23], v0, s[40:43], s49 offen offset:1024\n"
    "buffer_load_dwordx4 a[24:27], v0, s[40:43], s49 offen offset:2048\n"
    "buffer_load_dwordx4 a[28:31], v0, s[40:43], s49 offen offset:3072\n"
    "s_add_u32 s49, s49, 4096\n"
    "buffer_load_dwordx4 a[32:35], v0, s[40:43], s49 offen offset:0\n"
    "buffer_load_dwordx4 a[36:39], v0, s[40:43], s49 offen offset:1024\n"
    "buffer_load_dwordx4 a[40:43], v0, s[40:43], s49 offen offset:2048\n"
    "buffer_load_dwordx4 a[44:47], v0, s[40:43], s49 offen offset:3072\n"
    "s_add_u32 s49, s49, 4096\n"
    "buffer_load_dwordx4 a[48:51], v0, s[40:43], s49 offen offset:0\n"
    "buffer_load_dwordx4 a[52:55], v0, s[40:43], s49 offen offset:1024\n"
    "buffer_load_dwordx4 a[56:59], v0, s[40:43], s49 offen offset:2048\n"
    "buffer_load_dwordx4 a[60:63], v0, s[40:43], s49 offen offset:3072\n"
    "s_add_u32 s49, s49, 4096\n"
    "buffer_load_dwordx4 a[64:67], v0, s[40:43], s49 offen offset:0\n"
    "buffer_load_dwordx4 a[68:71], v0, s[40:43], s49 offen offset:1024\n"
    "buffer_load_dwordx4 a[72:75], v0, s[40:43], s49 offen offset:2048\n"
    "buffer_load_dwordx4 a[76:79], v0, s[40:43], s49 offen offset:3072\n"
    "s_add_u32 s49, s49, 4096\n"
    "buffer_load_dwordx4 a[80:83], v0, s[40:43], s49 offen offset:0\n"
    "buffer_load_dwordx4 a[84:87], v0, s[40:43], s49 offen offset:1024\n"
    "buffer_load_dwordx4 a[88:91], v0, s[40:43], s49 offen offset:2048\n"
    "buffer_load_dwordx4 a[92:95], v0, s[40:43], s49 offen offset:3072\n"
    "s_add_u32 s49, s49, 4096\n"
    "buffer_load_dwordx4 a[96:99], v0, s[40:43], s49 offen offset:0\n"
    "buffer_load_dwordx4 a[100:103], v0, s[40:43], s49 offen offset:1024\n"
    "buffer_load_dwordx4 a[104:107], v0, s[40:43], s49 offen offset:2048\n"
    "buffer_load_dwordx4 a[108:111], v0, s[40:43], s49 offen offset:3072\n"
    "s_add_u32 s49, s49, 4096\n"
    "buffer_load_dwordx4 a[112:115], v0, s[40:43], s49 offen offset:0\n"
    "buffer_load_dwordx4 a[116:119], v0, s[40:43], s49 offen offset:1024\n"
    "buffer_load_dwordx4 a[120:123], v0, s[40:43], s49 offen offset:2048\n"
    "buffer_load_dwordx4 a[124:127], v0, s[40:43], s49 offen offset:3072\n"
    "s_add_u32 s49, s49, 4096\n"
    "buffer_load_dwordx4 a[128:131], v0, s[40:43], s49 offen offset:0\n"
    "buffer_load_dwordx4 a[132:135], v0, s[40:43], s49 offen offset:1024\n"
    "buffer_load_dwordx4 a[136:139], v0, s[40:43], s49 offen offset:2048\n"
    "buffer_load_dwordx4 a[140:143], v0, s[40:43], s49 offen offset:3072\n"
    "s_add_u32 s49, s49, 4096\n"
    "buffer_load_dwordx4 a[144:147], v0, s[40:43], s49 offen offset:0\n"
    "buffer_load_dwordx4 a[148:151], v0, s[40:43], s49 offen offset:1024\n"
    "buffer_load_dwordx4 a[152:155], v0, s[40:43], s49 offen offset:2048\n"
    "buffer_load_dwordx4 a[156:159], v0, s[40:43], s49 offen offset:3072\n"
    "s_add_u32 s49, s49, 4096\n"
    "s_waitcnt vmcnt(40)\n"
    "buffer_load_dwordx4 a[160:163], v0, s[40:43], s49 offen offset:0\n"
    "buffer_load_dwordx4 a[164:167], v0, s[40:43], s49 offen offset:1024\n"
    "buffer_load_dwordx4 a[168:171], v0, s[40:43], s49 offen offset:2048\n"
    "buffer_load_dwordx4 a[172:175], v0, s[40:43], s49 offen offset:3072\n"
    "s_add_u32 s49, s49, 4096\n"
    "buffer_load_dwordx4 a[176:179], v0, s[40:43], s49 offen offset:0\n"
    "buffer_load_dwordx4 a[180:183], v0, s[40:43], s49 offen offset:1024\n"
    "buffer_load_dwordx4 a[184:187], v0, s[40:43], s49 offen offset:2048\n"
    "buffer_load_dwordx4 a[188:191], v0, s[40:43], s49 offen offset:3072\n"
    "s_add_u32 s49, s49, 4096\n"
    "s_waitcnt vmcnt(40)\n"
    "buffer_load_dwordx4 a[192:195], v0, s[40:43], s49 offen offset:0\n"
    "buffer_load_dwordx4 a[196:199], v0, s[40:43], s49 offen offset:1024\n"
    "buffer_load_dwordx4 a[200:203], v0, s[40:43], s49 offen offset:2048\n"
    "buffer_load_dwordx4 a[204:207], v0, s[40:43], s49 offen offset:3072\n"
    "s_add_u32 s49, s49, 4096\n"
    "buffer_load_dwordx4 a[208:211], v0, s[40:43], s49 offen offset:0\n"
    "buffer_load_dwordx4 a[212:215], v0, s[40:43], s49 offen offset:1024\n"
    "buffer_load_dwordx4 a[216:219], v0, s[40:43], s49 offen offset:2048\n"
    "buffer_load_dwordx4 a[220:223], v0, s[40:43], s49 offen offset:3072\n"
    "s_add_u32 s49, s49, 4096\n"
    "s_waitcnt vmcnt(40)\n"
    "buffer_load_dwordx4 a[224:227], v0, s[40:43], s49 offen offset:0\n"
    "buffer_load_dwordx4 a[228:231], v0, s[40:43], s49 offen offset:1024\n"
    "buffer_load_dwordx4 a[232:235], v0, s[40:43], s49 offen offset:2048\n"
    "buffer_load_dwordx4 a[236:239], v0, s[40:43], s49 offen offset:3072\n"
    "s_add_u32 s49, s49, 4096\n"
    "buffer_load_dwordx4 a[240:243], v0, s[40:43], s49 offen offset:0\n"
    "buffer_load_dwordx4 a[244:247], v0, s[40:43], s49 offen offset:1024\n"
    "buffer_load_dwordx4 a[248:251], v0, s[40:43], s49 offen offset:2048\n"
    "buffer_load_dwordx4 a[252:255], v0, s[40:43], s49 offen offset:3072\n"
    "s_add_u32 s49, s49, 4096\n"
    "s_waitcnt vmcnt(40)\n"
    "buffer_load_dwordx4 v[174:177], v0, s[40:43], s49 offen offset:0\n"
    "buffer_load_dwordx4 v[178:181], v0, s[40:43], s49 offen offset:1024\n"
    "buffer_load_dwordx4 v[182:185], v0, s[40:43], s49 offen offset:2048\n"
    "buffer_load_dwordx4 v[186:189], v0, s[40:43], s49 offen offset:3072\n"
    "s_add_u32 s49, s49, 4096\n"
    "buffer_load_dwordx4 v[190:193], v0, s[40:43], s49 offen offset:0\n"
    "buffer_load_dwordx4 v[194:197], v0, s[40:43], s49 offen offset:1024\n"
    "buffer_load_dwordx4 v[198:201], v0, s[40:43], s49 offen offset:2048\n"
    "buffer_load_dwordx4 v[202:205], v0, s[40:43], s49 offen offset:3072\n"
    "s_add_u32 s49, s49, 4096\n"
    "s_waitcnt vmcnt(40)\n"
    "buffer_load_dwordx4 v[206:209], v0, s[40:43], s49 offen offset:0\n"
    "buffer_load_dwordx4 v[210:213], v0, s[40:43], s49 offen offset:1024\n"
    "buffer_load_dwordx4 v[214:217], v0, s[40:43], s49 offen offset:2048\n"
    "buffer_load_dwordx4 v[218:221], v0, s[40:43], s49 offen offset:3072\n"
    "s_add_u32 s49, s49, 4096\n"
    "buffer_load_dwordx4 v[222:225], v0, s[40:43], s49 offen offset:0\n"
    "buffer_load_dwordx4 v[226:229], v0, s[40:43], s49 offen offset:1024\n"
    "buffer_load_dwordx4 v[230:233], v0, s[40:43], s49 offen offset:2048\n"
    "buffer_load_dwordx4 v[234:237], v0, s[40:43], s49 offen offset:3072\n"
    "s_add_u32 s49, s49, 4096\n"
    "s_waitcnt vmcnt(40)\n"
    "buffer_load_dwordx4 v[238:241], v0, s[40:43], s49 offen offset:0\n"
    "buffer_load_dwordx4 v[242:245], v0, s[40:43], s49 offen offset:1024\n"
    "buffer_load_dwordx4 v[246:249], v0, s[40:43], s49 offen offset:2048\n"
    "buffer_load_dwordx4 v[250:253], v0, s[40:43], s49 offen offset:3072\n"
    "v_min_u32 v168, 4, v10\n"
    "v_lshlrev_b32 v168, 2, v168\n"
    "v_add_u32 v168, 0xf800, v168\n"
    "s_mov_b32 s50, 0\n"
    "LS1_%=_pt:\n"
    "buffer_load_dword v169, v168, s[40:43], 0 offen sc1\n"
    "s_add_u32 s50, s50, 1\n"
    "s_cmp_gt_u32 s50, 30000\n"
    "s_cbranch_scc1 LS_exit_%=\n"
    "s_waitcnt vmcnt(0)\n"
    "v_cmp_eq_u32 vcc, -1, v169\n"
    "s_cbranch_vccnz LS1_%=_pt\n"
    "v_readlane_b32 s49, v169, 0\n"
    "s_nop 1\n"
    "s_bfe_u32 s39, s49, 0x70000\n"
    "s_lshl_b32 s39, s39, 12\n"
    "s_add_u32 s39, s39, %7\n"
    "s_bfe_u32 s54, s49, 0x70008\n"
    "s_lshl_b32 s54, s54, 12\n"
    "s_add_u32 s54, s54, %7\n"
    "s_bfe_u32 s55, s49, 0x70010\n"
    "s_lshl_b32 s55, s55, 12\n"
    "s_add_u32 s55, s55, %7\n"
    "s_bfe_u32 s56, s49, 0x70018\n"
    "s_lshl_b32 s56, s56, 12\n"
    "s_add_u32 s56, s56, %7\n"
    "v_readlane_b32 s49, v169, 1\n"
    "s_nop 1\n"
    "s_bfe_u32 s57, s49, 0x70000\n"
    "s_lshl_b32 s57, s57, 12\n"
    "s_add_u32 s57, s57, %7\n"
    "s_bfe_u32 s58, s49, 0x70008\n"
    "s_lshl_b32 s58, s58, 12\n"
    "s_add_u32 s58, s58, %7\n"
    "s_bfe_u32 s59, s49, 0x70010\n"
    "s_lshl_b32 s59, s59, 12\n"
    "s_add_u32 s59, s59, %7\n"
    "s_bfe_u32 s60, s49, 0x70018\n"
    "s_lshl_b32 s60, s60, 12\n"
    "s_add_u32 s60, s60, %7\n"
    "v_readlane_b32 s49, v169, 2\n"
    "s_nop 1\n"
    "s_bfe_u32 s61, s49, 0x70000\n"
    "s_lshl_b32 s61, s61, 12\n"
    "s_add_u32 s61, s61, %7\n"
    "s_bfe_u32 s62, s49, 0x70008\n"
    "s_lshl_b32 s62, s62, 12\n"
    "s_add_u32 s62, s62, %7\n"
    "s_bfe_u32 s63, s49, 0x70010\n"
    "s_lshl_b32 s63, s63, 12\n"
    "s_add_u32 s63, s63, %7\n"
    "s_bfe_u32 s64, s49, 0x70018\n"
    "s_lshl_b32 s64, s64, 12\n"
    "s_add_u32 s64, s64, %7\n"
    "v_readlane_b32 s49, v169, 3\n"
    "s_nop 1\n"
    "s_bfe_u32 s65, s49, 0x70000\n"
    "s_lshl_b32 s65, s65, 12\n"
    "s_add_u32 s65, s65, %7\n"
    "s_bfe_u32 s66, s49, 0x70008\n"
    "s_lshl_b32 s66, s66, 12\n"
    "s_add_u32 s66, s66, %7\n"
    "s_bfe_u32 s67, s49, 0x70010\n"
    "s_lshl_b32 s67, s67, 12\n"
    "s_add_u32 s67, s67, %7\n"
    "s_bfe_u32 s68, s49, 0x70018\n"
    "s_lshl_b32 s68, s68, 12\n"
    "s_add_u32 s68, s68, %7\n"
    "v_readlane_b32 s49, v169, 4\n"
    "s_nop 1\n"
    "s_bfe_u32 s77, s49, 0x70000\n"
    "s_lshl_b32 s77, s77, 12\n"
    "s_add_u32 s77, s77, %7\n"
    "s_bfe_u32 s78, s49, 0x70008\n"
    "s_lshl_b32 s78, s78, 12\n"
    "s_add_u32 s78, s78, %7\n"
    "s_bfe_u32 s48, s49, 0x70010\n"
    "s_lshl_b32 s48, s48, 12\n"
    "s_add_u32 s48, s48, %7\n"
    "v_cmp_gt_u32 s[46:47], v5, v10\n"
    "v_and_b32 v5, 15, v10\n"
    "v_lshlrev_b32 v5, 8, v5\n"
    "v_mov_b32 v2, s39\n"
    "v_mov_b32 v168, s48\n"
    "v_cmp_gt_u32 vcc, 16, v10\n"
    "s_nop 1\n"
    "v_cndmask_b32 v2, v168, v2, vcc\n"
    "v_add_u32 v5, v5, v2\n"
    "v_mov_b32 v2, 0x7fffff00\n"
    "v_cndmask_b32 v5, v2, v5, s[46:47]\n"
    "s_and_b32 s49, s72, 15\n"
    "s_lshl_b32 s49, s49, 8\n"
    "s_add_u32 s49, s49, s78\n"
    "s_add_u32 s34, %0, s49\n"
    "s_addc_u32 s35, %1, 0\n"
    "s_mov_b32 s28, -2\n"
    "s_mov_b32 s29, -2\n"
    "s_mov_b32 s30, -2\n"
    "s_mov_b32 s31, -2\n"
    ".p2align 6\n"
    "s_nop 0\n"
    "LS1_%=_top:\n"
    "s_mov_b32 s50, 0\n"
    "s_cmp_lt_u32 s72, 8\n"
    "s_cselect_b32 s36, s60, s61\n"
    "s_and_b32 s53, s72, 7\n"
    "s_lshl_b32 s53, s53, 9\n"
    "s_add_u32 s36, s36, s53\n"
    "s_lshl_b32 s53, s72, 7\n"
    "s_add_u32 s37, s53, s62\n"
    "s_cmp_lt_u32 s72, 8\n"
    "s_cselect_b32 s51, s66, s67\n"
    "s_and_b32 s53, s72, 7\n"
    "s_lshl_b32 s53, s53, 9\n"
    "s_add_u32 s51, s51, s53\n"
    "s_lshl_b32 s53, s72, 7\n"
    "s_add_u32 s52, s53, s68\n"
    "v_mov_b32 v44, 0\n"
    "v_mov_b32 v45, 0\n"
    "v_mov_b32 v46, 0\n"
    "v_mov_b32 v47, 0\n"
    "v_mov_b32 v48, 0\n"
    "v_mov_b32 v49, 0\n"
    "v_mov_b32 v50, 0\n"
    "v_mov_b32 v51, 0\n"
    "v_mov_b32 v52, 0\n"
    "v_mov_b32 v53, 0\n"
    "v_mov_b32 v54, 0\n"
    "v_mov_b32 v55, 0\n"
    "v_mov_b32 v56, 0\n"
    "v_mov_b32 v57, 0\n"
    "v_mov_b32 v58, 0\n"
    "v_mov_b32 v59, 0\n"
    "v_mov_b32 v60, 0\n"
    "v_mov_b32 v61, 0\n"
    "v_mov_b32 v62, 0\n"
    "v_mov_b32 v63, 0\n"
    "v_mov_b32 v64, 0\n"
    "v_mov_b32 v65, 0\n"
    "v_mov_b32 v66, 0\n"
    "v_mov_b32 v67, 0\n"
    "v_mov_b32 v15, s44\n"
    "s_sub_u32 s78, s44, 1\n"
    "s_cmp_lg_u32 s44, 0\n"
    "s_cbranch_scc1 LS1_%=_warm\n"
    "s_waitcnt vmcnt(0) lgkmcnt(0)\n"
    "LS1_%=_warm:\n"
    "s_mov_b32 s50, 0\n"
    "s_mov_b32 s48, s73\n"
    "s_load_dword s28, s[34:35], 0x0 glc\n"
    "s_nop 7\n"
    "s_nop 7\n"
    "s_nop 7\n"
    "s_load_dword s29, s[34:35], 0x0 glc\n"
    "s_nop 7\n"
    "s_nop 7\n"
    "s_nop 7\n"
    "s_load_dword s30, s[34:35], 0x0 glc\n"
    "s_nop 7\n"
    "s_nop 7\n"
    "s_nop 7\n"
    "LS1_%=_tk:\n"
    "s_load_dword s31, s[34:35], 0x0 glc\n"
    "s_waitcnt lgkmcnt(3)\n"
    "s_cmp_ge_i32 s28, s78\n"
    "s_cbranch_scc1 LS1_%=_seen\n"
    "s_load_dword s28, s[34:35], 0x0 glc\n"
    "s_waitcnt lgkmcnt(3)\n"
    "s_cmp_ge_i32 s29, s78\n"
    "s_cbranch_scc1 LS1_%=_seen\n"
    "s_load_dword s29, s[34:35], 0x0 glc\n"
    "s_waitcnt lgkmcnt(3)\n"
    "s_cmp_ge_i32 s30, s78\n"
    "s_cbranch_scc1 LS1_%=_seen\n"
    "s_load_dword s30, s[34:35], 0x0 glc\n"
    "s_waitcnt lgkmcnt(3)\n"
    "s_cmp_ge_i32 s31, s78\n"
    "s_cbranch_scc1 LS1_%=_seen\n"
    "s_add_u32 s50, s50, 1\n"
    "s_cmp_lt_u32 s50, 2000\n"
    "s_cbranch_scc1 LS1_%=_tk\n"
    "LS1_%=_seen:\n"
    "LS1_%=_dl:\n"
    "s_sub_u32 s48, s48, 1\n"
    "s_cbranch_scc0 LS1_%=_dl\n"
    "s_mov_b32 s50, 0\n"
    "s_barrier\n"
    "LS1_%=_issue:\n"
    "buffer_load_dwordx4 v[16:19], v0, s[40:43], s69 offen offset:0 sc1\n"
    "s_nop 10\n"
    "buffer_load_dwordx4 v[20:23], v0, s[40:43], s69 offen offset:1024 sc1\n"
    "s_nop 10\n"
    "buffer_load_dwordx4 v[24:27], v0, s[40:43], s69 offen offset:2048 sc1\n"
    "s_nop 10\n"
    "buffer_load_dwordx4 v[28:31], v0, s[40:43], s69 offen offset:3072 sc1\n"
    "s_nop 10\n"
    "buffer_load_dwordx4 v[32:35], v0, s[40:43], s79 offen offset:0 sc1\n"
    "s_nop 10\n"
    "buffer_load_dwordx4 v[36:39], v0, s[40:43], s79 offen offset:1024 sc1\n"
    "s_nop 10\n"
    "buffer_load_dwordx4 v[40:43], v0, s[40:43], s79 offen offset:2048 sc1\n"
    "LS1_%=_compute:\n"
    "s_waitcnt vmcnt(6)\n"
    "v_smfmac_f32_16x16x64_bf16 v[44:47], v[16:19], a[0:7], v1\n"
    "v_smfmac_f32_16x16x64_bf16 v[48:51], v[16:19], a[56:63], v1\n"
    "v_smfmac_f32_16x16x64_bf16 v[52:55], v[16:19], a[112:119], v1\n"
    "v_smfmac_f32_16x16x64_bf16 v[56:59], v[16:19], a[168:175], v1\n"
    "v_smfmac_f32_16x16x64_bf16 v[60:63], v[16:19], a[224:231], v1\n"
    "v_smfmac_f32_16x16x64_bf16 v[64:67], v[16:19], v[198:205], v1\n"
    "s_waitcnt vmcnt(5)\n"
    "v_smfmac_f32_16x16x64_bf16 v[44:47], v[20:23], a[8:15], v1\n"
    "v_smfmac_f32_16x16x64_bf16 v[48:51], v[20:23], a[64:71], v1\n"
    "v_smfmac_f32_16x16x64_bf16 v[52:55], v[20:23], a[120:127], v1\n"
    "v_smfmac_f32_16x16x64_bf16 v[56:59], v[20:23], a[176:183], v1\n"
    "v_smfmac_f32_16x16x64_bf16 v[60:63], v[20:23], a[232:239], v1\n"
    "v_smfmac_f32_16x16x64_bf16 v[64:67], v[20:23], v[206:213], v1\n"
    "s_waitcnt vmcnt(4)\n"
    "v_smfmac_f32_16x16x64_bf16 v[44:47], v[24:27], a[16:23], v1\n"
    "v_smfmac_f32_16x16x64_bf16 v[48:51], v[24:27], a[72:79], v1\n"
    "v_smfmac_f32_16x16x64_bf16 v[52:55], v[24:27], a[128:135], v1\n"
    "v_smfmac_f32_16x16x64_bf16 v[56:59], v[24:27], a[184:191], v1\n"
    "v_smfmac_f32_16x16x64_bf16 v[60:63], v[24:27], a[240:247], v1\n"
    "v_smfmac_f32_16x16x64_bf16 v[64:67], v[24:27], v[214:221], v1\n"
    "s_waitcnt vmcnt(3)\n"
    "v_smfmac_f32_16x16x64_bf16 v[44:47], v[28:31], a[24:31], v1\n"
    "v_smfmac_f32_16x16x64_bf16 v[48:51], v[28:31], a[80:87], v1\n"
    "v_smfmac_f32_16x16x64_bf16 v[52:55], v[28:31], a[136:143], v1\n"
    "v_smfmac_f32_16x16x64_bf16 v[56:59], v[28:31], a[192:199], v1\n"
    "v_smfmac_f32_16x16x64_bf16 v[60:63], v[28:31], a[248:255], v1\n"
    "v_smfmac_f32_16x16x64_bf16 v[64:67], v[28:31], v[222:229], v1\n"
    "s_waitcnt vmcnt(2)\n"
    "v_smfmac_f32_16x16x64_bf16 v[44:47], v[32:35], a[32:39], v1\n"
    "v_smfmac_f32_16x16x64_bf16 v[48:51], v[32:35], a[88:95], v1\n"
    "v_smfmac_f32_16x16x64_bf16 v[52:55], v[32:35], a[144:151], v1\n"
    "v_smfmac_f32_16x16x64_bf16 v[56:59], v[32:35], a[200:207], v1\n"
    "v_smfmac_f32_16x16x64_bf16 v[60:63], v[32:35], v[174:181], v1\n"
    "v_smfmac_f32_16x16x64_bf16 v[64:67], v[32:35], v[230:237], v1\n"
    "s_waitcnt vmcnt(1)\n"
    "v_smfmac_f32_16x16x64_bf16 v[44:47], v[36:39], a[40:47], v1\n"
    "s_waitcnt vmcnt(0)\n"
    "v_smfmac_f32_16x16x64_bf16 v[44:47], v[40:43], a[48:55], v1\n"
    "v_smfmac_f32_16x16x64_bf16 v[48:51], v[36:39], a[96:103], v1\n"
    "v_smfmac_f32_16x16x64_bf16 v[48:51], v[40:43], a[104:111], v1\n"
    "v_add_f32 v44, v44, v46\n"
    "v_smfmac_f32_16x16x64_bf16 v[52:55], v[36:39], a[152:159], v1\n"
    "v_add_f32 v45, v45, v47\n"
    "v_smfmac_f32_16x16x64_bf16 v[52:55], v[40:43], a[160:167], v1\n"
    "v_cmp_u_f32 vcc, v44, v45\n"
    "v_smfmac_f32_16x16x64_bf16 v[56:59], v[36:39], a[208:215], v1\n"
    "s_cbranch_vccnz LS1_%=_nan\n"
    "LS1_%=_good:\n"
    "v_smfmac_f32_16x16x64_bf16 v[56:59], v[40:43], a[216:223], v1\n"
    "buffer_store_dwordx4 v[146:149], v13, s[40:43], s51 offen sc1\n"
    "v_smfmac_f32_16x16x64_bf16 v[60:63], v[36:39], v[182:189], v1\n"
    "buffer_store_dword v146, v14, s[40:43], s52 offen sc1\n"
    "v_smfmac_f32_16x16x64_bf16 v[60:63], v[40:43], v[190:197], v1\n"
    "v_cvt_pk_bf16_f32 v44, v44, v45\n"
    "v_smfmac_f32_16x16x64_bf16 v[64:67], v[36:39], v[238:245], v1\n"
    "ds_write_b32 v3, v44 offset:1536\n"
    "v_smfmac_f32_16x16x64_bf16 v[64:67], v[40:43], v[246:253], v1\n"
    "v_add_f32 v48, v48, v50\n"
    "v_add_f32 v49, v49, v51\n"
    "v_cvt_pk_bf16_f32 v48, v48, v49\n"
    "v_pk_add_f32 v[52:53], v[52:53], v[54:55]\n"
    "v_cvt_pk_bf16_f32 v52, v52, v53\n"
    "v_pk_add_f32 v[56:57], v[56:57], v[58:59]\n"
    "v_cvt_pk_bf16_f32 v56, v56, v57\n"
    "v_pk_add_f32 v[60:61], v[60:61], v[62:63]\n"
    "v_cvt_pk_bf16_f32 v60, v60, v61\n"
    "v_pk_add_f32 v[64:65], v[64:65], v[66:67]\n"
    "v_cvt_pk_bf16_f32 v64, v64, v65\n"
    "ds_write_b32 v3, v48 offset:1600\n"
    "ds_write_b32 v3, v52 offset:2048\n"
    "ds_write_b32 v3, v56 offset:2112\n"
    "ds_write_b32 v3, v60 offset:2560\n"
    "ds_write_b32 v3, v64 offset:2624\n"
    "LS1_%=_sx:\n"
    "ds_read_b32 v2, v4\n"
    "ds_read_b128 v[124:127], v6\n"
    "ds_read_b128 v[128:131], v6 offset:16\n"
    "s_waitcnt lgkmcnt(2)\n"
    "v_cmp_ge_u32 vcc, s44, v2\n"
    "s_waitcnt lgkmcnt(0)\n"
    "s_cbranch_vccnz LS1_%=_sxwait\n"
    "buffer_store_dword v15, v9, s[40:43], s77 offen sc1\n"
    "v_max3_f32 v140, v124, v125, v126\n"
    "v_max3_f32 v141, v127, v128, v129\n"
    "v_max3_f32 v141, v141, v130, v131\n"
    "v_max_f32 v143, v140, v141\n"
    "v_max_f32 v140, v140, v141\n"
    "v_mov_b32 v138, 0\n"
    "v_mov_b32 v139, 0\n"
    "v_permlane16_swap_b32 v140, v143\n"
    "v_max_f32 v141, v140, v143\n"
    "v_max_f32 v140, v140, v143\n"
    "s_nop 1\n"
    "v_permlane32_swap_b32 v140, v141\n"
    "v_max_f32 v140, v140, v141\n"
    "v_pk_add_f32 v[124:125], v[124:125], v[140:141] op_sel_hi:[1,0] neg_lo:[0,1] neg_hi:[0,1]\n"
    "v_pk_add_f32 v[126:127], v[126:127], v[140:141] op_sel_hi:[1,0] neg_lo:[0,1] neg_hi:[0,1]\n"
    "v_pk_add_f32 v[128:129], v[128:129], v[140:141] op_sel_hi:[1,0] neg_lo:[0,1] neg_hi:[0,1]\n"
    "v_pk_add_f32 v[130:131], v[130:131], v[140:141] op_sel_hi:[1,0] neg_lo:[0,1] neg_hi:[0,1]\n"
    "v_cndmask_b32 v136, v151, -v140, s[70:71]\n"
    "v_mov_b32 v137, v136\n"
    "v_exp_f32 v124, v124\n"
    "v_exp_f32 v125, v125\n"
    "v_exp_f32 v126, v126\n"
    "v_exp_f32 v127, v127\n"
    "v_exp_f32 v128, v128\n"
    "v_exp_f32 v129, v129\n"
    "v_exp_f32 v130, v130\n"
    "v_exp_f32 v131, v131\n"
    "v_pk_add_f32 v[132:133], v[124:125], v[126:127]\n"
    "v_pk_add_f32 v[134:135], v[128:129], v[130:131]\n"
    "v_pk_add_f32 v[132:133], v[132:133], v[134:135]\n"
    "v_add_f32 v142, v132, v133\n"
    "buffer_store_dword v15, v5, s[40:43], 0 offen sc1\n"
    "s_waitcnt lgkmcnt(0)\n"
    "s_barrier\n"
    "ds_read_b64_tr_b16 v[76:77], v7 offset:0\n"
    "ds_read_b64_tr_b16 v[78:79], v8 offset:0\n"
    "ds_read_b64_tr_b16 v[80:81], v7 offset:1024\n"
    "ds_read_b64_tr_b16 v[82:83], v8 offset:1024\n"
    "ds_read_b64_tr_b16 v[84:85], v7 offset:2048\n"
    "ds_read_b64_tr_b16 v[86:87], v8 offset:2048\n"
    "ds_read_b64_tr_b16 v[88:89], v7 offset:3072\n"
    "ds_read_b64_tr_b16 v[90:91], v8 offset:3072\n"
    "ds_read_b64_tr_b16 v[92:93], v7 offset:4096\n"
    "ds_read_b64_tr_b16 v[94:95], v8 offset:4096\n"
    "ds_read_b64_tr_b16 v[96:97], v7 offset:5120\n"
    "ds_read_b64_tr_b16 v[98:99], v8 offset:5120\n"
    "ds_read_b64_tr_b16 v[100:101], v7 offset:6144\n"
    "ds_read_b64_tr_b16 v[102:103], v8 offset:6144\n"
    "s_waitcnt lgkmcnt(12)\n"
    "v_mfma_f32_16x16x32_bf16 v[136:139], v[76:79], v[16:19], v[136:139]\n"
    "s_waitcnt lgkmcnt(10)\n"
    "v_mfma_f32_16x16x32_bf16 v[136:139], v[80:83], v[20:23], v[136:139]\n"
    "s_waitcnt lgkmcnt(8)\n"
    "v_mfma_f32_16x16x32_bf16 v[136:139], v[84:87], v[24:27], v[136:139]\n"
    "s_waitcnt lgkmcnt(6)\n"
    "v_mfma_f32_16x16x32_bf16 v[136:139], v[88:91], v[28:31], v[136:139]\n"
    "s_waitcnt lgkmcnt(4)\n"
    "v_mfma_f32_16x16x32_bf16 v[136:139], v[92:95], v[32:35], v[136:139]\n"
    "s_waitcnt lgkmcnt(2)\n"
    "v_mfma_f32_16x16x32_bf16 v[136:139], v[96:99], v[36:39], v[136:139]\n"
    "s_waitcnt lgkmcnt(0)\n"
    "v_mfma_f32_16x16x32_bf16 v[136:139], v[100:103], v[40:43], v[136:139]\n"
    "s_nop 7\n"
    "v_add_f32_dpp v134, v138, v136 quad_perm:[2,3,0,1] row_mask:0xf bank_mask:0xf\n"
    "v_add_f32_dpp v135, v139, v137 quad_perm:[2,3,0,1] row_mask:0xf bank_mask:0xf\n"
    "v_exp_f32 v132, v134\n"
    "v_exp_f32 v133, v135\n"
    "v_add_f32 v141, v132, v142\n"
    "v_add_f32 v143, v141, v133\n"
    "v_add_f32 v144, v141, v133\n"
    "s_nop 1\n"
    "v_permlane16_swap_b32 v143, v144\n"
    "v_add_f32 v141, v143, v144\n"
    "v_add_f32 v143, v143, v144\n"
    "s_nop 1\n"
    "v_permlane32_swap_b32 v143, v141\n"
    "v_add_f32 v143, v143, v141\n"
    "v_cmp_nlt_f32 vcc, v143, v145\n"
    "v_rcp_f32 v144, v143\n"
    "s_nop 0\n"
    "v_cndmask_b32 v144, v144, v146, vcc\n"
    "v_pk_mul_f32 v[108:109], v[124:125], v[144:145] op_sel_hi:[1,0]\n"
    "v_pk_mul_f32 v[110:111], v[126:127], v[144:145] op_sel_hi:[1,0]\n"
    "v_pk_mul_f32 v[112:113], v[128:129], v[144:145] op_sel_hi:[1,0]\n"
    "v_pk_mul_f32 v[114:115], v[130:131], v[144:145] op_sel_hi:[1,0]\n"
    "v_cvt_pk_bf16_f32 v136, v108, v109\n"
    "v_cvt_pk_bf16_f32 v137, v110, v111\n"
    "v_cvt_pk_bf16_f32 v138, v112, v113\n"
    "v_cvt_pk_bf16_f32 v139, v114, v115\n"
    "v_pk_mul_f32 v[132:133], v[132:133], v[144:145] op_sel_hi:[1,0]\n"
    "v_cvt_pk_bf16_f32 v132, v132, v133\n"
    "buffer_store_dwordx4 v[136:139], v13, s[40:43], s36 offen sc1\n"
    "buffer_store_dword v132, v14, s[40:43], s37 offen sc1\n"
    "s_cbranch_vccnz LS1_%=_rescale\n"
    "LS1_%=_done:\n"
    "s_barrier\n"
    "s_cmp_eq_u32 s44, 63\n"
    "s_cbranch_scc1 LS_exit_%=\n"
    "s_add_u32 s44, s44, 1\n"
    "s_mov_b32 s49, s54\n"
    "s_mov_b32 s53, s55\n"
    "s_mov_b32 s54, s56\n"
    "s_mov_b32 s55, s57\n"
    "s_mov_b32 s56, s58\n"
    "s_mov_b32 s57, s59\n"
    "s_mov_b32 s58, s49\n"
    "s_mov_b32 s59, s53\n"
    "s_mov_b32 s69, s54\n"
    "s_mov_b32 s79, s55\n"
    "s_mov_b32 s49, s60\n"
    "s_mov_b32 s60, s63\n"
    "s_mov_b32 s63, s66\n"
    "s_mov_b32 s66, s49\n"
    "s_mov_b32 s49, s61\n"
    "s_mov_b32 s61, s64\n"
    "s_mov_b32 s64, s67\n"
    "s_mov_b32 s67, s49\n"
    "s_mov_b32 s49, s62\n"
    "s_mov_b32 s62, s65\n"
    "s_mov_b32 s65, s68\n"
    "s_mov_b32 s68, s49\n"
    "s_branch LS1_%=_top\n"
    "LS1_%=_nan:\n"
    "v_mov_b32 v2, 0\n"
    "v_max3_u32 v2, v16, v17, v2\n"
    "v_max3_u32 v2, v18, v19, v2\n"
    "v_max3_u32 v2, v20, v21, v2\n"
    "v_max3_u32 v2, v22, v23, v2\n"
    "v_max3_u32 v2, v24, v25, v2\n"
    "v_max3_u32 v2, v26, v27, v2\n"
    "v_max3_u32 v2, v28, v29, v2\n"
    "v_max3_u32 v2, v30, v31, v2\n"
    "v_max3_u32 v2, v32, v33, v2\n"
    "v_max3_u32 v2, v34, v35, v2\n"
    "v_max3_u32 v2, v36, v37, v2\n"
    "v_max3_u32 v2, v38, v39, v2\n"
    "v_max3_u32 v2, v40, v41, v2\n"
    "v_max3_u32 v2, v42, v43, v2\n"
    "v_cmp_eq_u32 vcc, -1, v2\n"
    "s_cbranch_vccz LS1_%=_good\n"
    "LS1_%=_again:\n"
    "s_nop 7\n"
    "buffer_load_dwordx4 v[16:19], v0, s[40:43], s69 offen offset:0 sc1\n"
    "buffer_load_dwordx4 v[20:23], v0, s[40:43], s69 offen offset:1024 sc1\n"
    "buffer_load_dwordx4 v[24:27], v0, s[40:43], s69 offen offset:2048 sc1\n"
    "buffer_load_dwordx4 v[28:31], v0, s[40:43], s69 offen offset:3072 sc1\n"
    "buffer_load_dwordx4 v[32:35], v0, s[40:43], s79 offen offset:0 sc1\n"
    "buffer_load_dwordx4 v[36:39], v0, s[40:43], s79 offen offset:1024 sc1\n"
    "buffer_load_dwordx4 v[40:43], v0, s[40:43], s79 offen offset:2048 sc1\n"
    "s_add_u32 s50, s50, 1\n"
    "s_add_u32 s75, s75, 1\n"
    "s_cmp_gt_u32 s50, 30000\n"
    "s_cbranch_scc1 LS_exit_%=\n"
    "s_waitcnt lgkmcnt(0)\n"
    "v_mov_b32 v44, 0\n"
    "v_mov_b32 v45, 0\n"
    "v_mov_b32 v46, 0\n"
    "v_mov_b32 v47, 0\n"
    "v_mov_b32 v48, 0\n"
    "v_mov_b32 v49, 0\n"
    "v_mov_b32 v50, 0\n"
    "v_mov_b32 v51, 0\n"
    "v_mov_b32 v52, 0\n"
    "v_mov_b32 v53, 0\n"
    "v_mov_b32 v54, 0\n"
    "v_mov_b32 v55, 0\n"
    "v_mov_b32 v56, 0\n"
    "v_mov_b32 v57, 0\n"
    "v_mov_b32 v58, 0\n"
    "v_mov_b32 v59, 0\n"
    "v_mov_b32 v60, 0\n"
    "v_mov_b32 v61, 0\n"
    "v_mov_b32 v62, 0\n"
    "v_mov_b32 v63, 0\n"
    "v_mov_b32 v64, 0\n"
    "v_mov_b32 v65, 0\n"
    "v_mov_b32 v66, 0\n"
    "v_mov_b32 v67, 0\n"
    "s_waitcnt vmcnt(0)\n"
    "v_mov_b32 v2, 0\n"
    "v_max3_u32 v2, v16, v17, v2\n"
    "v_max3_u32 v2, v18, v19, v2\n"
    "v_max3_u32 v2, v20, v21, v2\n"
    "v_max3_u32 v2, v22, v23, v2\n"
    "v_max3_u32 v2, v24, v25, v2\n"
    "v_max3_u32 v2, v26, v27, v2\n"
    "v_max3_u32 v2, v28, v29, v2\n"
    "v_max3_u32 v2, v30, v31, v2\n"
    "v_max3_u32 v2, v32, v33, v2\n"
    "v_max3_u32 v2, v34, v35, v2\n"
    "v_max3_u32 v2, v36, v37, v2\n"
    "v_max3_u32 v2, v38, v39, v2\n"
    "v_max3_u32 v2, v40, v41, v2\n"
    "v_max3_u32 v2, v42, v43, v2\n"
    "v_cmp_eq_u32 vcc, -1, v2\n"
    "s_cbranch_vccnz LS1_%=_again\n"
    "s_branch LS1_%=_compute\n"
    "LS1_%=_sxwait:\n"
    "s_add_u32 s50, s50, 1\n"
    "s_cmp_gt_u32 s50, 1920000\n"
    "s_cbranch_scc1 LS_exit_%=\n"
    "s_branch LS1_%=_sx\n"
    "LS1_%=_rescale:\n"
    "v_mov_b32 v2, 0x7fffff00\n"
    "v_cndmask_b32 v11, v2, v13, vcc\n"
    "v_cndmask_b32 v12, v2, v14, vcc\n"
    "v_max3_f32 v141, v134, v135, 0\n"
    "v_mov_b32 v143, v141\n"
    "s_nop 1\n"
    "v_permlane16_swap_b32 v141, v143\n"
    "v_max_f32 v141, v141, v143\n"
    "v_mov_b32 v143, v141\n"
    "s_nop 1\n"
    "v_permlane32_swap_b32 v141, v143\n"
    "v_max_f32 v141, v141, v143\n"
    "v_sub_f32 v144, 0, v141\n"
    "v_exp_f32 v144, v144\n"
    "s_nop 0\n"
    "v_mul_f32 v124, v124, v144\n"
    "v_mul_f32 v125, v125, v144\n"
    "v_mul_f32 v126, v126, v144\n"
    "v_mul_f32 v127, v127, v144\n"
    "v_mul_f32 v128, v128, v144\n"
    "v_mul_f32 v129, v129, v144\n"
    "v_mul_f32 v130, v130, v144\n"
    "v_mul_f32 v131, v131, v144\n"
    "v_mul_f32 v142, v142, v144\n"
    "v_sub_f32 v134, v134, v141\n"
    "v_sub_f32 v135, v135, v141\n"
    "v_exp_f32 v132, v134\n"
    "v_exp_f32 v133, v135\n"
    "v_add_f32 v141, v132, v142\n"
    "v_add_f32 v143, v141, v133\n"
    "v_add_f32 v144, v141, v133\n"
    "s_nop 1\n"
    "v_permlane16_swap_b32 v143, v144\n"
    "v_add_f32 v141, v143, v144\n"
    "v_add_f32 v143, v143, v144\n"
    "s_nop 1\n"
    "v_permlane32_swap_b32 v143, v141\n"
    "v_add_f32 v143, v143, v141\n"
    "v_rcp_f32 v144, v143\n"
    "s_nop 0\n"
    "v_pk_mul_f32 v[108:109], v[124:125], v[144:145] op_sel_hi:[1,0]\n"
    "v_pk_mul_f32 v[110:111], v[126:127], v[144:145] op_sel_hi:[1,0]\n"
    "v_pk_mul_f32 v[112:113], v[128:129], v[144:145] op_sel_hi:[1,0]\n"
    "v_pk_mul_f32 v[114:115], v[130:131], v[144:145] op_sel_hi:[1,0]\n"
    "v_cvt_pk_bf16_f32 v136, v108, v109\n"
    "v_cvt_pk_bf16_f32 v137, v110, v111\n"
    "v_cvt_pk_bf16_f32 v138, v112, v113\n"
    "v_cvt_pk_bf16_f32 v139, v114, v115\n"
    "v_pk_mul_f32 v[132:133], v[132:133], v[144:145] op_sel_hi:[1,0]\n"
    "v_cvt_pk_bf16_f32 v132, v132, v133\n"
    "buffer_store_dwordx4 v[136:139], v11, s[40:43], s36 offen sc1\n"
    "buffer_store_dword v132, v12, s[40:43], s37 offen sc1\n"
    "s_branch LS1_%=_done\n"
    "LS2_%=_start:\n"
    "v_and_b32 v11, 15, v10\n"
    "v_lshrrev_b32 v12, 4, v10\n"
    "v_and_b32 v2, 4, v11\n"
    "v_lshlrev_b32 v3, 5, v2\n"
    "v_and_b32 v2, 1, v11\n"
    "v_lshl_add_u32 v3, v2, 5, v3\n"
    "v_and_b32 v2, 8, v11\n"
    "v_lshlrev_b32 v13, 4, v2\n"
    "v_xor_b32 v3, v3, v13\n"
    "v_lshl_add_u32 v3, v2, 5, v3\n"
    "v_and_b32 v2, 2, v11\n"
    "v_lshl_add_u32 v3, v2, 1, v3\n"
    "v_lshl_add_u32 v3, v12, 3, v3\n"
    "v_add_u32 v3, 0x1d000, v3\n"
    "v_add_u32 v5, 0xf000, v0\n"
    "v_mov_b32 v4, 0x1f444\n"
    "v_mov_b32 v15, 0\n"
    "ds_write_b32 v4, v15\n"
    "s_lshr_b32 s49, s72, 2\n"
    "s_mul_i32 s49, s49, 896\n"
    "s_add_u32 s49, s49, 0x0\n"
    "v_min_u32 v170, 6, v10\n"
    "v_lshlrev_b32 v170, 7, v170\n"
    "v_add_u32 v170, s49, v170\n"
    "s_mul_i32 s51, s72, 401408\n"
    "s_add_u32 s51, s51, 0xd3000\n"
    "s_add_u32 s49, s51, 372736\n"
    "s_mov_b32 m0, 0x16000\n"
    "s_nop 0\n"
    "buffer_load_dwordx4 v0, s[40:43], s49 offen offset:0 lds\n"
    "buffer_load_dwordx4 v0, s[40:43], s49 offen offset:1024 lds\n"
    "buffer_load_dwordx4 v0, s[40:43], s49 offen offset:2048 lds\n"
    "buffer_load_dwordx4 v0, s[40:43], s49 offen offset:3072 lds\n"
    "s_mov_b32 m0, 0x17000\n"
    "s_add_u32 s49, s49, 4096\n"
    "buffer_load_dwordx4 v0, s[40:43], s49 offen offset:0 lds\n"
    "buffer_load_dwordx4 v0, s[40:43], s49 offen offset:1024 lds\n"
    "buffer_load_dwordx4 v0, s[40:43], s49 offen offset:2048 lds\n"
    "buffer_load_dwordx4 v0, s[40:43], s49 offen offset:3072 lds\n"
    "s_mov_b32 m0, 0x18000\n"
    "s_add_u32 s49, s49, 4096\n"
    "buffer_load_dwordx4 v0, s[40:43], s49 offen offset:0 lds\n"
    "buffer_load_dwordx4 v0, s[40:43], s49 offen offset:1024 lds\n"
    "buffer_load_dwordx4 v0, s[40:43], s49 offen offset:2048 lds\n"
    "buffer_load_dwordx4 v0, s[40:43], s49 offen offset:3072 lds\n"
    "s_mov_b32 m0, 0x19000\n"
    "s_add_u32 s49, s49, 4096\n"
    "buffer_load_dwordx4 v0, s[40:43], s49 offen offset:0 lds\n"
    "buffer_load_dwordx4 v0, s[40:43], s49 offen offset:1024 lds\n"
    "s_add_u32 s49, s51, 172032\n"
    "buffer_load_dwordx4 a[0:3], v0, s[40:43], s49 offen offset:0\n"
    "buffer_load_dwordx4 a[4:7], v0, s[40:43], s49 offen offset:1024\n"
    "buffer_load_dwordx4 a[8:11], v0, s[40:43], s49 offen offset:2048\n"
    "buffer_load_dwordx4 a[12:15], v0, s[40:43], s49 offen offset:3072\n"
    "s_add_u32 s49, s49, 4096\n"
    "buffer_load_dwordx4 a[16:19], v0, s[40:43], s49 offen offset:0\n"
    "buffer_load_dwordx4 a[20:23], v0, s[40:43], s49 offen offset:1024\n"
    "buffer_load_dwordx4 a[24:27], v0, s[40:43], s49 offen offset:2048\n"
    "buffer_load_dwordx4 a[28:31], v0, s[40:43], s49 offen offset:3072\n"
    "s_add_u32 s49, s49, 4096\n"
    "buffer_load_dwordx4 a[32:35], v0, s[40:43], s49 offen offset:0\n"
    "buffer_load_dwordx4 a[36:39], v0, s[40:43], s49 offen offset:1024\n"
    "buffer_load_dwordx4 a[40:43], v0, s[40:43], s49 offen offset:2048\n"
    "buffer_load_dwordx4 a[44:47], v0, s[40:43], s49 offen offset:3072\n"
    "s_add_u32 s49, s49, 4096\n"
    "buffer_load_dwordx4 a[48:51], v0, s[40:43], s49 offen offset:0\n"
    "buffer_load_dwordx4 a[52:55], v0, s[40:43], s49 offen offset:1024\n"
    "buffer_load_dwordx4 a[56:59], v0, s[40:43], s49 offen offset:2048\n"
    "buffer_load_dwordx4 a[60:63], v0, s[40:43], s49 offen offset:3072\n"
    "s_add_u32 s49, s49, 4096\n"
    "buffer_load_dwordx4 a[64:67], v0, s[40:43], s49 offen offset:0\n"
    "buffer_load_dwordx4 a[68:71], v0, s[40:43], s49 offen offset:1024\n"
    "buffer_load_dwordx4 a[72:75], v0, s[40:43], s49 offen offset:2048\n"
    "buffer_load_dwordx4 a[76:79], v0, s[40:43], s49 offen offset:3072\n"
    "s_add_u32 s49, s49, 4096\n"
    "buffer_load_dwordx4 a[80:83], v0, s[40:43], s49 offen offset:0\n"
    "buffer_load_dwordx4 a[84:87], v0, s[40:43], s49 offen offset:1024\n"
    "buffer_load_dwordx4 a[88:91], v0, s[40:43], s49 offen offset:2048\n"
    "buffer_load_dwordx4 a[92:95], v0, s[40:43], s49 offen offset:3072\n"
    "s_add_u32 s49, s49, 4096\n"
    "buffer_load_dwordx4 a[96:99], v0, s[40:43], s49 offen offset:0\n"
    "buffer_load_dwordx4 a[100:103], v0, s[40:43], s49 offen offset:1024\n"
    "buffer_load_dwordx4 a[104:107], v0, s[40:43], s49 offen offset:2048\n"
    "buffer_load_dwordx4 a[108:111], v0, s[40:43], s49 offen offset:3072\n"
    "s_add_u32 s49, s49, 4096\n"
    "buffer_load_dwordx4 a[112:115], v0, s[40:43], s49 offen offset:0\n"
    "buffer_load_dwordx4 a[116:119], v0, s[40:43], s49 offen offset:1024\n"
    "buffer_load_dwordx4 a[120:123], v0, s[40:43], s49 offen offset:2048\n"
    "buffer_load_dwordx4 a[124:127], v0, s[40:43], s49 offen offset:3072\n"
    "s_add_u32 s49, s49, 4096\n"
    "buffer_load_dwordx4 a[128:131], v0, s[40:43], s49 offen offset:0\n"
    "buffer_load_dwordx4 a[132:135], v0, s[40:43], s49 offen offset:1024\n"
    "buffer_load_dwordx4 a[136:139], v0, s[40:43], s49 offen offset:2048\n"
    "buffer_load_dwordx4 a[140:143], v0, s[40:43], s49 offen offset:3072\n"
    "s_add_u32 s49, s49, 4096\n"
    "buffer_load_dwordx4 a[144:147], v0, s[40:43], s49 offen offset:0\n"
    "buffer_load_dwordx4 a[148:151], v0, s[40:43], s49 offen offset:1024\n"
    "buffer_load_dwordx4 a[152:155], v0, s[40:43], s49 offen offset:2048\n"
    "buffer_load_dwordx4 a[156:159], v0, s[40:43], s49 offen offset:3072\n"
    "s_add_u32 s49, s49, 4096\n"
    "s_waitcnt vmcnt(40)\n"
    "buffer_load_dwordx4 a[160:163], v0, s[40:43], s49 offen offset:0\n"
    "buffer_load_dwordx4 a[164:167], v0, s[40:43], s49 offen offset:1024\n"
    "buffer_load_dwordx4 a[168:171], v0, s[40:43], s49 offen offset:2048\n"
    "buffer_load_dwordx4 a[172:175], v0, s[40:43], s49 offen offset:3072\n"
    "s_add_u32 s49, s49, 4096\n"
    "buffer_load_dwordx4 a[176:179], v0, s[40:43], s49 offen offset:0\n"
    "buffer_load_dwordx4 a[180:183], v0, s[40:43], s49 offen offset:1024\n"
    "buffer_load_dwordx4 a[184:187], v0, s[40:43], s49 offen offset:2048\n"
    "buffer_load_dwordx4 a[188:191], v0, s[40:43], s49 offen offset:3072\n"
    "s_add_u32 s49, s49, 4096\n"
    "s_waitcnt vmcnt(40)\n"
    "buffer_load_dwordx4 a[192:195], v0, s[40:43], s49 offen offset:0\n"
    "buffer_load_dwordx4 a[196:199], v0, s[40:43], s49 offen offset:1024\n"
    "buffer_load_dwordx4 a[200:203], v0, s[40:43], s49 offen offset:2048\n"
    "buffer_load_dwordx4 a[204:207], v0, s[40:43], s49 offen offset:3072\n"
    "s_add_u32 s49, s49, 4096\n"
    "buffer_load_dwordx4 a[208:211], v0, s[40:43], s49 offen offset:0\n"
    "buffer_load_dwordx4 a[212:215], v0, s[40:43], s49 offen offset:1024\n"
    "buffer_load_dwordx4 a[216:219], v0, s[40:43], s49 offen offset:2048\n"
    "buffer_load_dwordx4 a[220:223], v0, s[40:43], s49 offen offset:3072\n"
    "s_add_u32 s49, s49, 4096\n"
    "s_waitcnt vmcnt(40)\n"
    "buffer_load_dwordx4 a[224:227], v0, s[40:43], s49 offen offset:0\n"
    "buffer_load_dwordx4 a[228:231], v0, s[40:43], s49 offen offset:1024\n"
    "buffer_load_dwordx4 a[232:235], v0, s[40:43], s49 offen offset:2048\n"
    "buffer_load_dwordx4 a[236:239], v0, s[40:43], s49 offen offset:3072\n"
    "s_add_u32 s49, s49, 4096\n"
    "buffer_load_dwordx4 a[240:243], v0, s[40:43], s49 offen offset:0\n"
    "buffer_load_dwordx4 a[244:247], v0, s[40:43], s49 offen offset:1024\n"
    "buffer_load_dwordx4 a[248:251], v0, s[40:43], s49 offen offset:2048\n"
    "buffer_load_dwordx4 a[252:255], v0, s[40:43], s49 offen offset:3072\n"
    "s_add_u32 s49, s49, 4096\n"
    "s_waitcnt vmcnt(40)\n"
    "buffer_load_dwordx4 v[174:177], v0, s[40:43], s49 offen offset:0\n"
    "buffer_load_dwordx4 v[178:181], v0, s[40:43], s49 offen offset:1024\n"
    "buffer_load_dwordx4 v[182:185], v0, s[40:43], s49 offen offset:2048\n"
    "buffer_load_dwordx4 v[186:189], v0, s[40:43], s49 offen offset:3072\n"
    "s_add_u32 s49, s49, 4096\n"
    "buffer_load_dwordx4 v[190:193], v0, s[40:43], s49 offen offset:0\n"
    "buffer_load_dwordx4 v[194:197], v0, s[40:43], s49 offen offset:1024\n"
    "buffer_load_dwordx4 v[198:201], v0, s[40:43], s49 offen offset:2048\n"
    "buffer_load_dwordx4 v[202:205], v0, s[40:43], s49 offen offset:3072\n"
    "s_add_u32 s49, s49, 4096\n"
    "s_waitcnt vmcnt(40)\n"
    "buffer_load_dwordx4 v[206:209], v0, s[40:43], s49 offen offset:0\n"
    "buffer_load_dwordx4 v[210:213], v0, s[40:43], s49 offen offset:1024\n"
    "buffer_load_dwordx4 v[214:217], v0, s[40:43], s49 offen offset:2048\n"
    "buffer_load_dwordx4 v[218:221], v0, s[40:43], s49 offen offset:3072\n"
    "s_add_u32 s49, s49, 4096\n"
    "buffer_load_dwordx4 v[222:225], v0, s[40:43], s49 offen offset:0\n"
    "buffer_load_dwordx4 v[226:229], v0, s[40:43], s49 offen offset:1024\n"
    "buffer_load_dwordx4 v[230:233], v0, s[40:43], s49 offen offset:2048\n"
    "buffer_load_dwordx4 v[234:237], v0, s[40:43], s49 offen offset:3072\n"
    "s_add_u32 s49, s49, 4096\n"
    "s_waitcnt vmcnt(40)\n"
    "buffer_load_dwordx4 v[238:241], v0, s[40:43], s49 offen offset:0\n"
    "buffer_load_dwordx4 v[242:245], v0, s[40:43], s49 offen offset:1024\n"
    "buffer_load_dwordx4 v[246:249], v0, s[40:43], s49 offen offset:2048\n"
    "buffer_load_dwordx4 v[250:253], v0, s[40:43], s49 offen offset:3072\n"
    "v_min_u32 v168, 4, v10\n"
    "v_lshlrev_b32 v168, 2, v168\n"
    "v_add_u32 v168, 0xf800, v168\n"
    "s_mov_b32 s50, 0\n"
    "LS2_%=_pt:\n"
    "buffer_load_dword v169, v168, s[40:43], 0 offen sc1\n"
    "s_add_u32 s50, s50, 1\n"
    "s_cmp_gt_u32 s50, 30000\n"
    "s_cbranch_scc1 LS_exit_%=\n"
    "s_waitcnt vmcnt(0)\n"
    "v_cmp_eq_u32 vcc, -1, v169\n"
    "s_cbranch_vccnz LS2_%=_pt\n"
    "v_readlane_b32 s49, v169, 0\n"
    "s_nop 1\n"
    "s_bfe_u32 s39, s49, 0x70000\n"
    "s_lshl_b32 s39, s39, 12\n"
    "s_add_u32 s39, s39, %7\n"
    "s_bfe_u32 s54, s49, 0x70008\n"
    "s_lshl_b32 s54, s54, 12\n"
    "s_add_u32 s54, s54, %7\n"
    "s_bfe_u32 s55, s49, 0x70010\n"
    "s_lshl_b32 s55, s55, 12\n"
    "s_add_u32 s55, s55, %7\n"
    "s_bfe_u32 s56, s49, 0x70018\n"
    "s_lshl_b32 s56, s56, 12\n"
    "s_add_u32 s56, s56, %7\n"
    "v_readlane_b32 s49, v169, 1\n"
    "s_nop 1\n"
    "s_bfe_u32 s57, s49, 0x70000\n"
    "s_lshl_b32 s57, s57, 12\n"
    "s_add_u32 s57, s57, %7\n"
    "s_bfe_u32 s58, s49, 0x70008\n"
    "s_lshl_b32 s58, s58, 12\n"
    "s_add_u32 s58, s58, %7\n"
    "s_bfe_u32 s59, s49, 0x70010\n"
    "s_lshl_b32 s59, s59, 12\n"
    "s_add_u32 s59, s59, %7\n"
    "s_bfe_u32 s60, s49, 0x70018\n"
    "s_lshl_b32 s60, s60, 12\n"
    "s_add_u32 s60, s60, %7\n"
    "v_readlane_b32 s49, v169, 2\n"
    "s_nop 1\n"
    "s_bfe_u32 s61, s49, 0x70000\n"
    "s_lshl_b32 s61, s61, 12\n"
    "s_add_u32 s61, s61, %7\n"
    "s_bfe_u32 s62, s49, 0x70008\n"
    "s_lshl_b32 s62, s62, 12\n"
    "s_add_u32 s62, s62, %7\n"
    "s_bfe_u32 s63, s49, 0x70010\n"
    "s_lshl_b32 s63, s63, 12\n"
    "s_add_u32 s63, s63, %7\n"
    "s_bfe_u32 s64, s49, 0x70018\n"
    "s_lshl_b32 s64, s64, 12\n"
    "s_add_u32 s64, s64, %7\n"
    "v_readlane_b32 s49, v169, 3\n"
    "s_nop 1\n"
    "s_bfe_u32 s65, s49, 0x70000\n"
    "s_lshl_b32 s65, s65, 12\n"
    "s_add_u32 s65, s65, %7\n"
    "s_bfe_u32 s66, s49, 0x70008\n"
    "s_lshl_b32 s66, s66, 12\n"
    "s_add_u32 s66, s66, %7\n"
    "s_bfe_u32 s67, s49, 0x70010\n"
    "s_lshl_b32 s67, s67, 12\n"
    "s_add_u32 s67, s67, %7\n"
    "s_bfe_u32 s68, s49, 0x70018\n"
    "s_lshl_b32 s68, s68, 12\n"
    "s_add_u32 s68, s68, %7\n"
    "v_readlane_b32 s49, v169, 4\n"
    "s_nop 1\n"
    "s_bfe_u32 s77, s49, 0x70000\n"
    "s_lshl_b32 s77, s77, 12\n"
    "s_add_u32 s77, s77, %7\n"
    "s_bfe_u32 s78, s49, 0x70008\n"
    "s_lshl_b32 s78, s78, 12\n"
    "s_add_u32 s78, s78, %7\n"
    "s_bfe_u32 s48, s49, 0x70010\n"
    "s_lshl_b32 s48, s48, 12\n"
    "s_add_u32 s48, s48, %7\n"
    "s_mov_b32 s96, 1\n"
    "s_mov_b32 s52, 1\n"
    "s_mov_b32 s50, 0\n"
    "LS2_%=_pf0_poll:\n"
    "buffer_load_dword v171, v170, s[40:43], 0 offen sc1\n"
    "s_add_u32 s50, s50, 1\n"
    "s_cmp_gt_u32 s50, 30000\n"
    "s_cbranch_scc1 LS_exit_%=\n"
    "s_waitcnt vmcnt(0)\n"
    "v_cmp_gt_u32 vcc, s52, v171\n"
    "s_cbranch_vccnz LS2_%=_pf0_poll\n"
    "LS2_%=_pf0_ok:\n"
    "s_sub_u32 s49, s96, 1\n"
    "s_and_b32 s99, s49, 1\n"
    "s_lshl_b32 s99, s99, 15\n"
    "s_add_u32 s99, s99, 6144\n"
    "s_min_u32 s49, s49, 63\n"
    "s_lshl_b32 s49, s49, 4\n"
    "s_add_u32 s49, s49, s72\n"
    "s_mul_i32 s49, s49, 28672\n"
    "s_add_u32 s97, s49, 0xed4800\n"
    "s_add_u32 m0, s99, 0\n"
    "s_add_u32 s98, s97, 0\n"
    "buffer_load_dwordx4 v0, s[40:43], s98 offen offset:0 sc1 lds\n"
    "buffer_load_dwordx4 v0, s[40:43], s98 offen offset:1024 sc1 lds\n"
    "buffer_load_dwordx4 v0, s[40:43], s98 offen offset:2048 sc1 lds\n"
    "buffer_load_dwordx4 v0, s[40:43], s98 offen offset:3072 sc1 lds\n"
    "s_add_u32 m0, s99, 4096\n"
    "s_add_u32 s98, s97, 4096\n"
    "buffer_load_dwordx4 v0, s[40:43], s98 offen offset:0 sc1 lds\n"
    "buffer_load_dwordx4 v0, s[40:43], s98 offen offset:1024 sc1 lds\n"
    "buffer_load_dwordx4 v0, s[40:43], s98 offen offset:2048 sc1 lds\n"
    "buffer_load_dwordx4 v0, s[40:43], s98 offen offset:3072 sc1 lds\n"
    "s_add_u32 m0, s99, 8192\n"
    "s_add_u32 s98, s97, 8192\n"
    "buffer_load_dwordx4 v0, s[40:43], s98 offen offset:0 sc1 lds\n"
    "buffer_load_dwordx4 v0, s[40:43], s98 offen offset:1024 sc1 lds\n"
    "buffer_load_dwordx4 v0, s[40:43], s98 offen offset:2048 sc1 lds\n"
    "s_waitcnt vmcnt(0)\n"
    ".p2align 6\n"
    "s_nop 0\n"
    "LS2_%=_top:\n"
    "s_mov_b32 s50, 0\n"
    "s_add_u32 s49, s44, 1\n"
    "v_mov_b32 v15, s49\n"
    "v_mov_b32 v44, 0\n"
    "v_mov_b32 v45, 0\n"
    "v_mov_b32 v46, 0\n"
    "v_mov_b32 v47, 0\n"
    "v_mov_b32 v48, 0\n"
    "v_mov_b32 v49, 0\n"
    "v_mov_b32 v50, 0\n"
    "v_mov_b32 v51, 0\n"
    "v_mov_b32 v52, 0\n"
    "v_mov_b32 v53, 0\n"
    "v_mov_b32 v54, 0\n"
    "v_mov_b32 v55, 0\n"
    "v_mov_b32 v56, 0\n"
    "v_mov_b32 v57, 0\n"
    "v_mov_b32 v58, 0\n"
    "v_mov_b32 v59, 0\n"
    "v_mov_b32 v60, 0\n"
    "v_mov_b32 v61, 0\n"
    "v_mov_b32 v62, 0\n"
    "v_mov_b32 v63, 0\n"
    "v_mov_b32 v64, 0\n"
    "v_mov_b32 v65, 0\n"
    "v_mov_b32 v66, 0\n"
    "v_mov_b32 v67, 0\n"
    "v_mov_b32 v68, 0\n"
    "v_mov_b32 v69, 0\n"
    "v_mov_b32 v70, 0\n"
    "v_mov_b32 v71, 0\n"
    "v_mov_b32 v72, 0\n"
    "v_mov_b32 v73, 0\n"
    "v_mov_b32 v74, 0\n"
    "v_mov_b32 v75, 0\n"
    "s_waitcnt lgkmcnt(0)\n"
    "s_barrier\n"
    "LS2_%=_issue:\n"
    "buffer_load_dwordx4 v[16:19], v0, s[40:43], s69 offen offset:0 sc1\n"
    "ds_read_b128 v[76:79], v5 offset:0\n"
    "ds_read_b128 v[80:83], v5 offset:1024\n"
    "s_nop 8\n"
    "buffer_load_dwordx4 v[20:23], v0, s[40:43], s69 offen offset:1024 sc1\n"
    "ds_read_b128 v[84:87], v5 offset:14336\n"
    "ds_read_b128 v[88:91], v5 offset:15360\n"
    "s_nop 8\n"
    "buffer_load_dwordx4 v[24:27], v0, s[40:43], s69 offen offset:2048 sc1\n"
    "ds_read_b128 v[92:95], v5 offset:2048\n"
    "ds_read_b128 v[96:99], v5 offset:3072\n"
    "s_nop 8\n"
    "buffer_load_dwordx4 v[28:31], v0, s[40:43], s69 offen offset:3072 sc1\n"
    "ds_read_b128 v[100:103], v5 offset:16384\n"
    "ds_read_b128 v[104:107], v5 offset:17408\n"
    "s_nop 8\n"
    "buffer_load_dwordx4 v[32:35], v0, s[40:43], s79 offen offset:0 sc1\n"
    "ds_read_b128 v[108:111], v5 offset:4096\n"
    "ds_read_b128 v[112:115], v5 offset:5120\n"
    "s_nop 8\n"
    "buffer_load_dwordx4 v[36:39], v0, s[40:43], s79 offen offset:1024 sc1\n"
    "ds_read_b128 v[116:119], v5 offset:18432\n"
    "ds_read_b128 v[120:123], v5 offset:19456\n"
    "s_nop 8\n"
    "buffer_load_dwordx4 v[40:43], v0, s[40:43], s79 offen offset:2048 sc1\n"
    "LS2_%=_compute:\n"
    "s_waitcnt vmcnt(6)\n"
    "v_smfmac_f32_16x16x64_bf16 v[44:47], v[16:19], a[0:7], v1\n"
    "v_smfmac_f32_16x16x64_bf16 v[48:51], v[16:19], a[56:63], v1\n"
    "v_smfmac_f32_16x16x64_bf16 v[52:55], v[16:19], a[112:119], v1\n"
    "v_smfmac_f32_16x16x64_bf16 v[56:59], v[16:19], a[168:175], v1\n"
    "v_smfmac_f32_16x16x64_bf16 v[60:63], v[16:19], a[224:231], v1\n"
    "v_smfmac_f32_16x16x64_bf16 v[64:67], v[16:19], v[198:205], v1\n"
    "s_waitcnt lgkmcnt(10)\n"
    "v_smfmac_f32_16x16x64_bf16 v[68:71], v[16:19], v[76:83], v1\n"
    "ds_read_b128 v[76:79], v5 offset:6144\n"
    "ds_read_b128 v[80:83], v5 offset:7168\n"
    "s_waitcnt lgkmcnt(10)\n"
    "v_smfmac_f32_16x16x64_bf16 v[72:75], v[16:19], v[84:91], v1\n"
    "ds_read_b128 v[84:87], v5 offset:8192\n"
    "ds_read_b128 v[88:91], v5 offset:9216\n"
    "s_waitcnt vmcnt(5)\n"
    "v_smfmac_f32_16x16x64_bf16 v[44:47], v[20:23], a[8:15], v1\n"
    "v_smfmac_f32_16x16x64_bf16 v[48:51], v[20:23], a[64:71], v1\n"
    "v_smfmac_f32_16x16x64_bf16 v[52:55], v[20:23], a[120:127], v1\n"
    "v_smfmac_f32_16x16x64_bf16 v[56:59], v[20:23], a[176:183], v1\n"
    "v_smfmac_f32_16x16x64_bf16 v[60:63], v[20:23], a[232:239], v1\n"
    "v_smfmac_f32_16x16x64_bf16 v[64:67], v[20:23], v[206:213], v1\n"
    "s_waitcnt lgkmcnt(10)\n"
    "v_smfmac_f32_16x16x64_bf16 v[68:71], v[20:23], v[92:99], v1\n"
    "ds_read_b128 v[92:95], v5 offset:10240\n"
    "ds_read_b128 v[96:99], v5 offset:11264\n"
    "s_waitcnt lgkmcnt(10)\n"
    "v_smfmac_f32_16x16x64_bf16 v[72:75], v[20:23], v[100:107], v1\n"
    "ds_read_b128 v[100:103], v5 offset:12288\n"
    "ds_read_b128 v[104:107], v5 offset:13312\n"
    "s_waitcnt vmcnt(4)\n"
    "v_smfmac_f32_16x16x64_bf16 v[44:47], v[24:27], a[16:23], v1\n"
    "v_smfmac_f32_16x16x64_bf16 v[48:51], v[24:27], a[72:79], v1\n"
    "v_smfmac_f32_16x16x64_bf16 v[52:55], v[24:27], a[128:135], v1\n"
    "v_smfmac_f32_16x16x64_bf16 v[56:59], v[24:27], a[184:191], v1\n"
    "v_smfmac_f32_16x16x64_bf16 v[60:63], v[24:27], a[240:247], v1\n"
    "v_smfmac_f32_16x16x64_bf16 v[64:67], v[24:27], v[214:221], v1\n"
    "s_waitcnt lgkmcnt(10)\n"
    "v_smfmac_f32_16x16x64_bf16 v[68:71], v[24:27], v[108:115], v1\n"
    "ds_read_b128 v[108:111], v5 offset:20480\n"
    "ds_read_b128 v[112:115], v5 offset:21504\n"
    "s_waitcnt lgkmcnt(10)\n"
    "v_smfmac_f32_16x16x64_bf16 v[72:75], v[24:27], v[116:123], v1\n"
    "ds_read_b128 v[116:119], v5 offset:22528\n"
    "ds_read_b128 v[120:123], v5 offset:23552\n"
    "s_waitcnt vmcnt(3)\n"
    "s_waitcnt lgkmcnt(10)\n"
    "v_smfmac_f32_16x16x64_bf16 v[68:71], v[28:31], v[76:83], v1\n"
    "ds_read_b128 v[76:79], v5 offset:24576\n"
    "ds_read_b128 v[80:83], v5 offset:25600\n"
    "s_waitcnt vmcnt(2)\n"
    "s_waitcnt lgkmcnt(10)\n"
    "v_smfmac_f32_16x16x64_bf16 v[68:71], v[32:35], v[84:91], v1\n"
    "ds_read_b128 v[84:87], v5 offset:26624\n"
    "ds_read_b128 v[88:91], v5 offset:27648\n"
    "s_waitcnt vmcnt(1)\n"
    "s_waitcnt lgkmcnt(10)\n"
    "v_smfmac_f32_16x16x64_bf16 v[68:71], v[36:39], v[92:99], v1\n"
    "s_waitcnt vmcnt(0)\n"
    "s_waitcnt lgkmcnt(8)\n"
    "v_smfmac_f32_16x16x64_bf16 v[68:71], v[40:43], v[100:107], v1\n"
    "s_waitcnt lgkmcnt(6)\n"
    "v_smfmac_f32_16x16x64_bf16 v[72:75], v[28:31], v[108:115], v1\n"
    "s_waitcnt lgkmcnt(4)\n"
    "v_smfmac_f32_16x16x64_bf16 v[72:75], v[32:35], v[116:123], v1\n"
    "v_add_f32 v68, v68, v70\n"
    "s_waitcnt lgkmcnt(2)\n"
    "v_smfmac_f32_16x16x64_bf16 v[72:75], v[36:39], v[76:83], v1\n"
    "v_add_f32 v69, v69, v71\n"
    "s_waitcnt lgkmcnt(0)\n"
    "v_smfmac_f32_16x16x64_bf16 v[72:75], v[40:43], v[84:91], v1\n"
    "v_cmp_u_f32 vcc, v68, v69\n"
    "v_smfmac_f32_16x16x64_bf16 v[44:47], v[28:31], a[24:31], v1\n"
    "s_cbranch_vccnz LS2_%=_nan\n"
    "LS2_%=_good:\n"
    "ds_write_b32 v4, v15\n"
    "v_smfmac_f32_16x16x64_bf16 v[44:47], v[32:35], a[32:39], v1\n"
    "v_cvt_pk_bf16_f32 v68, v68, v69\n"
    "v_smfmac_f32_16x16x64_bf16 v[44:47], v[36:39], a[40:47], v1\n"
    "ds_write_b32 v3, v68 offset:6144\n"
    "v_smfmac_f32_16x16x64_bf16 v[44:47], v[40:43], a[48:55], v1\n"
    "v_add_f32 v72, v72, v74\n"
    "v_smfmac_f32_16x16x64_bf16 v[48:51], v[28:31], a[80:87], v1\n"
    "v_add_f32 v73, v73, v75\n"
    "v_smfmac_f32_16x16x64_bf16 v[48:51], v[32:35], a[88:95], v1\n"
    "v_cvt_pk_bf16_f32 v72, v72, v73\n"
    "v_smfmac_f32_16x16x64_bf16 v[48:51], v[36:39], a[96:103], v1\n"
    "ds_write_b32 v3, v72 offset:6208\n"
    "v_smfmac_f32_16x16x64_bf16 v[48:51], v[40:43], a[104:111], v1\n"
    "v_add_f32 v44, v44, v46\n"
    "v_smfmac_f32_16x16x64_bf16 v[52:55], v[28:31], a[136:143], v1\n"
    "v_add_f32 v45, v45, v47\n"
    "v_smfmac_f32_16x16x64_bf16 v[52:55], v[32:35], a[144:151], v1\n"
    "v_cvt_pk_bf16_f32 v44, v44, v45\n"
    "v_smfmac_f32_16x16x64_bf16 v[52:55], v[36:39], a[152:159], v1\n"
    "ds_write_b32 v3, v44 offset:3072\n"
    "v_smfmac_f32_16x16x64_bf16 v[52:55], v[40:43], a[160:167], v1\n"
    "v_add_f32 v48, v48, v50\n"
    "v_smfmac_f32_16x16x64_bf16 v[56:59], v[28:31], a[192:199], v1\n"
    "v_add_f32 v49, v49, v51\n"
    "v_smfmac_f32_16x16x64_bf16 v[56:59], v[32:35], a[200:207], v1\n"
    "v_cvt_pk_bf16_f32 v48, v48, v49\n"
    "v_smfmac_f32_16x16x64_bf16 v[56:59], v[36:39], a[208:215], v1\n"
    "ds_write_b32 v3, v48 offset:3136\n"
    "v_smfmac_f32_16x16x64_bf16 v[56:59], v[40:43], a[216:223], v1\n"
    "v_add_f32 v52, v52, v54\n"
    "v_smfmac_f32_16x16x64_bf16 v[60:63], v[28:31], a[248:255], v1\n"
    "v_add_f32 v53, v53, v55\n"
    "v_smfmac_f32_16x16x64_bf16 v[60:63], v[32:35], v[174:181], v1\n"
    "v_cvt_pk_bf16_f32 v52, v52, v53\n"
    "v_smfmac_f32_16x16x64_bf16 v[60:63], v[36:39], v[182:189], v1\n"
    "ds_write_b32 v3, v52 offset:3584\n"
    "v_smfmac_f32_16x16x64_bf16 v[60:63], v[40:43], v[190:197], v1\n"
    "v_add_f32 v56, v56, v58\n"
    "v_smfmac_f32_16x16x64_bf16 v[64:67], v[28:31], v[222:229], v1\n"
    "v_add_f32 v57, v57, v59\n"
    "v_smfmac_f32_16x16x64_bf16 v[64:67], v[32:35], v[230:237], v1\n"
    "v_cvt_pk_bf16_f32 v56, v56, v57\n"
    "v_smfmac_f32_16x16x64_bf16 v[64:67], v[36:39], v[238:245], v1\n"
    "ds_write_b32 v3, v56 offset:3648\n"
    "v_smfmac_f32_16x16x64_bf16 v[64:67], v[40:43], v[246:253], v1\n"
    "v_add_f32 v60, v60, v62\n"
    "v_add_f32 v61, v61, v63\n"
    "v_cvt_pk_bf16_f32 v60, v60, v61\n"
    "s_nop 4\n"
    "v_pk_add_f32 v[64:65], v[64:65], v[66:67]\n"
    "v_cvt_pk_bf16_f32 v64, v64, v65\n"
    "ds_write_b32 v3, v60 offset:4096\n"
    "ds_write_b32 v3, v64 offset:4160\n"
    "s_waitcnt lgkmcnt(0)\n"
    "s_barrier\n"
    "s_barrier\n"
    "s_add_u32 s52, s96, 1\n"
    "s_add_u32 s48, s44, 2\n"
    "s_cmp_lt_u32 s96, s48\n"
    "s_cbranch_scc1 LS2_%=_late\n"
    "LS2_%=_early:\n"
    "v_cmp_gt_u32 vcc, s52, v171\n"
    "s_cbranch_vccnz LS2_%=_none\n"
    "s_add_u32 s96, s96, 1\n"
    "s_sub_u32 s49, s96, 1\n"
    "s_and_b32 s99, s49, 1\n"
    "s_lshl_b32 s99, s99, 15\n"
    "s_add_u32 s99, s99, 6144\n"
    "s_min_u32 s49, s49, 63\n"
    "s_lshl_b32 s49, s49, 4\n"
    "s_add_u32 s49, s49, s72\n"
    "s_mul_i32 s49, s49, 28672\n"
    "s_add_u32 s97, s49, 0xed4800\n"
    "s_add_u32 m0, s99, 0\n"
    "s_add_u32 s98, s97, 0\n"
    "buffer_load_dwordx4 v0, s[40:43], s98 offen offset:0 sc1 lds\n"
    "buffer_load_dwordx4 v0, s[40:43], s98 offen offset:1024 sc1 lds\n"
    "buffer_load_dwordx4 v0, s[40:43], s98 offen offset:2048 sc1 lds\n"
    "buffer_load_dwordx4 v0, s[40:43], s98 offen offset:3072 sc1 lds\n"
    "s_add_u32 m0, s99, 4096\n"
    "s_add_u32 s98, s97, 4096\n"
    "buffer_load_dwordx4 v0, s[40:43], s98 offen offset:0 sc1 lds\n"
    "buffer_load_dwordx4 v0, s[40:43], s98 offen offset:1024 sc1 lds\n"
    "buffer_load_dwordx4 v0, s[40:43], s98 offen offset:2048 sc1 lds\n"
    "buffer_load_dwordx4 v0, s[40:43], s98 offen offset:3072 sc1 lds\n"
    "s_add_u32 m0, s99, 8192\n"
    "s_add_u32 s98, s97, 8192\n"
    "buffer_load_dwordx4 v0, s[40:43], s98 offen offset:0 sc1 lds\n"
    "buffer_load_dwordx4 v0, s[40:43], s98 offen offset:1024 sc1 lds\n"
    "buffer_load_dwordx4 v0, s[40:43], s98 offen offset:2048 sc1 lds\n"
    "LS2_%=_none:\n"
    "buffer_load_dword v171, v170, s[40:43], 0 offen sc1\n"
    "s_cmp_eq_u32 s44, 63\n"
    "s_cbranch_scc1 LS_exit_%=\n"
    "s_add_u32 s44, s44, 1\n"
    "s_mov_b32 s49, s54\n"
    "s_mov_b32 s53, s55\n"
    "s_mov_b32 s54, s56\n"
    "s_mov_b32 s55, s57\n"
    "s_mov_b32 s56, s58\n"
    "s_mov_b32 s57, s59\n"
    "s_mov_b32 s58, s49\n"
    "s_mov_b32 s59, s53\n"
    "s_mov_b32 s69, s54\n"
    "s_mov_b32 s79, s55\n"
    "s_mov_b32 s49, s60\n"
    "s_mov_b32 s60, s63\n"
    "s_mov_b32 s63, s66\n"
    "s_mov_b32 s66, s49\n"
    "s_mov_b32 s49, s61\n"
    "s_mov_b32 s61, s64\n"
    "s_mov_b32 s64, s67\n"
    "s_mov_b32 s67, s49\n"
    "s_mov_b32 s49, s62\n"
    "s_mov_b32 s62, s65\n"
    "s_mov_b32 s65, s68\n"
    "s_mov_b32 s68, s49\n"
    "s_branch LS2_%=_top\n"
    "LS2_%=_late:\n"
    "s_min_u32 s52, s52, 64\n"
    "v_cmp_gt_u32 vcc, s52, v171\n"
    "s_cbranch_vccz LS2_%=_f_ok\n"
    "s_mov_b32 s50, 0\n"
    "LS2_%=_f_poll:\n"
    "buffer_load_dword v171, v170, s[40:43], 0 offen sc1\n"
    "s_add_u32 s50, s50, 1\n"
    "s_cmp_gt_u32 s50, 30000\n"
    "s_cbranch_scc1 LS_exit_%=\n"
    "s_waitcnt vmcnt(0)\n"
    "v_cmp_gt_u32 vcc, s52, v171\n"
    "s_cbranch_vccnz LS2_%=_f_poll\n"
    "LS2_%=_f_ok:\n"
    "s_add_u32 s96, s96, 1\n"
    "s_sub_u32 s49, s96, 1\n"
    "s_and_b32 s99, s49, 1\n"
    "s_lshl_b32 s99, s99, 15\n"
    "s_add_u32 s99, s99, 6144\n"
    "s_min_u32 s49, s49, 63\n"
    "s_lshl_b32 s49, s49, 4\n"
    "s_add_u32 s49, s49, s72\n"
    "s_mul_i32 s49, s49, 28672\n"
    "s_add_u32 s97, s49, 0xed4800\n"
    "s_add_u32 m0, s99, 0\n"
    "s_add_u32 s98, s97, 0\n"
    "buffer_load_dwordx4 v0, s[40:43], s98 offen offset:0 sc1 lds\n"
    "buffer_load_dwordx4 v0, s[40:43], s98 offen offset:1024 sc1 lds\n"
    "buffer_load_dwordx4 v0, s[40:43], s98 offen offset:2048 sc1 lds\n"
    "buffer_load_dwordx4 v0, s[40:43], s98 offen offset:3072 sc1 lds\n"
    "s_add_u32 m0, s99, 4096\n"
    "s_add_u32 s98, s97, 4096\n"
    "buffer_load_dwordx4 v0, s[40:43], s98 offen offset:0 sc1 lds\n"
    "buffer_load_dwordx4 v0, s[40:43], s98 offen offset:1024 sc1 lds\n"
    "buffer_load_dwordx4 v0, s[40:43], s98 offen offset:2048 sc1 lds\n"
    "buffer_load_dwordx4 v0, s[40:43], s98 offen offset:3072 sc1 lds\n"
    "s_add_u32 m0, s99, 8192\n"
    "s_add_u32 s98, s97, 8192\n"
    "buffer_load_dwordx4 v0, s[40:43], s98 offen offset:0 sc1 lds\n"
    "buffer_load_dwordx4 v0, s[40:43], s98 offen offset:1024 sc1 lds\n"
    "buffer_load_dwordx4 v0, s[40:43], s98 offen offset:2048 sc1 lds\n"
    "s_waitcnt vmcnt(0)\n"
    "s_add_u32 s52, s96, 1\n"
    "s_branch LS2_%=_early\n"
    "LS2_%=_nan:\n"
    "v_mov_b32 v2, 0\n"
    "v_max3_u32 v2, v16, v17, v2\n"
    "v_max3_u32 v2, v18, v19, v2\n"
    "v_max3_u32 v2, v20, v21, v2\n"
    "v_max3_u32 v2, v22, v23, v2\n"
    "v_max3_u32 v2, v24, v25, v2\n"
    "v_max3_u32 v2, v26, v27, v2\n"
    "v_max3_u32 v2, v28, v29, v2\n"
    "v_max3_u32 v2, v30, v31, v2\n"
    "v_max3_u32 v2, v32, v33, v2\n"
    "v_max3_u32 v2, v34, v35, v2\n"
    "v_max3_u32 v2, v36, v37, v2\n"
    "v_max3_u32 v2, v38, v39, v2\n"
    "v_max3_u32 v2, v40, v41, v2\n"
    "v_max3_u32 v2, v42, v43, v2\n"
    "v_cmp_eq_u32 vcc, -1, v2\n"
    "s_cbranch_vccz LS2_%=_good\n"
    "LS2_%=_again:\n"
    "s_nop 7\n"
    "buffer_load_dwordx4 v[16:19], v0, s[40:43], s69 offen offset:0 sc1\n"
    "buffer_load_dwordx4 v[20:23], v0, s[40:43], s69 offen offset:1024 sc1\n"
    "buffer_load_dwordx4 v[24:27], v0, s[40:43], s69 offen offset:2048 sc1\n"
    "buffer_load_dwordx4 v[28:31], v0, s[40:43], s69 offen offset:3072 sc1\n"
    "buffer_load_dwordx4 v[32:35], v0, s[40:43], s79 offen offset:0 sc1\n"
    "buffer_load_dwordx4 v[36:39], v0, s[40:43], s79 offen offset:1024 sc1\n"
    "buffer_load_dwordx4 v[40:43], v0, s[40:43], s79 offen offset:2048 sc1\n"
    "s_add_u32 s50, s50, 1\n"
    "s_add_u32 s75, s75, 1\n"
    "s_cmp_gt_u32 s50, 30000\n"
    "s_cbranch_scc1 LS_exit_%=\n"
    "s_waitcnt lgkmcnt(0)\n"
    "ds_read_b128 v[76:79], v5 offset:0\n"
    "ds_read_b128 v[80:83], v5 offset:1024\n"
    "ds_read_b128 v[84:87], v5 offset:14336\n"
    "ds_read_b128 v[88:91], v5 offset:15360\n"
    "ds_read_b128 v[92:95], v5 offset:2048\n"
    "ds_read_b128 v[96:99], v5 offset:3072\n"
    "ds_read_b128 v[100:103], v5 offset:16384\n"
    "ds_read_b128 v[104:107], v5 offset:17408\n"
    "ds_read_b128 v[108:111], v5 offset:4096\n"
    "ds_read_b128 v[112:115], v5 offset:5120\n"
    "ds_read_b128 v[116:119], v5 offset:18432\n"
    "ds_read_b128 v[120:123], v5 offset:19456\n"
    "v_mov_b32 v44, 0\n"
    "v_mov_b32 v45, 0\n"
    "v_mov_b32 v46, 0\n"
    "v_mov_b32 v47, 0\n"
    "v_mov_b32 v48, 0\n"
    "v_mov_b32 v49, 0\n"
    "v_mov_b32 v50, 0\n"
    "v_mov_b32 v51, 0\n"
    "v_mov_b32 v52, 0\n"
    "v_mov_b32 v53, 0\n"
    "v_mov_b32 v54, 0\n"
    "v_mov_b32 v55, 0\n"
    "v_mov_b32 v56, 0\n"
    "v_mov_b32 v57, 0\n"
    "v_mov_b32 v58, 0\n"
    "v_mov_b32 v59, 0\n"
    "v_mov_b32 v60, 0\n"
    "v_mov_b32 v61, 0\n"
    "v_mov_b32 v62, 0\n"
    "v_mov_b32 v63, 0\n"
    "v_mov_b32 v64, 0\n"
    "v_mov_b32 v65, 0\n"
    "v_mov_b32 v66, 0\n"
    "v_mov_b32 v67, 0\n"
    "v_mov_b32 v68, 0\n"
    "v_mov_b32 v69, 0\n"
    "v_mov_b32 v70, 0\n"
    "v_mov_b32 v71, 0\n"
    "v_mov_b32 v72, 0\n"
    "v_mov_b32 v73, 0\n"
    "v_mov_b32 v74, 0\n"
    "v_mov_b32 v75, 0\n"
    "s_waitcnt vmcnt(0)\n"
    "v_mov_b32 v2, 0\n"
    "v_max3_u32 v2, v16, v17, v2\n"
    "v_max3_u32 v2, v18, v19, v2\n"
    "v_max3_u32 v2, v20, v21, v2\n"
    "v_max3_u32 v2, v22, v23, v2\n"
    "v_max3_u32 v2, v24, v25, v2\n"
    "v_max3_u32 v2, v26, v27, v2\n"
    "v_max3_u32 v2, v28, v29, v2\n"
    "v_max3_u32 v2, v30, v31, v2\n"
    "v_max3_u32 v2, v32, v33, v2\n"
    "v_max3_u32 v2, v34, v35, v2\n"
    "v_max3_u32 v2, v36, v37, v2\n"
    "v_max3_u32 v2, v38, v39, v2\n"
    "v_max3_u32 v2, v40, v41, v2\n"
    "v_max3_u32 v2, v42, v43, v2\n"
    "v_cmp_eq_u32 vcc, -1, v2\n"
    "s_cbranch_vccnz LS2_%=_again\n"
    "s_branch LS2_%=_compute\n"
    "LS3_%=_start:\n"
    "v_and_b32 v11, 15, v10\n"
    "v_lshrrev_b32 v12, 4, v10\n"
    "v_and_b32 v2, 4, v11\n"
    "v_lshlrev_b32 v3, 5, v2\n"
    "v_and_b32 v2, 1, v11\n"
    "v_lshl_add_u32 v3, v2, 5, v3\n"
    "v_and_b32 v2, 8, v11\n"
    "v_lshlrev_b32 v13, 4, v2\n"
    "v_xor_b32 v3, v3, v13\n"
    "v_lshl_add_u32 v3, v2, 5, v3\n"
    "v_and_b32 v2, 2, v11\n"
    "v_lshl_add_u32 v3, v2, 1, v3\n"
    "v_lshl_add_u32 v3, v12, 3, v3\n"
    "v_add_u32 v3, 0x1d000, v3\n"
    "v_add_u32 v5, 0x16000, v0\n"
    "v_mov_b32 v4, 0x1f448\n"
    "v_mov_b32 v15, 0\n"
    "ds_write_b32 v4, v15\n"
    "s_lshr_b32 s49, s72, 2\n"
    "s_mul_i32 s49, s49, 896\n"
    "s_add_u32 s49, s49, 0x0\n"
    "v_min_u32 v170, 6, v10\n"
    "v_lshlrev_b32 v170, 7, v170\n"
    "v_add_u32 v170, s49, v170\n"
    "s_mul_i32 s51, s72, 401408\n"
    "s_add_u32 s51, s51, 0xd3000\n"
    "s_add_u32 s49, s51, 387072\n"
    "s_mov_b32 m0, 0x19800\n"
    "s_nop 0\n"
    "buffer_load_dwordx4 v0, s[40:43], s49 offen offset:0 lds\n"
    "buffer_load_dwordx4 v0, s[40:43], s49 offen offset:1024 lds\n"
    "buffer_load_dwordx4 v0, s[40:43], s49 offen offset:2048 lds\n"
    "buffer_load_dwordx4 v0, s[40:43], s49 offen offset:3072 lds\n"
    "s_mov_b32 m0, 0x1a800\n"
    "s_add_u32 s49, s49, 4096\n"
    "buffer_load_dwordx4 v0, s[40:43], s49 offen offset:0 lds\n"
    "buffer_load_dwordx4 v0, s[40:43], s49 offen offset:1024 lds\n"
    "buffer_load_dwordx4 v0, s[40:43], s49 offen offset:2048 lds\n"
    "buffer_load_dwordx4 v0, s[40:43], s49 offen offset:3072 lds\n"
    "s_mov_b32 m0, 0x1b800\n"
    "s_add_u32 s49, s49, 4096\n"
    "buffer_load_dwordx4 v0, s[40:43], s49 offen offset:0 lds\n"
    "buffer_load_dwordx4 v0, s[40:43], s49 offen offset:1024 lds\n"
    "buffer_load_dwordx4 v0, s[40:43], s49 offen offset:2048 lds\n"
    "buffer_load_dwordx4 v0, s[40:43], s49 offen offset:3072 lds\n"
    "s_mov_b32 m0, 0x1c800\n"
    "s_add_u32 s49, s49, 4096\n"
    "buffer_load_dwordx4 v0, s[40:43], s49 offen offset:0 lds\n"
    "buffer_load_dwordx4 v0, s[40:43], s49 offen offset:1024 lds\n"
    "s_add_u32 s49, s51, 258048\n"
    "buffer_load_dwordx4 a[0:3], v0, s[40:43], s49 offen offset:0\n"
    "buffer_load_dwordx4 a[4:7], v0, s[40:43], s49 offen offset:1024\n"
    "buffer_load_dwordx4 a[8:11], v0, s[40:43], s49 offen offset:2048\n"
    "buffer_load_dwordx4 a[12:15], v0, s[40:43], s49 offen offset:3072\n"
    "s_add_u32 s49, s49, 4096\n"
    "buffer_load_dwordx4 a[16:19], v0, s[40:43], s49 offen offset:0\n"
    "buffer_load_dwordx4 a[20:23], v0, s[40:43], s49 offen offset:1024\n"
    "buffer_load_dwordx4 a[24:27], v0, s[40:43], s49 offen offset:2048\n"
    "buffer_load_dwordx4 a[28:31], v0, s[40:43], s49 offen offset:3072\n"
    "s_add_u32 s49, s49, 4096\n"
    "buffer_load_dwordx4 a[32:35], v0, s[40:43], s49 offen offset:0\n"
    "buffer_load_dwordx4 a[36:39], v0, s[40:43], s49 offen offset:1024\n"
    "buffer_load_dwordx4 a[40:43], v0, s[40:43], s49 offen offset:2048\n"
    "buffer_load_dwordx4 a[44:47], v0, s[40:43], s49 offen offset:3072\n"
    "s_add_u32 s49, s49, 4096\n"
    "buffer_load_dwordx4 a[48:51], v0, s[40:43], s49 offen offset:0\n"
    "buffer_load_dwordx4 a[52:55], v0, s[40:43], s49 offen offset:1024\n"
    "buffer_load_dwordx4 a[56:59], v0, s[40:43], s49 offen offset:2048\n"
    "buffer_load_dwordx4 a[60:63], v0, s[40:43], s49 offen offset:3072\n"
    "s_add_u32 s49, s49, 4096\n"
    "buffer_load_dwordx4 a[64:67], v0, s[40:43], s49 offen offset:0\n"
    "buffer_load_dwordx4 a[68:71], v0, s[40:43], s49 offen offset:1024\n"
    "buffer_load_dwordx4 a[72:75], v0, s[40:43], s49 offen offset:2048\n"
    "buffer_load_dwordx4 a[76:79], v0, s[40:43], s49 offen offset:3072\n"
    "s_add_u32 s49, s49, 4096\n"
    "buffer_load_dwordx4 a[80:83], v0, s[40:43], s49 offen offset:0\n"
    "buffer_load_dwordx4 a[84:87], v0, s[40:43], s49 offen offset:1024\n"
    "buffer_load_dwordx4 a[88:91], v0, s[40:43], s49 offen offset:2048\n"
    "buffer_load_dwordx4 a[92:95], v0, s[40:43], s49 offen offset:3072\n"
    "s_add_u32 s49, s49, 4096\n"
    "buffer_load_dwordx4 a[96:99], v0, s[40:43], s49 offen offset:0\n"
    "buffer_load_dwordx4 a[100:103], v0, s[40:43], s49 offen offset:1024\n"
    "buffer_load_dwordx4 a[104:107], v0, s[40:43], s49 offen offset:2048\n"
    "buffer_load_dwordx4 a[108:111], v0, s[40:43], s49 offen offset:3072\n"
    "s_add_u32 s49, s49, 4096\n"
    "buffer_load_dwordx4 a[112:115], v0, s[40:43], s49 offen offset:0\n"
    "buffer_load_dwordx4 a[116:119], v0, s[40:43], s49 offen offset:1024\n"
    "buffer_load_dwordx4 a[120:123], v0, s[40:43], s49 offen offset:2048\n"
    "buffer_load_dwordx4 a[124:127], v0, s[40:43], s49 offen offset:3072\n"
    "s_add_u32 s49, s49, 4096\n"
    "buffer_load_dwordx4 a[128:131], v0, s[40:43], s49 offen offset:0\n"
    "buffer_load_dwordx4 a[132:135], v0, s[40:43], s49 offen offset:1024\n"
    "buffer_load_dwordx4 a[136:139], v0, s[40:43], s49 offen offset:2048\n"
    "buffer_load_dwordx4 a[140:143], v0, s[40:43], s49 offen offset:3072\n"
    "s_add_u32 s49, s49, 4096\n"
    "buffer_load_dwordx4 a[144:147], v0, s[40:43], s49 offen offset:0\n"
    "buffer_load_dwordx4 a[148:151], v0, s[40:43], s49 offen offset:1024\n"
    "buffer_load_dwordx4 a[152:155], v0, s[40:43], s49 offen offset:2048\n"
    "buffer_load_dwordx4 a[156:159], v0, s[40:43], s49 offen offset:3072\n"
    "s_add_u32 s49, s49, 4096\n"
    "s_waitcnt vmcnt(40)\n"
    "buffer_load_dwordx4 a[160:163], v0, s[40:43], s49 offen offset:0\n"
    "buffer_load_dwordx4 a[164:167], v0, s[40:43], s49 offen offset:1024\n"
    "buffer_load_dwordx4 a[168:171], v0, s[40:43], s49 offen offset:2048\n"
    "buffer_load_dwordx4 a[172:175], v0, s[40:43], s49 offen offset:3072\n"
    "s_add_u32 s49, s49, 4096\n"
    "buffer_load_dwordx4 a[176:179], v0, s[40:43], s49 offen offset:0\n"
    "buffer_load_dwordx4 a[180:183], v0, s[40:43], s49 offen offset:1024\n"
    "buffer_load_dwordx4 a[184:187], v0, s[40:43], s49 offen offset:2048\n"
    "buffer_load_dwordx4 a[188:191], v0, s[40:43], s49 offen offset:3072\n"
    "s_add_u32 s49, s49, 4096\n"
    "s_waitcnt vmcnt(40)\n"
    "buffer_load_dwordx4 a[192:195], v0, s[40:43], s49 offen offset:0\n"
    "buffer_load_dwordx4 a[196:199], v0, s[40:43], s49 offen offset:1024\n"
    "buffer_load_dwordx4 a[200:203], v0, s[40:43], s49 offen offset:2048\n"
    "buffer_load_dwordx4 a[204:207], v0, s[40:43], s49 offen offset:3072\n"
    "s_add_u32 s49, s49, 4096\n"
    "buffer_load_dwordx4 a[208:211], v0, s[40:43], s49 offen offset:0\n"
    "buffer_load_dwordx4 a[212:215], v0, s[40:43], s49 offen offset:1024\n"
    "buffer_load_dwordx4 a[216:219], v0, s[40:43], s49 offen offset:2048\n"
    "buffer_load_dwordx4 a[220:223], v0, s[40:43], s49 offen offset:3072\n"
    "s_add_u32 s49, s49, 4096\n"
    "s_waitcnt vmcnt(40)\n"
    "buffer_load_dwordx4 a[224:227], v0, s[40:43], s49 offen offset:0\n"
    "buffer_load_dwordx4 a[228:231], v0, s[40:43], s49 offen offset:1024\n"
    "buffer_load_dwordx4 a[232:235], v0, s[40:43], s49 offen offset:2048\n"
    "buffer_load_dwordx4 a[236:239], v0, s[40:43], s49 offen offset:3072\n"
    "s_add_u32 s49, s49, 4096\n"
    "buffer_load_dwordx4 a[240:243], v0, s[40:43], s49 offen offset:0\n"
    "buffer_load_dwordx4 a[244:247], v0, s[40:43], s49 offen offset:1024\n"
    "buffer_load_dwordx4 a[248:251], v0, s[40:43], s49 offen offset:2048\n"
    "buffer_load_dwordx4 a[252:255], v0, s[40:43], s49 offen offset:3072\n"
    "s_add_u32 s49, s49, 4096\n"
    "s_waitcnt vmcnt(40)\n"
    "buffer_load_dwordx4 v[174:177], v0, s[40:43], s49 offen offset:0\n"
    "buffer_load_dwordx4 v[178:181], v0, s[40:43], s49 offen offset:1024\n"
    "buffer_load_dwordx4 v[182:185], v0, s[40:43], s49 offen offset:2048\n"
    "buffer_load_dwordx4 v[186:189], v0, s[40:43], s49 offen offset:3072\n"
    "s_add_u32 s49, s49, 4096\n"
    "buffer_load_dwordx4 v[190:193], v0, s[40:43], s49 offen offset:0\n"
    "buffer_load_dwordx4 v[194:197], v0, s[40:43], s49 offen offset:1024\n"
    "buffer_load_dwordx4 v[198:201], v0, s[40:43], s49 offen offset:2048\n"
    "buffer_load_dwordx4 v[202:205], v0, s[40:43], s49 offen offset:3072\n"
    "s_add_u32 s49, s49, 4096\n"
    "s_waitcnt vmcnt(40)\n"
    "buffer_load_dwordx4 v[206:209], v0, s[40:43], s49 offen offset:0\n"
    "buffer_load_dwordx4 v[210:213], v0, s[40:43], s49 offen offset:1024\n"
    "buffer_load_dwordx4 v[214:217], v0, s[40:43], s49 offen offset:2048\n"
    "buffer_load_dwordx4 v[218:221], v0, s[40:43], s49 offen offset:3072\n"
    "s_add_u32 s49, s49, 4096\n"
    "buffer_load_dwordx4 v[222:225], v0, s[40:43], s49 offen offset:0\n"
    "buffer_load_dwordx4 v[226:229], v0, s[40:43], s49 offen offset:1024\n"
    "buffer_load_dwordx4 v[230:233], v0, s[40:43], s49 offen offset:2048\n"
    "buffer_load_dwordx4 v[234:237], v0, s[40:43], s49 offen offset:3072\n"
    "s_add_u32 s49, s49, 4096\n"
    "s_waitcnt vmcnt(40)\n"
    "buffer_load_dwordx4 v[238:241], v0, s[40:43], s49 offen offset:0\n"
    "buffer_load_dwordx4 v[242:245], v0, s[40:43], s49 offen offset:1024\n"
    "buffer_load_dwordx4 v[246:249], v0, s[40:43], s49 offen offset:2048\n"
    "buffer_load_dwordx4 v[250:253], v0, s[40:43], s49 offen offset:3072\n"
    "v_min_u32 v168, 4, v10\n"
    "v_lshlrev_b32 v168, 2, v168\n"
    "v_add_u32 v168, 0xf800, v168\n"
    "s_mov_b32 s50, 0\n"
    "LS3_%=_pt:\n"
    "buffer_load_dword v169, v168, s[40:43], 0 offen sc1\n"
    "s_add_u32 s50, s50, 1\n"
    "s_cmp_gt_u32 s50, 30000\n"
    "s_cbranch_scc1 LS_exit_%=\n"
    "s_waitcnt vmcnt(0)\n"
    "v_cmp_eq_u32 vcc, -1, v169\n"
    "s_cbranch_vccnz LS3_%=_pt\n"
    "v_readlane_b32 s49, v169, 0\n"
    "s_nop 1\n"
    "s_bfe_u32 s39, s49, 0x70000\n"
    "s_lshl_b32 s39, s39, 12\n"
    "s_add_u32 s39, s39, %7\n"
    "s_bfe_u32 s54, s49, 0x70008\n"
    "s_lshl_b32 s54, s54, 12\n"
    "s_add_u32 s54, s54, %7\n"
    "s_bfe_u32 s55, s49, 0x70010\n"
    "s_lshl_b32 s55, s55, 12\n"
    "s_add_u32 s55, s55, %7\n"
    "s_bfe_u32 s56, s49, 0x70018\n"
    "s_lshl_b32 s56, s56, 12\n"
    "s_add_u32 s56, s56, %7\n"
    "v_readlane_b32 s49, v169, 1\n"
    "s_nop 1\n"
    "s_bfe_u32 s57, s49, 0x70000\n"
    "s_lshl_b32 s57, s57, 12\n"
    "s_add_u32 s57, s57, %7\n"
    "s_bfe_u32 s58, s49, 0x70008\n"
    "s_lshl_b32 s58, s58, 12\n"
    "s_add_u32 s58, s58, %7\n"
    "s_bfe_u32 s59, s49, 0x70010\n"
    "s_lshl_b32 s59, s59, 12\n"
    "s_add_u32 s59, s59, %7\n"
    "s_bfe_u32 s60, s49, 0x70018\n"
    "s_lshl_b32 s60, s60, 12\n"
    "s_add_u32 s60, s60, %7\n"
    "v_readlane_b32 s49, v169, 2\n"
    "s_nop 1\n"
    "s_bfe_u32 s61, s49, 0x70000\n"
    "s_lshl_b32 s61, s61, 12\n"
    "s_add_u32 s61, s61, %7\n"
    "s_bfe_u32 s62, s49, 0x70008\n"
    "s_lshl_b32 s62, s62, 12\n"
    "s_add_u32 s62, s62, %7\n"
    "s_bfe_u32 s63, s49, 0x70010\n"
    "s_lshl_b32 s63, s63, 12\n"
    "s_add_u32 s63, s63, %7\n"
    "s_bfe_u32 s64, s49, 0x70018\n"
    "s_lshl_b32 s64, s64, 12\n"
    "s_add_u32 s64, s64, %7\n"
    "v_readlane_b32 s49, v169, 3\n"
    "s_nop 1\n"
    "s_bfe_u32 s65, s49, 0x70000\n"
    "s_lshl_b32 s65, s65, 12\n"
    "s_add_u32 s65, s65, %7\n"
    "s_bfe_u32 s66, s49, 0x70008\n"
    "s_lshl_b32 s66, s66, 12\n"
    "s_add_u32 s66, s66, %7\n"
    "s_bfe_u32 s67, s49, 0x70010\n"
    "s_lshl_b32 s67, s67, 12\n"
    "s_add_u32 s67, s67, %7\n"
    "s_bfe_u32 s68, s49, 0x70018\n"
    "s_lshl_b32 s68, s68, 12\n"
    "s_add_u32 s68, s68, %7\n"
    "v_readlane_b32 s49, v169, 4\n"
    "s_nop 1\n"
    "s_bfe_u32 s77, s49, 0x70000\n"
    "s_lshl_b32 s77, s77, 12\n"
    "s_add_u32 s77, s77, %7\n"
    "s_bfe_u32 s78, s49, 0x70008\n"
    "s_lshl_b32 s78, s78, 12\n"
    "s_add_u32 s78, s78, %7\n"
    "s_bfe_u32 s48, s49, 0x70010\n"
    "s_lshl_b32 s48, s48, 12\n"
    "s_add_u32 s48, s48, %7\n"
    "s_mov_b32 s96, 1\n"
    "s_mov_b32 s52, 1\n"
    "s_mov_b32 s50, 0\n"
    "LS3_%=_pf0_poll:\n"
    "buffer_load_dword v171, v170, s[40:43], 0 offen sc1\n"
    "s_add_u32 s50, s50, 1\n"
    "s_cmp_gt_u32 s50, 30000\n"
    "s_cbranch_scc1 LS_exit_%=\n"
    "s_waitcnt vmcnt(0)\n"
    "v_cmp_gt_u32 vcc, s52, v171\n"
    "s_cbranch_vccnz LS3_%=_pf0_poll\n"
    "LS3_%=_pf0_ok:\n"
    "s_sub_u32 s49, s96, 1\n"
    "s_and_b32 s99, s49, 1\n"
    "s_lshl_b32 s99, s99, 15\n"
    "s_add_u32 s99, s99, 17408\n"
    "s_min_u32 s49, s49, 63\n"
    "s_lshl_b32 s49, s49, 4\n"
    "s_add_u32 s49, s49, s72\n"
    "s_mul_i32 s49, s49, 28672\n"
    "s_add_u32 s97, s49, 0xed7400\n"
    "s_add_u32 m0, s99, 0\n"
    "s_add_u32 s98, s97, 0\n"
    "buffer_load_dwordx4 v0, s[40:43], s98 offen offset:0 sc1 lds\n"
    "buffer_load_dwordx4 v0, s[40:43], s98 offen offset:1024 sc1 lds\n"
    "buffer_load_dwordx4 v0, s[40:43], s98 offen offset:2048 sc1 lds\n"
    "buffer_load_dwordx4 v0, s[40:43], s98 offen offset:3072 sc1 lds\n"
    "s_add_u32 m0, s99, 4096\n"
    "s_add_u32 s98, s97, 4096\n"
    "buffer_load_dwordx4 v0, s[40:43], s98 offen offset:0 sc1 lds\n"
    "buffer_load_dwordx4 v0, s[40:43], s98 offen offset:1024 sc1 lds\n"
    "buffer_load_dwordx4 v0, s[40:43], s98 offen offset:2048 sc1 lds\n"
    "buffer_load_dwordx4 v0, s[40:43], s98 offen offset:3072 sc1 lds\n"
    "s_add_u32 m0, s99, 8192\n"
    "s_add_u32 s98, s97, 8192\n"
    "buffer_load_dwordx4 v0, s[40:43], s98 offen offset:0 sc1 lds\n"
    "buffer_load_dwordx4 v0, s[40:43], s98 offen offset:1024 sc1 lds\n"
    "buffer_load_dwordx4 v0, s[40:43], s98 offen offset:2048 sc1 lds\n"
    "s_waitcnt vmcnt(0)\n"
    ".p2align 6\n"
    "s_nop 0\n"
    "LS3_%=_top:\n"
    "s_mov_b32 s50, 0\n"
    "s_add_u32 s49, s44, 1\n"
    "v_mov_b32 v15, s49\n"
    "v_mov_b32 v44, 0\n"
    "v_mov_b32 v45, 0\n"
    "v_mov_b32 v46, 0\n"
    "v_mov_b32 v47, 0\n"
    "v_mov_b32 v48, 0\n"
    "v_mov_b32 v49, 0\n"
    "v_mov_b32 v50, 0\n"
    "v_mov_b32 v51, 0\n"
    "v_mov_b32 v52, 0\n"
    "v_mov_b32 v53, 0\n"
    "v_mov_b32 v54, 0\n"
    "v_mov_b32 v55, 0\n"
    "v_mov_b32 v56, 0\n"
    "v_mov_b32 v57, 0\n"
    "v_mov_b32 v58, 0\n"
    "v_mov_b32 v59, 0\n"
    "v_mov_b32 v60, 0\n"
    "v_mov_b32 v61, 0\n"
    "v_mov_b32 v62, 0\n"
    "v_mov_b32 v63, 0\n"
    "v_mov_b32 v64, 0\n"
    "v_mov_b32 v65, 0\n"
    "v_mov_b32 v66, 0\n"
    "v_mov_b32 v67, 0\n"
    "v_mov_b32 v68, 0\n"
    "v_mov_b32 v69, 0\n"
    "v_mov_b32 v70, 0\n"
    "v_mov_b32 v71, 0\n"
    "v_mov_b32 v72, 0\n"
    "v_mov_b32 v73, 0\n"
    "v_mov_b32 v74, 0\n"
    "v_mov_b32 v75, 0\n"
    "s_waitcnt lgkmcnt(0)\n"
    "s_barrier\n"
    "LS3_%=_issue:\n"
    "buffer_load_dwordx4 v[16:19], v0, s[40:43], s69 offen offset:0 sc1\n"
    "ds_read_b128 v[76:79], v5 offset:0\n"
    "ds_read_b128 v[80:83], v5 offset:1024\n"
    "s_nop 8\n"
    "buffer_load_dwordx4 v[20:23], v0, s[40:43], s69 offen offset:1024 sc1\n"
    "ds_read_b128 v[84:87], v5 offset:14336\n"
    "ds_read_b128 v[88:91], v5 offset:15360\n"
    "s_nop 8\n"
    "buffer_load_dwordx4 v[24:27], v0, s[40:43], s69 offen offset:2048 sc1\n"
    "ds_read_b128 v[92:95], v5 offset:2048\n"
    "ds_read_b128 v[96:99], v5 offset:3072\n"
    "s_nop 8\n"
    "buffer_load_dwordx4 v[28:31], v0, s[40:43], s69 offen offset:3072 sc1\n"
    "ds_read_b128 v[100:103], v5 offset:16384\n"
    "ds_read_b128 v[104:107], v5 offset:17408\n"
    "s_nop 8\n"
    "buffer_load_dwordx4 v[32:35], v0, s[40:43], s79 offen offset:0 sc1\n"
    "ds_read_b128 v[108:111], v5 offset:4096\n"
    "ds_read_b128 v[112:115], v5 offset:5120\n"
    "s_nop 8\n"
    "buffer_load_dwordx4 v[36:39], v0, s[40:43], s79 offen offset:1024 sc1\n"
    "ds_read_b128 v[116:119], v5 offset:18432\n"
    "ds_read_b128 v[120:123], v5 offset:19456\n"
    "s_nop 8\n"
    "buffer_load_dwordx4 v[40:43], v0, s[40:43], s79 offen offset:2048 sc1\n"
    "LS3_%=_compute:\n"
    "s_waitcnt vmcnt(6)\n"
    "v_smfmac_f32_16x16x64_bf16 v[44:47], v[16:19], a[0:7], v1\n"
    "v_smfmac_f32_16x16x64_bf16 v[48:51], v[16:19], a[56:63], v1\n"
    "v_smfmac_f32_16x16x64_bf16 v[52:55], v[16:19], a[112:119], v1\n"
    "v_smfmac_f32_16x16x64_bf16 v[56:59], v[16:19], a[168:175], v1\n"
    "v_smfmac_f32_16x16x64_bf16 v[60:63], v[16:19], a[224:231], v1\n"
    "v_smfmac_f32_16x16x64_bf16 v[64:67], v[16:19], v[198:205], v1\n"
    "s_waitcnt lgkmcnt(10)\n"
    "v_smfmac_f32_16x16x64_bf16 v[68:71], v[16:19], v[76:83], v1\n"
    "ds_read_b128 v[76:79], v5 offset:6144\n"
    "ds_read_b128 v[80:83], v5 offset:7168\n"
    "s_waitcnt lgkmcnt(10)\n"
    "v_smfmac_f32_16x16x64_bf16 v[72:75], v[16:19], v[84:91], v1\n"
    "ds_read_b128 v[84:87], v5 offset:8192\n"
    "ds_read_b128 v[88:91], v5 offset:9216\n"
    "s_waitcnt vmcnt(5)\n"
    "v_smfmac_f32_16x16x64_bf16 v[44:47], v[20:23], a[8:15], v1\n"
    "v_smfmac_f32_16x16x64_bf16 v[48:51], v[20:23], a[64:71], v1\n"
    "v_smfmac_f32_16x16x64_bf16 v[52:55], v[20:23], a[120:127], v1\n"
    "v_smfmac_f32_16x16x64_bf16 v[56:59], v[20:23], a[176:183], v1\n"
    "v_smfmac_f32_16x16x64_bf16 v[60:63], v[20:23], a[232:239], v1\n"
    "v_smfmac_f32_16x16x64_bf16 v[64:67], v[20:23], v[206:213], v1\n"
    "s_waitcnt lgkmcnt(10)\n"
    "v_smfmac_f32_16x16x64_bf16 v[68:71], v[20:23], v[92:99], v1\n"
    "ds_read_b128 v[92:95], v5 offset:10240\n"
    "ds_read_b128 v[96:99], v5 offset:11264\n"
    "s_waitcnt lgkmcnt(10)\n"
    "v_smfmac_f32_16x16x64_bf16 v[72:75], v[20:23], v[100:107], v1\n"
    "ds_read_b128 v[100:103], v5 offset:12288\n"
    "ds_read_b128 v[104:107], v5 offset:13312\n"
    "s_waitcnt vmcnt(4)\n"
    "v_smfmac_f32_16x16x64_bf16 v[44:47], v[24:27], a[16:23], v1\n"
    "v_smfmac_f32_16x16x64_bf16 v[48:51], v[24:27], a[72:79], v1\n"
    "v_smfmac_f32_16x16x64_bf16 v[52:55], v[24:27], a[128:135], v1\n"
    "v_smfmac_f32_16x16x64_bf16 v[56:59], v[24:27], a[184:191], v1\n"
    "v_smfmac_f32_16x16x64_bf16 v[60:63], v[24:27], a[240:247], v1\n"
    "v_smfmac_f32_16x16x64_bf16 v[64:67], v[24:27], v[214:221], v1\n"
    "s_waitcnt lgkmcnt(10)\n"
    "v_smfmac_f32_16x16x64_bf16 v[68:71], v[24:27], v[108:115], v1\n"
    "ds_read_b128 v[108:111], v5 offset:20480\n"
    "ds_read_b128 v[112:115], v5 offset:21504\n"
    "s_waitcnt lgkmcnt(10)\n"
    "v_smfmac_f32_16x16x64_bf16 v[72:75], v[24:27], v[116:123], v1\n"
    "ds_read_b128 v[116:119], v5 offset:22528\n"
    "ds_read_b128 v[120:123], v5 offset:23552\n"
    "s_waitcnt vmcnt(3)\n"
    "s_waitcnt lgkmcnt(10)\n"
    "v_smfmac_f32_16x16x64_bf16 v[68:71], v[28:31], v[76:83], v1\n"
    "ds_read_b128 v[76:79], v5 offset:24576\n"
    "ds_read_b128 v[80:83], v5 offset:25600\n"
    "s_waitcnt vmcnt(2)\n"
    "s_waitcnt lgkmcnt(10)\n"
    "v_smfmac_f32_16x16x64_bf16 v[68:71], v[32:35], v[84:91], v1\n"
    "ds_read_b128 v[84:87], v5 offset:26624\n"
    "ds_read_b128 v[88:91], v5 offset:27648\n"
    "s_waitcnt vmcnt(1)\n"
    "s_waitcnt lgkmcnt(10)\n"
    "v_smfmac_f32_16x16x64_bf16 v[68:71], v[36:39], v[92:99], v1\n"
    "s_waitcnt vmcnt(0)\n"
    "s_waitcnt lgkmcnt(8)\n"
    "v_smfmac_f32_16x16x64_bf16 v[68:71], v[40:43], v[100:107], v1\n"
    "s_waitcnt lgkmcnt(6)\n"
    "v_smfmac_f32_16x16x64_bf16 v[72:75], v[28:31], v[108:115], v1\n"
    "s_waitcnt lgkmcnt(4)\n"
    "v_smfmac_f32_16x16x64_bf16 v[72:75], v[32:35], v[116:123], v1\n"
    "v_add_f32 v68, v68, v70\n"
    "s_waitcnt lgkmcnt(2)\n"
    "v_smfmac_f32_16x16x64_bf16 v[72:75], v[36:39], v[76:83], v1\n"
    "v_add_f32 v69, v69, v71\n"
    "s_waitcnt lgkmcnt(0)\n"
    "v_smfmac_f32_16x16x64_bf16 v[72:75], v[40:43], v[84:91], v1\n"
    "v_cmp_u_f32 vcc, v68, v69\n"
    "v_smfmac_f32_16x16x64_bf16 v[44:47], v[28:31], a[24:31], v1\n"
    "s_cbranch_vccnz LS3_%=_nan\n"
    "LS3_%=_good:\n"
    "ds_write_b32 v4, v15\n"
    "v_smfmac_f32_16x16x64_bf16 v[44:47], v[32:35], a[32:39], v1\n"
    "v_cvt_pk_bf16_f32 v68, v68, v69\n"
    "v_smfmac_f32_16x16x64_bf16 v[44:47], v[36:39], a[40:47], v1\n"
    "ds_write_b32 v3, v68 offset:6656\n"
    "v_smfmac_f32_16x16x64_bf16 v[44:47], v[40:43], a[48:55], v1\n"
    "v_add_f32 v72, v72, v74\n"
    "v_smfmac_f32_16x16x64_bf16 v[48:51], v[28:31], a[80:87], v1\n"
    "v_add_f32 v73, v73, v75\n"
    "v_smfmac_f32_16x16x64_bf16 v[48:51], v[32:35], a[88:95], v1\n"
    "v_cvt_pk_bf16_f32 v72, v72, v73\n"
    "v_smfmac_f32_16x16x64_bf16 v[48:51], v[36:39], a[96:103], v1\n"
    "ds_write_b32 v3, v72 offset:6720\n"
    "v_smfmac_f32_16x16x64_bf16 v[48:51], v[40:43], a[104:111], v1\n"
    "v_add_f32 v44, v44, v46\n"
    "v_smfmac_f32_16x16x64_bf16 v[52:55], v[28:31], a[136:143], v1\n"
    "v_add_f32 v45, v45, v47\n"
    "v_smfmac_f32_16x16x64_bf16 v[52:55], v[32:35], a[144:151], v1\n"
    "v_cvt_pk_bf16_f32 v44, v44, v45\n"
    "v_smfmac_f32_16x16x64_bf16 v[52:55], v[36:39], a[152:159], v1\n"
    "ds_write_b32 v3, v44 offset:4608\n"
    "v_smfmac_f32_16x16x64_bf16 v[52:55], v[40:43], a[160:167], v1\n"
    "v_add_f32 v48, v48, v50\n"
    "v_smfmac_f32_16x16x64_bf16 v[56:59], v[28:31], a[192:199], v1\n"
    "v_add_f32 v49, v49, v51\n"
    "v_smfmac_f32_16x16x64_bf16 v[56:59], v[32:35], a[200:207], v1\n"
    "v_cvt_pk_bf16_f32 v48, v48, v49\n"
    "v_smfmac_f32_16x16x64_bf16 v[56:59], v[36:39], a[208:215], v1\n"
    "ds_write_b32 v3, v48 offset:4672\n"
    "v_smfmac_f32_16x16x64_bf16 v[56:59], v[40:43], a[216:223], v1\n"
    "v_add_f32 v52, v52, v54\n"
    "v_smfmac_f32_16x16x64_bf16 v[60:63], v[28:31], a[248:255], v1\n"
    "v_add_f32 v53, v53, v55\n"
    "v_smfmac_f32_16x16x64_bf16 v[60:63], v[32:35], v[174:181], v1\n"
    "v_cvt_pk_bf16_f32 v52, v52, v53\n"
    "v_smfmac_f32_16x16x64_bf16 v[60:63], v[36:39], v[182:189], v1\n"
    "ds_write_b32 v3, v52 offset:5120\n"
    "v_smfmac_f32_16x16x64_bf16 v[60:63], v[40:43], v[190:197], v1\n"
    "v_add_f32 v56, v56, v58\n"
    "v_smfmac_f32_16x16x64_bf16 v[64:67], v[28:31], v[222:229], v1\n"
    "v_add_f32 v57, v57, v59\n"
    "v_smfmac_f32_16x16x64_bf16 v[64:67], v[32:35], v[230:237], v1\n"
    "v_cvt_pk_bf16_f32 v56, v56, v57\n"
    "v_smfmac_f32_16x16x64_bf16 v[64:67], v[36:39], v[238:245], v1\n"
    "ds_write_b32 v3, v56 offset:5184\n"
    "v_smfmac_f32_16x16x64_bf16 v[64:67], v[40:43], v[246:253], v1\n"
    "v_add_f32 v60, v60, v62\n"
    "v_add_f32 v61, v61, v63\n"
    "v_cvt_pk_bf16_f32 v60, v60, v61\n"
    "s_nop 4\n"
    "v_pk_add_f32 v[64:65], v[64:65], v[66:67]\n"
    "v_cvt_pk_bf16_f32 v64, v64, v65\n"
    "ds_write_b32 v3, v60 offset:5632\n"
    "ds_write_b32 v3, v64 offset:5696\n"
    "s_waitcnt lgkmcnt(0)\n"
    "s_barrier\n"
    "s_barrier\n"
    "s_add_u32 s52, s96, 1\n"
    "s_add_u32 s48, s44, 2\n"
    "s_cmp_lt_u32 s96, s48\n"
    "s_cbranch_scc1 LS3_%=_late\n"
    "LS3_%=_early:\n"
    "v_cmp_gt_u32 vcc, s52, v171\n"
    "s_cbranch_vccnz LS3_%=_none\n"
    "s_add_u32 s96, s96, 1\n"
    "s_sub_u32 s49, s96, 1\n"
    "s_and_b32 s99, s49, 1\n"
    "s_lshl_b32 s99, s99, 15\n"
    "s_add_u32 s99, s99, 17408\n"
    "s_min_u32 s49, s49, 63\n"
    "s_lshl_b32 s49, s49, 4\n"
    "s_add_u32 s49, s49, s72\n"
    "s_mul_i32 s49, s49, 28672\n"
    "s_add_u32 s97, s49, 0xed7400\n"
    "s_add_u32 m0, s99, 0\n"
    "s_add_u32 s98, s97, 0\n"
    "buffer_load_dwordx4 v0, s[40:43], s98 offen offset:0 sc1 lds\n"
    "buffer_load_dwordx4 v0, s[40:43], s98 offen offset:1024 sc1 lds\n"
    "buffer_load_dwordx4 v0, s[40:43], s98 offen offset:2048 sc1 lds\n"
    "buffer_load_dwordx4 v0, s[40:43], s98 offen offset:3072 sc1 lds\n"
    "s_add_u32 m0, s99, 4096\n"
    "s_add_u32 s98, s97, 4096\n"
    "buffer_load_dwordx4 v0, s[40:43], s98 offen offset:0 sc1 lds\n"
    "buffer_load_dwordx4 v0, s[40:43], s98 offen offset:1024 sc1 lds\n"
    "buffer_load_dwordx4 v0, s[40:43], s98 offen offset:2048 sc1 lds\n"
    "buffer_load_dwordx4 v0, s[40:43], s98 offen offset:3072 sc1 lds\n"
    "s_add_u32 m0, s99, 8192\n"
    "s_add_u32 s98, s97, 8192\n"
    "buffer_load_dwordx4 v0, s[40:43], s98 offen offset:0 sc1 lds\n"
    "buffer_load_dwordx4 v0, s[40:43], s98 offen offset:1024 sc1 lds\n"
    "buffer_load_dwordx4 v0, s[40:43], s98 offen offset:2048 sc1 lds\n"
    "LS3_%=_none:\n"
    "buffer_load_dword v171, v170, s[40:43], 0 offen sc1\n"
    "s_cmp_eq_u32 s44, 63\n"
    "s_cbranch_scc1 LS_exit_%=\n"
    "s_add_u32 s44, s44, 1\n"
    "s_mov_b32 s49, s54\n"
    "s_mov_b32 s53, s55\n"
    "s_mov_b32 s54, s56\n"
    "s_mov_b32 s55, s57\n"
    "s_mov_b32 s56, s58\n"
    "s_mov_b32 s57, s59\n"
    "s_mov_b32 s58, s49\n"
    "s_mov_b32 s59, s53\n"
    "s_mov_b32 s69, s54\n"
    "s_mov_b32 s79, s55\n"
    "s_mov_b32 s49, s60\n"
    "s_mov_b32 s60, s63\n"
    "s_mov_b32 s63, s66\n"
    "s_mov_b32 s66, s49\n"
    "s_mov_b32 s49, s61\n"
    "s_mov_b32 s61, s64\n"
    "s_mov_b32 s64, s67\n"
    "s_mov_b32 s67, s49\n"
    "s_mov_b32 s49, s62\n"
    "s_mov_b32 s62, s65\n"
    "s_mov_b32 s65, s68\n"
    "s_mov_b32 s68, s49\n"
    "s_branch LS3_%=_top\n"
    "LS3_%=_late:\n"
    "s_min_u32 s52, s52, 64\n"
    "v_cmp_gt_u32 vcc, s52, v171\n"
    "s_cbranch_vccz LS3_%=_f_ok\n"
    "s_mov_b32 s50, 0\n"
    "LS3_%=_f_poll:\n"
    "buffer_load_dword v171, v170, s[40:43], 0 offen sc1\n"
    "s_add_u32 s50, s50, 1\n"
    "s_cmp_gt_u32 s50, 30000\n"
    "s_cbranch_scc1 LS_exit_%=\n"
    "s_waitcnt vmcnt(0)\n"
    "v_cmp_gt_u32 vcc, s52, v171\n"
    "s_cbranch_vccnz LS3_%=_f_poll\n"
    "LS3_%=_f_ok:\n"
    "s_add_u32 s96, s96, 1\n"
    "s_sub_u32 s49, s96, 1\n"
    "s_and_b32 s99, s49, 1\n"
    "s_lshl_b32 s99, s99, 15\n"
    "s_add_u32 s99, s99, 17408\n"
    "s_min_u32 s49, s49, 63\n"
    "s_lshl_b32 s49, s49, 4\n"
    "s_add_u32 s49, s49, s72\n"
    "s_mul_i32 s49, s49, 28672\n"
    "s_add_u32 s97, s49, 0xed7400\n"
    "s_add_u32 m0, s99, 0\n"
    "s_add_u32 s98, s97, 0\n"
    "buffer_load_dwordx4 v0, s[40:43], s98 offen offset:0 sc1 lds\n"
    "buffer_load_dwordx4 v0, s[40:43], s98 offen offset:1024 sc1 lds\n"
    "buffer_load_dwordx4 v0, s[40:43], s98 offen offset:2048 sc1 lds\n"
    "buffer_load_dwordx4 v0, s[40:43], s98 offen offset:3072 sc1 lds\n"
    "s_add_u32 m0, s99, 4096\n"
    "s_add_u32 s98, s97, 4096\n"
    "buffer_load_dwordx4 v0, s[40:43], s98 offen offset:0 sc1 lds\n"
    "buffer_load_dwordx4 v0, s[40:43], s98 offen offset:1024 sc1 lds\n"
    "buffer_load_dwordx4 v0, s[40:43], s98 offen offset:2048 sc1 lds\n"
    "buffer_load_dwordx4 v0, s[40:43], s98 offen offset:3072 sc1 lds\n"
    "s_add_u32 m0, s99, 8192\n"
    "s_add_u32 s98, s97, 8192\n"
    "buffer_load_dwordx4 v0, s[40:43], s98 offen offset:0 sc1 lds\n"
    "buffer_load_dwordx4 v0, s[40:43], s98 offen offset:1024 sc1 lds\n"
    "buffer_load_dwordx4 v0, s[40:43], s98 offen offset:2048 sc1 lds\n"
    "s_waitcnt vmcnt(0)\n"
    "s_add_u32 s52, s96, 1\n"
    "s_branch LS3_%=_early\n"
    "LS3_%=_nan:\n"
    "v_mov_b32 v2, 0\n"
    "v_max3_u32 v2, v16, v17, v2\n"
    "v_max3_u32 v2, v18, v19, v2\n"
    "v_max3_u32 v2, v20, v21, v2\n"
    "v_max3_u32 v2, v22, v23, v2\n"
    "v_max3_u32 v2, v24, v25, v2\n"
    "v_max3_u32 v2, v26, v27, v2\n"
    "v_max3_u32 v2, v28, v29, v2\n"
    "v_max3_u32 v2, v30, v31, v2\n"
    "v_max3_u32 v2, v32, v33, v2\n"
    "v_max3_u32 v2, v34, v35, v2\n"
    "v_max3_u32 v2, v36, v37, v2\n"
    "v_max3_u32 v2, v38, v39, v2\n"
    "v_max3_u32 v2, v40, v41, v2\n"
    "v_max3_u32 v2, v42, v43, v2\n"
    "v_cmp_eq_u32 vcc, -1, v2\n"
    "s_cbranch_vccz LS3_%=_good\n"
    "LS3_%=_again:\n"
    "s_nop 7\n"
    "buffer_load_dwordx4 v[16:19], v0, s[40:43], s69 offen offset:0 sc1\n"
    "buffer_load_dwordx4 v[20:23], v0, s[40:43], s69 offen offset:1024 sc1\n"
    "buffer_load_dwordx4 v[24:27], v0, s[40:43], s69 offen offset:2048 sc1\n"
    "buffer_load_dwordx4 v[28:31], v0, s[40:43], s69 offen offset:3072 sc1\n"
    "buffer_load_dwordx4 v[32:35], v0, s[40:43], s79 offen offset:0 sc1\n"
    "buffer_load_dwordx4 v[36:39], v0, s[40:43], s79 offen offset:1024 sc1\n"
    "buffer_load_dwordx4 v[40:43], v0, s[40:43], s79 offen offset:2048 sc1\n"
    "s_add_u32 s50, s50, 1\n"
    "s_add_u32 s75, s75, 1\n"
    "s_cmp_gt_u32 s50, 30000\n"
    "s_cbranch_scc1 LS_exit_%=\n"
    "s_waitcnt lgkmcnt(0)\n"
    "ds_read_b128 v[76:79], v5 offset:0\n"
    "ds_read_b128 v[80:83], v5 offset:1024\n"
    "ds_read_b128 v[84:87], v5 offset:14336\n"
    "ds_read_b128 v[88:91], v5 offset:15360\n"
    "ds_read_b128 v[92:95], v5 offset:2048\n"
    "ds_read_b128 v[96:99], v5 offset:3072\n"
    "ds_read_b128 v[100:103], v5 offset:16384\n"
    "ds_read_b128 v[104:107], v5 offset:17408\n"
    "ds_read_b128 v[108:111], v5 offset:4096\n"
    "ds_read_b128 v[112:115], v5 offset:5120\n"
    "ds_read_b128 v[116:119], v5 offset:18432\n"
    "ds_read_b128 v[120:123], v5 offset:19456\n"
    "v_mov_b32 v44, 0\n"
    "v_mov_b32 v45, 0\n"
    "v_mov_b32 v46, 0\n"
    "v_mov_b32 v47, 0\n"
    "v_mov_b32 v48, 0\n"
    "v_mov_b32 v49, 0\n"
    "v_mov_b32 v50, 0\n"
    "v_mov_b32 v51, 0\n"
    "v_mov_b32 v52, 0\n"
    "v_mov_b32 v53, 0\n"
    "v_mov_b32 v54, 0\n"
    "v_mov_b32 v55, 0\n"
    "v_mov_b32 v56, 0\n"
    "v_mov_b32 v57, 0\n"
    "v_mov_b32 v58, 0\n"
    "v_mov_b32 v59, 0\n"
    "v_mov_b32 v60, 0\n"
    "v_mov_b32 v61, 0\n"
    "v_mov_b32 v62, 0\n"
    "v_mov_b32 v63, 0\n"
    "v_mov_b32 v64, 0\n"
    "v_mov_b32 v65, 0\n"
    "v_mov_b32 v66, 0\n"
    "v_mov_b32 v67, 0\n"
    "v_mov_b32 v68, 0\n"
    "v_mov_b32 v69, 0\n"
    "v_mov_b32 v70, 0\n"
    "v_mov_b32 v71, 0\n"
    "v_mov_b32 v72, 0\n"
    "v_mov_b32 v73, 0\n"
    "v_mov_b32 v74, 0\n"
    "v_mov_b32 v75, 0\n"
    "s_waitcnt vmcnt(0)\n"
    "v_mov_b32 v2, 0\n"
    "v_max3_u32 v2, v16, v17, v2\n"
    "v_max3_u32 v2, v18, v19, v2\n"
    "v_max3_u32 v2, v20, v21, v2\n"
    "v_max3_u32 v2, v22, v23, v2\n"
    "v_max3_u32 v2, v24, v25, v2\n"
    "v_max3_u32 v2, v26, v27, v2\n"
    "v_max3_u32 v2, v28, v29, v2\n"
    "v_max3_u32 v2, v30, v31, v2\n"
    "v_max3_u32 v2, v32, v33, v2\n"
    "v_max3_u32 v2, v34, v35, v2\n"
    "v_max3_u32 v2, v36, v37, v2\n"
    "v_max3_u32 v2, v38, v39, v2\n"
    "v_max3_u32 v2, v40, v41, v2\n"
    "v_max3_u32 v2, v42, v43, v2\n"
    "v_cmp_eq_u32 vcc, -1, v2\n"
    "s_cbranch_vccnz LS3_%=_again\n"
    "s_branch LS3_%=_compute\n"
    "LS_exit_%=:\n"
    "s_endpgm\n"
            :: "s"(wlo), "s"(whi), "s"(r), "s"(w), "s"(p.ws0), "s"(p.ws1),
               "s"(p.ws_bytes), "s"(ring)
            : CHAIN_CLOBBERS);
            __builtin_unreachable();
        } else {
            asm volatile(
    "s_mov_b32 s40, %0\n"
    "s_and_b32 s41, %1, 0xffff\n"
    "s_mov_b32 s42, %6\n"
    "s_mov_b32 s43, 0x00020000\n"
    "s_mov_b32 s72, %2\n"
    "s_mov_b32 s73, %4\n"
    "s_mov_b32 s74, %5\n"
    "s_mov_b32 s44, 0\n"
    "s_mov_b32 s75, 0\n"
    "s_mov_b32 s69, 0x10000\n"
    "s_mov_b32 s79, 0x11000\n"
    "v_mbcnt_lo_u32_b32 v10, -1, 0\n"
    "v_mbcnt_hi_u32_b32 v10, -1, v10\n"
    "v_lshlrev_b32 v0, 4, v10\n"
    "v_and_b32 v11, 2, v10\n"
    "v_cmp_eq_u32 vcc, 0, v11\n"
    "v_mov_b32 v12, 0xeeee\n"
    "v_mov_b32 v13, 0x4444\n"
    "v_cndmask_b32 v1, v12, v13, vcc\n"
    "s_cmp_eq_u32 %3, 1\n"
    "s_cbranch_scc1 LV1_%=_start\n"
    "s_cmp_eq_u32 %3, 2\n"
    "s_cbranch_scc1 LV2_%=_start\n"
    "s_cmp_eq_u32 %3, 3\n"
    "s_cbranch_scc1 LV3_%=_start\n"
    "LV0_%=_start:\n"
    "v_and_b32 v11, 15, v10\n"
    "v_lshrrev_b32 v12, 4, v10\n"
    "v_lshrrev_b32 v2, 1, v12\n"
    "v_lshlrev_b32 v3, 8, v2\n"
    "v_lshl_add_u32 v3, v11, 4, v3\n"
    "v_and_b32 v2, 1, v12\n"
    "v_lshl_add_u32 v3, v2, 3, v3\n"
    "v_add_u32 v3, 0x0, v3\n"
    "v_and_b32 v2, 7, v11\n"
    "v_lshlrev_b32 v4, 5, v2\n"
    "v_lshl_add_u32 v4, v12, 3, v4\n"
    "v_lshrrev_b32 v2, 3, v11\n"
    "v_lshl_add_u32 v4, v2, 2, v4\n"
    "v_add_u32 v4, 0x1000, v4\n"
    "v_lshlrev_b32 v5, 3, v11\n"
    "v_add_u32 v5, 0x1000, v5\n"
    "v_lshlrev_b32 v8, 8, v12\n"
    "v_and_b32 v2, 2, v11\n"
    "v_lshl_add_u32 v8, v2, 6, v8\n"
    "v_lshrrev_b32 v2, 2, v11\n"
    "v_lshl_add_u32 v8, v2, 5, v8\n"
    "v_and_b32 v2, 1, v11\n"
    "v_lshl_add_u32 v8, v2, 4, v8\n"
    "s_and_b32 s49, s72, 15\n"
    "s_lshl_b32 s49, s49, 8\n"
    "v_add_u32 v9, s49, v0\n"
    "v_mov_b32 v2, 0x7fffff00\n"
    "v_cmp_gt_u32 vcc, 16, v10\n"
    "s_nop 1\n"
    "v_cndmask_b32 v9, v2, v9, vcc\n"
    "v_mov_b32 v192, -1\n"
    "v_mov_b32 v193, -1\n"
    "v_mov_b32 v194, -1\n"
    "v_mov_b32 v195, -1\n"
    "v_add_u32 v7, 0x8000, v0\n"
    "s_cmp_eq_u32 s72, 0\n"
    "s_cselect_b32 s49, 0, 0x7fffff00\n"
    "v_mov_b32 v13, 0x7fffff00\n"
    "v_mov_b32 v2, s49\n"
    "v_cmp_eq_u32 vcc, 0, v10\n"
    "s_nop 1\n"
    "v_cndmask_b32 v13, v13, v2, vcc\n"
    "v_lshlrev_b32 v14, 7, v11\n"
    "s_mul_i32 s49, s72, 14336\n"
    "s_add_u32 s49, s49, 0x6f3000\n"
    "buffer_load_dwordx4 a[0:3], v0, s[40:43], s49 offen offset:0\n"
    "buffer_load_dwordx4 a[4:7], v0, s[40:43], s49 offen offset:1024\n"
    "buffer_load_dwordx4 a[8:11], v0, s[40:43], s49 offen offset:2048\n"
    "buffer_load_dwordx4 a[12:15], v0, s[40:43], s49 offen offset:3072\n"
    "s_add_u32 s49, s49, 4096\n"
    "buffer_load_dwordx4 a[16:19], v0, s[40:43], s49 offen offset:0\n"
    "buffer_load_dwordx4 a[20:23], v0, s[40:43], s49 offen offset:1024\n"
    "buffer_load_dwordx4 a[24:27], v0, s[40:43], s49 offen offset:2048\n"
    "buffer_load_dwordx4 a[28:31], v0, s[40:43], s49 offen offset:3072\n"
    "s_add_u32 s49, s49, 4096\n"
    "buffer_load_dwordx4 a[32:35], v0, s[40:43], s49 offen offset:0\n"
    "buffer_load_dwordx4 a[36:39], v0, s[40:43], s49 offen offset:1024\n"
    "buffer_load_dwordx4 a[40:43], v0, s[40:43], s49 offen offset:2048\n"
    "buffer_load_dwordx4 a[44:47], v0, s[40:43], s49 offen offset:3072\n"
    "s_add_u32 s49, s49, 4096\n"
    "buffer_load_dwordx4 a[48:51], v0, s[40:43], s49 offen offset:0\n"
    "buffer_load_dwordx4 a[52:55], v0, s[40:43], s49 offen offset:1024\n"
    "s_mul_i32 s49, s72, 14336\n"
    "s_add_u32 s49, s49, 0x755000\n"
    "buffer_load_dwordx4 a[56:59], v0, s[40:43], s49 offen offset:0\n"
    "buffer_load_dwordx4 a[60:63], v0, s[40:43], s49 offen offset:1024\n"
    "buffer_load_dwordx4 a[64:67], v0, s[40:43], s49 offen offset:2048\n"
    "buffer_load_dwordx4 a[68:71], v0, s[40:43], s49 offen offset:3072\n"
    "s_add_u32 s49, s49, 4096\n"
    "buffer_load_dwordx4 a[72:75], v0, s[40:43], s49 offen offset:0\n"
    "buffer_load_dwordx4 a[76:79], v0, s[40:43], s49 offen offset:1024\n"
    "buffer_load_dwordx4 a[80:83], v0, s[40:43], s49 offen offset:2048\n"
    "buffer_load_dwordx4 a[84:87], v0, s[40:43], s49 offen offset:3072\n"
    "s_add_u32 s49, s49, 4096\n"
    "buffer_load_dwordx4 a[88:91], v0, s[40:43], s49 offen offset:0\n"
    "buffer_load_dwordx4 a[92:95], v0, s[40:43], s49 offen offset:1024\n"
    "buffer_load_dwordx4 a[96:99], v0, s[40:43], s49 offen offset:2048\n"
    "buffer_load_dwordx4 a[100:103], v0, s[40:43], s49 offen offset:3072\n"
    "s_add_u32 s49, s49, 4096\n"
    "buffer_load_dwordx4 a[104:107], v0, s[40:43], s49 offen offset:0\n"
    "buffer_load_dwordx4 a[108:111], v0, s[40:43], s49 offen offset:1024\n"
    "s_mul_i32 s49, s72, 14336\n"
    "s_add_u32 s49, s49, 0x7b7000\n"
    "buffer_load_dwordx4 a[112:115], v0, s[40:43], s49 offen offset:0\n"
    "buffer_load_dwordx4 a[116:119], v0, s[40:43], s49 offen offset:1024\n"
    "buffer_load_dwordx4 a[120:123], v0, s[40:43], s49 offen offset:2048\n"
    "buffer_load_dwordx4 a[124:127], v0, s[40:43], s49 offen offset:3072\n"
    "s_add_u32 s49, s49, 4096\n"
    "buffer_load_dwordx4 a[128:131], v0, s[40:43], s49 offen offset:0\n"
    "buffer_load_dwordx4 a[132:135], v0, s[40:43], s49 offen offset:1024\n"
    "buffer_load_dwordx4 a[136:139], v0, s[40:43], s49 offen offset:2048\n"
    "buffer_load_dwordx4 a[140:143], v0, s[40:43], s49 offen offset:3072\n"
    "s_add_u32 s49, s49, 4096\n"
    "buffer_load_dwordx4 a[144:147], v0, s[40:43], s49 offen offset:0\n"
    "buffer_load_dwordx4 a[148:151], v0, s[40:43], s49 offen offset:1024\n"
    "buffer_load_dwordx4 a[152:155], v0, s[40:43], s49 offen offset:2048\n"
    "buffer_load_dwordx4 a[156:159], v0, s[40:43], s49 offen offset:3072\n"
    "s_add_u32 s49, s49, 4096\n"
    "buffer_load_dwordx4 a[160:163], v0, s[40:43], s49 offen offset:0\n"
    "buffer_load_dwordx4 a[164:167], v0, s[40:43], s49 offen offset:1024\n"
    "s_mul_i32 s49, s72, 14336\n"
    "s_add_u32 s49, s49, 0x819000\n"
    "buffer_load_dwordx4 a[168:171], v0, s[40:43], s49 offen offset:0\n"
    "buffer_load_dwordx4 a[172:175], v0, s[40:43], s49 offen offset:1024\n"
    "buffer_load_dwordx4 a[176:179], v0, s[40:43], s49 offen offset:2048\n"
    "buffer_load_dwordx4 a[180:183], v0, s[40:43], s49 offen offset:3072\n"
    "s_add_u32 s49, s49, 4096\n"
    "buffer_load_dwordx4 a[184:187], v0, s[40:43], s49 offen offset:0\n"
    "buffer_load_dwordx4 a[188:191], v0, s[40:43], s49 offen offset:1024\n"
    "buffer_load_dwordx4 a[192:195], v0, s[40:43], s49 offen offset:2048\n"
    "buffer_load_dwordx4 a[196:199], v0, s[40:43], s49 offen offset:3072\n"
    "s_add_u32 s49, s49, 4096\n"
    "buffer_load_dwordx4 a[200:203], v0, s[40:43], s49 offen offset:0\n"
    "buffer_load_dwordx4 a[204:207], v0, s[40:43], s49 offen offset:1024\n"
    "buffer_load_dwordx4 a[208:211], v0, s[40:43], s49 offen offset:2048\n"
    "buffer_load_dwordx4 a[212:215], v0, s[40:43], s49 offen offset:3072\n"
    "s_add_u32 s49, s49, 4096\n"
    "buffer_load_dwordx4 a[216:219], v0, s[40:43], s49 offen offset:0\n"
    "buffer_load_dwordx4 a[220:223], v0, s[40:43], s49 offen offset:1024\n"
    "v_min_u32 v168, 4, v10\n"
    "v_lshlrev_b32 v168, 2, v168\n"
    "v_add_u32 v168, 0xf800, v168\n"
    "s_mov_b32 s50, 0\n"
    "LV0_%=_pt:\n"
    "buffer_load_dword v169, v168, s[40:43], 0 offen sc1\n"
    "s_add_u32 s50, s50, 1\n"
    "s_cmp_gt_u32 s50, 30000\n"
    "s_cbranch_scc1 LV_exit_%=\n"
    "s_waitcnt vmcnt(0)\n"
    "v_cmp_eq_u32 vcc, -1, v169\n"
    "s_cbranch_vccnz LV0_%=_pt\n"
    "v_readlane_b32 s49, v169, 0\n"
    "s_nop 1\n"
    "s_bfe_u32 s39, s49, 0x70000\n"
    "s_lshl_b32 s39, s39, 12\n"
    "s_add_u32 s39, s39, %7\n"
    "s_bfe_u32 s54, s49, 0x70008\n"
    "s_lshl_b32 s54, s54, 12\n"
    "s_add_u32 s54, s54, %7\n"
    "s_bfe_u32 s55, s49, 0x70010\n"
    "s_lshl_b32 s55, s55, 12\n"
    "s_add_u32 s55, s55, %7\n"
    "s_bfe_u32 s56, s49, 0x70018\n"
    "s_lshl_b32 s56, s56, 12\n"
    "s_add_u32 s56, s56, %7\n"
    "v_readlane_b32 s49, v169, 1\n"
    "s_nop 1\n"
    "s_bfe_u32 s57, s49, 0x70000\n"
    "s_lshl_b32 s57, s57, 12\n"
    "s_add_u32 s57, s57, %7\n"
    "s_bfe_u32 s58, s49, 0x70008\n"
    "s_lshl_b32 s58, s58, 12\n"
    "s_add_u32 s58, s58, %7\n"
    "s_bfe_u32 s59, s49, 0x70010\n"
    "s_lshl_b32 s59, s59, 12\n"
    "s_add_u32 s59, s59, %7\n"
    "s_bfe_u32 s60, s49, 0x70018\n"
    "s_lshl_b32 s60, s60, 12\n"
    "s_add_u32 s60, s60, %7\n"
    "v_readlane_b32 s49, v169, 2\n"
    "s_nop 1\n"
    "s_bfe_u32 s61, s49, 0x70000\n"
    "s_lshl_b32 s61, s61, 12\n"
    "s_add_u32 s61, s61, %7\n"
    "s_bfe_u32 s62, s49, 0x70008\n"
    "s_lshl_b32 s62, s62, 12\n"
    "s_add_u32 s62, s62, %7\n"
    "s_bfe_u32 s63, s49, 0x70010\n"
    "s_lshl_b32 s63, s63, 12\n"
    "s_add_u32 s63, s63, %7\n"
    "s_bfe_u32 s64, s49, 0x70018\n"
    "s_lshl_b32 s64, s64, 12\n"
    "s_add_u32 s64, s64, %7\n"
    "v_readlane_b32 s49, v169, 3\n"
    "s_nop 1\n"
    "s_bfe_u32 s65, s49, 0x70000\n"
    "s_lshl_b32 s65, s65, 12\n"
    "s_add_u32 s65, s65, %7\n"
    "s_bfe_u32 s66, s49, 0x70008\n"
    "s_lshl_b32 s66, s66, 12\n"
    "s_add_u32 s66, s66, %7\n"
    "s_bfe_u32 s67, s49, 0x70010\n"
    "s_lshl_b32 s67, s67, 12\n"
    "s_add_u32 s67, s67, %7\n"
    "s_bfe_u32 s68, s49, 0x70018\n"
    "s_lshl_b32 s68, s68, 12\n"
    "s_add_u32 s68, s68, %7\n"
    "v_readlane_b32 s49, v169, 4\n"
    "s_nop 1\n"
    "s_bfe_u32 s77, s49, 0x70000\n"
    "s_lshl_b32 s77, s77, 12\n"
    "s_add_u32 s77, s77, %7\n"
    "s_bfe_u32 s78, s49, 0x70008\n"
    "s_lshl_b32 s78, s78, 12\n"
    "s_add_u32 s78, s78, %7\n"
    "s_bfe_u32 s48, s49, 0x70010\n"
    "s_lshl_b32 s48, s48, 12\n"
    "s_add_u32 s48, s48, %7\n"
    "s_cmp_eq_u32 s72, 27\n"
    "s_cselect_b32 s49, 16, 0\n"
    "v_lshlrev_b32 v203, 8, v10\n"
    "v_add_u32 v203, s78, v203\n"
    "v_mov_b32 v2, 0x7fffff00\n"
    "v_cmp_gt_u32 vcc, s49, v10\n"
    "s_nop 1\n"
    "v_cndmask_b32 v203, v2, v203, vcc\n"
    "s_cmp_lt_u32 s72, 16\n"
    "s_cselect_b32 s39, s39, s48\n"
    "s_and_b32 s49, s72, 15\n"
    "s_lshl_b32 s49, s49, 8\n"
    "s_add_u32 s49, s49, s39\n"
    "s_add_u32 s34, %0, s49\n"
    "s_addc_u32 s35, %1, 0\n"
    "s_mov_b32 s28, -2\n"
    "s_mov_b32 s29, -2\n"
    "s_mov_b32 s30, -2\n"
    "s_mov_b32 s31, -2\n"
    "s_mov_b32 s33, 1\n"
    "s_barrier\n"
    "s_memtime s[46:47]\n"
    "s_waitcnt lgkmcnt(0)\n"
    "s_mov_b32 s45, s46\n"
    "s_mov_b32 s51, s46\n"
    ".p2align 6\n"
    "s_nop 0\n"
    "LV0_%=_top:\n"
    "ds_read_b128 v[64:67], v7 offset:0\n"
    "ds_read_b128 v[68:71], v7 offset:1024\n"
    "ds_read_b128 v[72:75], v7 offset:2048\n"
    "ds_read_b128 v[76:79], v7 offset:3072\n"
    "ds_read_b128 v[80:83], v7 offset:4096\n"
    "ds_read_b128 v[84:87], v7 offset:5120\n"
    "ds_read_b128 v[88:91], v7 offset:6144\n"
    "ds_read_b128 v[92:95], v7 offset:7168\n"
    "ds_read_b128 v[96:99], v7 offset:8192\n"
    "ds_read_b128 v[100:103], v7 offset:9216\n"
    "ds_read_b128 v[104:107], v7 offset:10240\n"
    "ds_read_b128 v[108:111], v7 offset:11264\n"
    "ds_read_b128 v[112:115], v7 offset:12288\n"
    "ds_read_b128 v[116:119], v7 offset:13312\n"
    "ds_read_b128 v[120:123], v7 offset:14336\n"
    "ds_read_b128 v[124:127], v7 offset:15360\n"
    "v_xor_b32 v7, 0x4000, v7\n"
    "s_mov_b32 s50, 0\n"
    "LV0_%=_mpoll:\n"
    "v_mov_b32 v44, 0\n"
    "v_mov_b32 v45, 0\n"
    "v_mov_b32 v46, 0\n"
    "v_mov_b32 v47, 0\n"
    "v_mov_b32 v48, 0\n"
    "v_mov_b32 v49, 0\n"
    "v_mov_b32 v50, 0\n"
    "v_mov_b32 v51, 0\n"
    "v_mov_b32 v52, 0\n"
    "v_mov_b32 v53, 0\n"
    "v_mov_b32 v54, 0\n"
    "v_mov_b32 v55, 0\n"
    "v_mov_b32 v56, 0\n"
    "v_mov_b32 v57, 0\n"
    "v_mov_b32 v58, 0\n"
    "v_mov_b32 v59, 0\n"
    "v_mov_b32 v184, 0\n"
    "v_mov_b32 v185, 0\n"
    "v_mov_b32 v186, 0\n"
    "v_mov_b32 v187, 0\n"
    "v_mov_b32 v15, s44\n"
    "s_mov_b32 s78, s44\n"
    "s_barrier\n"
    "ds_read_b128 v[16:19], v0 offset:8192\n"
    "ds_read_b128 v[20:23], v0 offset:9216\n"
    "ds_read_b128 v[24:27], v0 offset:10240\n"
    "ds_read_b128 v[28:31], v0 offset:11264\n"
    "ds_read_b128 v[32:35], v0 offset:12288\n"
    "ds_read_b128 v[36:39], v0 offset:13312\n"
    "ds_read_b128 v[40:43], v0 offset:14336\n"
    "s_cmp_lt_u32 s72, 16\n"
    "s_cselect_b32 s49, s58, s59\n"
    "s_cselect_b32 s36, s56, s57\n"
    "s_waitcnt lgkmcnt(6)\n"
    "v_smfmac_f32_16x16x64_bf16 v[44:47], v[16:19], a[0:7], v1\n"
    "s_waitcnt lgkmcnt(5)\n"
    "v_smfmac_f32_16x16x64_bf16 v[44:47], v[20:23], a[8:15], v1\n"
    "s_waitcnt lgkmcnt(4)\n"
    "v_smfmac_f32_16x16x64_bf16 v[44:47], v[24:27], a[16:23], v1\n"
    "s_waitcnt lgkmcnt(3)\n"
    "v_smfmac_f32_16x16x64_bf16 v[44:47], v[28:31], a[24:31], v1\n"
    "s_waitcnt lgkmcnt(2)\n"
    "v_smfmac_f32_16x16x64_bf16 v[44:47], v[32:35], a[32:39], v1\n"
    "s_waitcnt lgkmcnt(1)\n"
    "v_smfmac_f32_16x16x64_bf16 v[44:47], v[36:39], a[40:47], v1\n"
    "s_waitcnt lgkmcnt(0)\n"
    "v_smfmac_f32_16x16x64_bf16 v[44:47], v[40:43], a[48:55], v1\n"
    "v_smfmac_f32_16x16x64_bf16 v[48:51], v[16:19], a[56:63], v1\n"
    "v_smfmac_f32_16x16x64_bf16 v[48:51], v[20:23], a[64:71], v1\n"
    "v_smfmac_f32_16x16x64_bf16 v[48:51], v[24:27], a[72:79], v1\n"
    "v_add_f32 v44, v44, v46\n"
    "v_smfmac_f32_16x16x64_bf16 v[48:51], v[28:31], a[80:87], v1\n"
    "v_add_f32 v45, v45, v47\n"
    "v_smfmac_f32_16x16x64_bf16 v[48:51], v[32:35], a[88:95], v1\n"
    "v_cmp_u_f32 vcc, v44, v45\n"
    "v_smfmac_f32_16x16x64_bf16 v[48:51], v[36:39], a[96:103], v1\n"
    "s_cbranch_vccnz LV0_%=_mnan\n"
    "LV0_%=_mok:\n"
    "v_smfmac_f32_16x16x64_bf16 v[48:51], v[40:43], a[104:111], v1\n"
    "v_cvt_pk_bf16_f32 v44, v44, v45\n"
    "v_smfmac_f32_16x16x64_bf16 v[52:55], v[16:19], a[112:119], v1\n"
    "ds_write_b32 v3, v44 offset:0\n"
    "v_smfmac_f32_16x16x64_bf16 v[52:55], v[20:23], a[120:127], v1\n"
    "buffer_store_dwordx4 v[192:195], v9, s[40:43], s49 offen sc1\n"
    "v_smfmac_f32_16x16x64_bf16 v[52:55], v[24:27], a[128:135], v1\n"
    "v_add_f32 v48, v48, v50\n"
    "v_smfmac_f32_16x16x64_bf16 v[52:55], v[28:31], a[136:143], v1\n"
    "v_add_f32 v49, v49, v51\n"
    "v_smfmac_f32_16x16x64_bf16 v[52:55], v[32:35], a[144:151], v1\n"
    "v_cvt_pk_bf16_f32 v48, v48, v49\n"
    "v_smfmac_f32_16x16x64_bf16 v[52:55], v[36:39], a[152:159], v1\n"
    "ds_write_b32 v3, v48 offset:4\n"
    "v_smfmac_f32_16x16x64_bf16 v[52:55], v[40:43], a[160:167], v1\n"
    "v_smfmac_f32_16x16x64_bf16 v[56:59], v[16:19], a[168:175], v1\n"
    "v_smfmac_f32_16x16x64_bf16 v[56:59], v[20:23], a[176:183], v1\n"
    "v_smfmac_f32_16x16x64_bf16 v[56:59], v[24:27], a[184:191], v1\n"
    "v_add_f32 v52, v52, v54\n"
    "v_smfmac_f32_16x16x64_bf16 v[56:59], v[28:31], a[192:199], v1\n"
    "v_add_f32 v53, v53, v55\n"
    "v_smfmac_f32_16x16x64_bf16 v[56:59], v[32:35], a[200:207], v1\n"
    "v_cvt_pk_bf16_f32 v52, v52, v53\n"
    "v_smfmac_f32_16x16x64_bf16 v[56:59], v[36:39], a[208:215], v1\n"
    "ds_write_b32 v3, v52 offset:512\n"
    "v_smfmac_f32_16x16x64_bf16 v[56:59], v[40:43], a[216:223], v1\n"
    "s_nop 7\n"
    "v_add_f32 v56, v56, v58\n"
    "v_add_f32 v57, v57, v59\n"
    "v_cvt_pk_bf16_f32 v56, v56, v57\n"
    "ds_write_b32 v3, v56 offset:516\n"
    "s_barrier\n"
    "s_cmp_eq_u32 s44, 63\n"
    "s_cbranch_scc1 LV0_%=_last\n"
    "ds_read_b128 v[128:131], v0 offset:0\n"
    "ds_read_b128 v[132:135], v0 offset:1024\n"
    "ds_read_b128 v[136:139], v0 offset:2048\n"
    "ds_read_b128 v[140:143], v0 offset:3072\n"
    "s_cmp_lg_u32 s33, 0\n"
    "s_cbranch_scc1 LV0_%=_all\n"
    "LV0_%=_fast:\n"
    "s_waitcnt lgkmcnt(0)\n"
    "s_mov_b32 s50, 0\n"
    "s_mov_b32 s48, s74\n"
    "s_load_dword s28, s[34:35], 0x0 glc\n"
    "s_nop 7\n"
    "s_nop 7\n"
    "s_nop 7\n"
    "s_load_dword s29, s[34:35], 0x0 glc\n"
    "s_nop 7\n"
    "s_nop 7\n"
    "s_nop 7\n"
    "s_load_dword s30, s[34:35], 0x0 glc\n"
    "s_nop 7\n"
    "s_nop 7\n"
    "s_nop 7\n"
    "LV0_%=_tk:\n"
    "s_load_dword s31, s[34:35], 0x0 glc\n"
    "s_waitcnt lgkmcnt(3)\n"
    "s_cmp_ge_i32 s28, s78\n"
    "s_cbranch_scc1 LV0_%=_seen\n"
    "s_load_dword s28, s[34:35], 0x0 glc\n"
    "s_waitcnt lgkmcnt(3)\n"
    "s_cmp_ge_i32 s29, s78\n"
    "s_cbranch_scc1 LV0_%=_seen\n"
    "s_load_dword s29, s[34:35], 0x0 glc\n"
    "s_waitcnt lgkmcnt(3)\n"
    "s_cmp_ge_i32 s30, s78\n"
    "s_cbranch_scc1 LV0_%=_seen\n"
    "s_load_dword s30, s[34:35], 0x0 glc\n"
    "s_waitcnt lgkmcnt(3)\n"
    "s_cmp_ge_i32 s31, s78\n"
    "s_cbranch_scc1 LV0_%=_seen\n"
    "s_add_u32 s50, s50, 1\n"
    "s_cmp_lt_u32 s50, 2000\n"
    "s_cbranch_scc1 LV0_%=_tk\n"
    "LV0_%=_seen:\n"
    "LV0_%=_dl:\n"
    "s_sub_u32 s48, s48, 1\n"
    "s_cbranch_scc0 LV0_%=_dl\n"
    "s_mov_b32 s50, 0\n"
    "s_waitcnt vmcnt(0)\n"
    "LV0_%=_ppoll:\n"
    "buffer_load_dwordx4 v[144:147], v0, s[40:43], s60 offen offset:0 sc1\n"
    "buffer_load_dwordx4 v[148:151], v0, s[40:43], s60 offen offset:1024 sc1\n"
    "buffer_load_dwordx4 v[152:155], v0, s[40:43], s60 offen offset:2048 sc1\n"
    "buffer_load_dwordx4 v[156:159], v0, s[40:43], s60 offen offset:3072 sc1\n"
    "buffer_load_dwordx4 v[160:163], v0, s[40:43], s61 offen offset:0 sc1\n"
    "buffer_load_dwordx4 v[164:167], v0, s[40:43], s61 offen offset:1024 sc1\n"
    "buffer_load_dwordx4 v[168:171], v0, s[40:43], s61 offen offset:2048 sc1\n"
    "buffer_load_dwordx4 v[172:175], v0, s[40:43], s61 offen offset:3072 sc1\n"
    "buffer_load_dwordx4 v[176:179], v8, s[40:43], s62 offen offset:0 sc1\n"
    "buffer_load_dwordx4 v[180:183], v8, s[40:43], s62 offen offset:1024 sc1\n"
    "buffer_store_dword v15, v203, s[40:43], 0 offen sc1\n"
    "buffer_store_dword v15, v13, s[40:43], s77 offen offset:2048 sc1\n"
    "s_waitcnt vmcnt(11)\n"
    "v_smfmac_f32_16x16x64_bf16 v[184:187], v[144:147], v[64:71], v1\n"
    "s_waitcnt vmcnt(10)\n"
    "v_smfmac_f32_16x16x64_bf16 v[184:187], v[148:151], v[72:79], v1\n"
    "s_waitcnt vmcnt(9)\n"
    "v_smfmac_f32_16x16x64_bf16 v[184:187], v[152:155], v[80:87], v1\n"
    "s_waitcnt vmcnt(8)\n"
    "v_smfmac_f32_16x16x64_bf16 v[184:187], v[156:159], v[88:95], v1\n"
    "s_waitcnt vmcnt(7)\n"
    "v_smfmac_f32_16x16x64_bf16 v[184:187], v[160:163], v[96:103], v1\n"
    "s_waitcnt vmcnt(6)\n"
    "v_smfmac_f32_16x16x64_bf16 v[184:187], v[164:167], v[104:111], v1\n"
    "s_waitcnt vmcnt(5)\n"
    "v_smfmac_f32_16x16x64_bf16 v[184:187], v[168:171], v[112:119], v1\n"
    "s_waitcnt vmcnt(4)\n"
    "v_smfmac_f32_16x16x64_bf16 v[184:187], v[172:175], v[120:127], v1\n"
    "s_waitcnt vmcnt(3)\n"
    "v_smfmac_f32_16x16x64_bf16 v[184:187], v[176:179], v[128:135], v1\n"
    "s_waitcnt vmcnt(2)\n"
    "v_smfmac_f32_16x16x64_bf16 v[184:187], v[180:183], v[136:143], v1\n"
    "s_nop 7\n"
    "v_pk_add_f32 v[184:185], v[184:185], v[186:187]\n"
    "v_cvt_pk_bf16_f32 v186, v184, v185\n"
    "ds_write_b32 v4, v186\n"
    "ds_read_b64_tr_b16 v[188:189], v5\n"
    "ds_read_b64_tr_b16 v[190:191], v5 offset:128\n"
    "v_cmp_u_f32 vcc, v184, v185\n"
    "s_cbranch_vccnz LV0_%=_pnan\n"
    "LV0_%=_pgood:\n"
    "s_waitcnt lgkmcnt(0)\n"
    "buffer_store_dwordx4 v[188:191], v9, s[40:43], s36 offen sc1\n"
    "s_barrier\n"
    "s_memtime s[46:47]\n"
    "s_waitcnt lgkmcnt(0)\n"
    "s_mov_b32 s51, s46\n"
    "s_add_u32 s45, s46, s73\n"
    "s_add_u32 s44, s44, 1\n"
    "s_mov_b32 s49, s54\n"
    "s_mov_b32 s53, s55\n"
    "s_mov_b32 s54, s56\n"
    "s_mov_b32 s55, s57\n"
    "s_mov_b32 s56, s58\n"
    "s_mov_b32 s57, s59\n"
    "s_mov_b32 s58, s49\n"
    "s_mov_b32 s59, s53\n"
    "s_mov_b32 s69, s54\n"
    "s_mov_b32 s79, s55\n"
    "s_mov_b32 s49, s60\n"
    "s_mov_b32 s60, s63\n"
    "s_mov_b32 s63, s66\n"
    "s_mov_b32 s66, s49\n"
    "s_mov_b32 s49, s61\n"
    "s_mov_b32 s61, s64\n"
    "s_mov_b32 s64, s67\n"
    "s_mov_b32 s67, s49\n"
    "s_mov_b32 s49, s62\n"
    "s_mov_b32 s62, s65\n"
    "s_mov_b32 s65, s68\n"
    "s_mov_b32 s68, s49\n"
    "s_branch LV0_%=_top\n"
    "LV0_%=_last:\n"
    "s_waitcnt lgkmcnt(0)\n"
    "s_barrier\n"
    "s_branch LV_exit_%=\n"
    "LV0_%=_mnan:\n"
    "v_mov_b32 v2, 0\n"
    "v_max3_u32 v2, v16, v17, v2\n"
    "v_max3_u32 v2, v18, v19, v2\n"
    "v_max3_u32 v2, v20, v21, v2\n"
    "v_max3_u32 v2, v22, v23, v2\n"
    "v_max3_u32 v2, v24, v25, v2\n"
    "v_max3_u32 v2, v26, v27, v2\n"
    "v_max3_u32 v2, v28, v29, v2\n"
    "v_max3_u32 v2, v30, v31, v2\n"
    "v_max3_u32 v2, v32, v33, v2\n"
    "v_max3_u32 v2, v34, v35, v2\n"
    "v_max3_u32 v2, v36, v37, v2\n"
    "v_max3_u32 v2, v38, v39, v2\n"
    "v_max3_u32 v2, v40, v41, v2\n"
    "v_max3_u32 v2, v42, v43, v2\n"
    "v_cmp_eq_u32 vcc, -1, v2\n"
    "s_cbranch_vccz LV0_%=_mok\n"
    "s_add_u32 s50, s50, 1\n"
    "s_cmp_gt_u32 s50, 30000\n"
    "s_cbranch_scc1 LV_exit_%=\n"
    "s_barrier\n"
    "s_branch LV0_%=_mpoll\n"
    "LV0_%=_all:\n"
    "s_mov_b32 s33, 0\n"
    "s_mov_b32 s50, 0\n"
    "LV0_%=_all_tk:\n"
    "buffer_load_dword v2, v14, s[40:43], s77 offen offset:0 sc1\n"
    "s_add_u32 s50, s50, 1\n"
    "s_cmp_gt_u32 s50, 30000\n"
    "s_cbranch_scc1 LV_exit_%=\n"
    "s_mov_b32 s48, s33\n"
    "s_waitcnt vmcnt(0)\n"
    "v_cmp_gt_i32 vcc, s78, v2\n"
    "s_cbranch_vccnz LV0_%=_all_tk\n"
    "LV0_%=_all_dl:\n"
    "s_sub_u32 s48, s48, 1\n"
    "s_cbranch_scc0 LV0_%=_all_dl\n"
    "s_mov_b32 s50, 0\n"
    "s_branch LV0_%=_fast\n"
    "LV0_%=_pnan:\n"
    "v_mov_b32 v2, 0\n"
    "v_max3_u32 v2, v144, v145, v2\n"
    "v_max3_u32 v2, v146, v147, v2\n"
    "v_max3_u32 v2, v148, v149, v2\n"
    "v_max3_u32 v2, v150, v151, v2\n"
    "v_max3_u32 v2, v152, v153, v2\n"
    "v_max3_u32 v2, v154, v155, v2\n"
    "v_max3_u32 v2, v156, v157, v2\n"
    "v_max3_u32 v2, v158, v159, v2\n"
    "v_max3_u32 v2, v160, v161, v2\n"
    "v_max3_u32 v2, v162, v163, v2\n"
    "v_max3_u32 v2, v164, v165, v2\n"
    "v_max3_u32 v2, v166, v167, v2\n"
    "v_max3_u32 v2, v168, v169, v2\n"
    "v_max3_u32 v2, v170, v171, v2\n"
    "v_max3_u32 v2, v172, v173, v2\n"
    "v_max3_u32 v2, v174, v175, v2\n"
    "v_max3_u32 v2, v176, v177, v2\n"
    "v_max3_u32 v2, v178, v179, v2\n"
    "v_max3_u32 v2, v180, v181, v2\n"
    "v_max3_u32 v2, v182, v183, v2\n"
    "v_cmp_eq_u32 vcc, -1, v2\n"
    "s_cbranch_vccz LV0_%=_pgood\n"
    "s_add_u32 s50, s50, 1\n"
    "s_add_u32 s75, s75, 1\n"
    "s_cmp_gt_u32 s50, 30000\n"
    "s_cbranch_scc1 LV_exit_%=\n"
    "s_waitcnt lgkmcnt(0)\n"
    "v_mov_b32 v184, 0\n"
    "v_mov_b32 v185, 0\n"
    "v_mov_b32 v186, 0\n"
    "v_mov_b32 v187, 0\n"
    "s_mov_b32 s33, 1\n"
    "s_branch LV0_%=_ppoll\n"
    "LV1_%=_start:\n"
    "v_and_b32 v11, 15, v10\n"
    "v_lshrrev_b32 v12, 4, v10\n"
    "v_lshrrev_b32 v2, 1, v12\n"
    "v_lshlrev_b32 v3, 8, v2\n"
    "v_lshl_add_u32 v3, v11, 4, v3\n"
    "v_and_b32 v2, 1, v12\n"
    "v_lshl_add_u32 v3, v2, 3, v3\n"
    "v_add_u32 v3, 0x400, v3\n"
    "v_mov_b32 v11, 0\n"
    "s_lshl_b32 s49, s72, 7\n"
    "s_add_u32 s49, s49, 0x2400\n"
    "v_mov_b32 v9, 0x7fffff00\n"
    "v_mov_b32 v2, s49\n"
    "v_cmp_eq_u32 vcc, 0, v10\n"
    "s_nop 1\n"
    "v_cndmask_b32 v9, v9, v2, vcc\n"
    "s_mul_i32 s49, s72, 14336\n"
    "s_add_u32 s49, s49, 0x87b000\n"
    "buffer_load_dwordx4 a[0:3], v0, s[40:43], s49 offen offset:0\n"
    "buffer_load_dwordx4 a[4:7], v0, s[40:43], s49 offen offset:1024\n"
    "buffer_load_dwordx4 a[8:11], v0, s[40:43], s49 offen offset:2048\n"
    "buffer_load_dwordx4 a[12:15], v0, s[40:43], s49 offen offset:3072\n"
    "s_add_u32 s49, s49, 4096\n"
    "buffer_load_dwordx4 a[16:19], v0, s[40:43], s49 offen offset:0\n"
    "buffer_load_dwordx4 a[20:23], v0, s[40:43], s49 offen offset:1024\n"
    "buffer_load_dwordx4 a[24:27], v0, s[40:43], s49 offen offset:2048\n"
    "buffer_load_dwordx4 a[28:31], v0, s[40:43], s49 offen offset:3072\n"
    "s_add_u32 s49, s49, 4096\n"
    "buffer_load_dwordx4 a[32:35], v0, s[40:43], s49 offen offset:0\n"
    "buffer_load_dwordx4 a[36:39], v0, s[40:43], s49 offen offset:1024\n"
    "buffer_load_dwordx4 a[40:43], v0, s[40:43], s49 offen offset:2048\n"
    "buffer_load_dwordx4 a[44:47], v0, s[40:43], s49 offen offset:3072\n"
    "s_add_u32 s49, s49, 4096\n"
    "buffer_load_dwordx4 a[48:51], v0, s[40:43], s49 offen offset:0\n"
    "buffer_load_dwordx4 a[52:55], v0, s[40:43], s49 offen offset:1024\n"
    "s_mul_i32 s49, s72, 14336\n"
    "s_add_u32 s49, s49, 0x8dd000\n"
    "buffer_load_dwordx4 a[56:59], v0, s[40:43], s49 offen offset:0\n"
    "buffer_load_dwordx4 a[60:63], v0, s[40:43], s49 offen offset:1024\n"
    "buffer_load_dwordx4 a[64:67], v0, s[40:43], s49 offen offset:2048\n"
    "buffer_load_dwordx4 a[68:71], v0, s[40:43], s49 offen offset:3072\n"
    "s_add_u32 s49, s49, 4096\n"
    "buffer_load_dwordx4 a[72:75], v0, s[40:43], s49 offen offset:0\n"
    "buffer_load_dwordx4 a[76:79], v0, s[40:43], s49 offen offset:1024\n"
    "buffer_load_dwordx4 a[80:83], v0, s[40:43], s49 offen offset:2048\n"
    "buffer_load_dwordx4 a[84:87], v0, s[40:43], s49 offen offset:3072\n"
    "s_add_u32 s49, s49, 4096\n"
    "buffer_load_dwordx4 a[88:91], v0, s[40:43], s49 offen offset:0\n"
    "buffer_load_dwordx4 a[92:95], v0, s[40:43], s49 offen offset:1024\n"
    "buffer_load_dwordx4 a[96:99], v0, s[40:43], s49 offen offset:2048\n"
    "buffer_load_dwordx4 a[100:103], v0, s[40:43], s49 offen offset:3072\n"
    "s_add_u32 s49, s49, 4096\n"
    "buffer_load_dwordx4 a[104:107], v0, s[40:43], s49 offen offset:0\n"
    "buffer_load_dwordx4 a[108:111], v0, s[40:43], s49 offen offset:1024\n"
    "s_mul_i32 s49, s72, 14336\n"
    "s_add_u32 s49, s49, 0x93f000\n"
    "buffer_load_dwordx4 a[112:115], v0, s[40:43], s49 offen offset:0\n"
    "buffer_load_dwordx4 a[116:119], v0, s[40:43], s49 offen offset:1024\n"
    "buffer_load_dwordx4 a[120:123], v0, s[40:43], s49 offen offset:2048\n"
    "buffer_load_dwordx4 a[124:127], v0, s[40:43], s49 offen offset:3072\n"
    "s_add_u32 s49, s49, 4096\n"
    "buffer_load_dwordx4 a[128:131], v0, s[40:43], s49 offen offset:0\n"
    "buffer_load_dwordx4 a[132:135], v0, s[40:43], s49 offen offset:1024\n"
    "buffer_load_dwordx4 a[136:139], v0, s[40:43], s49 offen offset:2048\n"
    "buffer_load_dwordx4 a[140:143], v0, s[40:43], s49 offen offset:3072\n"
    "s_add_u32 s49, s49, 4096\n"
    "buffer_load_dwordx4 a[144:147], v0, s[40:43], s49 offen offset:0\n"
    "buffer_load_dwordx4 a[148:151], v0, s[40:43], s49 offen offset:1024\n"
    "buffer_load_dwordx4 a[152:155], v0, s[40:43], s49 offen offset:2048\n"
    "buffer_load_dwordx4 a[156:159], v0, s[40:43], s49 offen offset:3072\n"
    "s_add_u32 s49, s49, 4096\n"
    "buffer_load_dwordx4 a[160:163], v0, s[40:43], s49 offen offset:0\n"
    "buffer_load_dwordx4 a[164:167], v0, s[40:43], s49 offen offset:1024\n"
    "s_mul_i32 s49, s72, 14336\n"
    "s_add_u32 s49, s49, 0x9a1000\n"
    "buffer_load_dwordx4 a[168:171], v0, s[40:43], s49 offen offset:0\n"
    "buffer_load_dwordx4 a[172:175], v0, s[40:43], s49 offen offset:1024\n"
    "buffer_load_dwordx4 a[176:179], v0, s[40:43], s49 offen offset:2048\n"
    "buffer_load_dwordx4 a[180:183], v0, s[40:43], s49 offen offset:3072\n"
    "s_add_u32 s49, s49, 4096\n"
    "buffer_load_dwordx4 a[184:187], v0, s[40:43], s49 offen offset:0\n"
    "buffer_load_dwordx4 a[188:191], v0, s[40:43], s49 offen offset:1024\n"
    "buffer_load_dwordx4 a[192:195], v0, s[40:43], s49 offen offset:2048\n"
    "buffer_load_dwordx4 a[196:199], v0, s[40:43], s49 offen offset:3072\n"
    "s_add_u32 s49, s49, 4096\n"
    "buffer_load_dwordx4 a[200:203], v0, s[40:43], s49 offen offset:0\n"
    "buffer_load_dwordx4 a[204:207], v0, s[40:43], s49 offen offset:1024\n"
    "buffer_load_dwordx4 a[208:211], v0, s[40:43], s49 offen offset:2048\n"
    "buffer_load_dwordx4 a[212:215], v0, s[40:43], s49 offen offset:3072\n"
    "s_add_u32 s49, s49, 4096\n"
    "buffer_load_dwordx4 a[216:219], v0, s[40:43], s49 offen offset:0\n"
    "buffer_load_dwordx4 a[220:223], v0, s[40:43], s49 offen offset:1024\n"
    "v_min_u32 v168, 4, v10\n"
    "v_lshlrev_b32 v168, 2, v168\n"
    "v_add_u32 v168, 0xf800, v168\n"
    "s_mov_b32 s50, 0\n"
    "LV1_%=_pt:\n"
    "buffer_load_dword v169, v168, s[40:43], 0 offen sc1\n"
    "s_add_u32 s50, s50, 1\n"
    "s_cmp_gt_u32 s50, 30000\n"
    "s_cbranch_scc1 LV_exit_%=\n"
    "s_waitcnt vmcnt(0)\n"
    "v_cmp_eq_u32 vcc, -1, v169\n"
    "s_cbranch_vccnz LV1_%=_pt\n"
    "v_readlane_b32 s49, v169, 0\n"
    "s_nop 1\n"
    "s_bfe_u32 s39, s49, 0x70000\n"
    "s_lshl_b32 s39, s39, 12\n"
    "s_add_u32 s39, s39, %7\n"
    "s_bfe_u32 s54, s49, 0x70008\n"
    "s_lshl_b32 s54, s54, 12\n"
    "s_add_u32 s54, s54, %7\n"
    "s_bfe_u32 s55, s49, 0x70010\n"
    "s_lshl_b32 s55, s55, 12\n"
    "s_add_u32 s55, s55, %7\n"
    "s_bfe_u32 s56, s49, 0x70018\n"
    "s_lshl_b32 s56, s56, 12\n"
    "s_add_u32 s56, s56, %7\n"
    "v_readlane_b32 s49, v169, 1\n"
    "s_nop 1\n"
    "s_bfe_u32 s57, s49, 0x70000\n"
    "s_lshl_b32 s57, s57, 12\n"
    "s_add_u32 s57, s57, %7\n"
    "s_bfe_u32 s58, s49, 0x70008\n"
    "s_lshl_b32 s58, s58, 12\n"
    "s_add_u32 s58, s58, %7\n"
    "s_bfe_u32 s59, s49, 0x70010\n"
    "s_lshl_b32 s59, s59, 12\n"
    "s_add_u32 s59, s59, %7\n"
    "s_bfe_u32 s60, s49, 0x70018\n"
    "s_lshl_b32 s60, s60, 12\n"
    "s_add_u32 s60, s60, %7\n"
    "v_readlane_b32 s49, v169, 2\n"
    "s_nop 1\n"
    "s_bfe_u32 s61, s49, 0x70000\n"
    "s_lshl_b32 s61, s61, 12\n"
    "s_add_u32 s61, s61, %7\n"
    "s_bfe_u32 s62, s49, 0x70008\n"
    "s_lshl_b32 s62, s62, 12\n"
    "s_add_u32 s62, s62, %7\n"
    "s_bfe_u32 s63, s49, 0x70010\n"
    "s_lshl_b32 s63, s63, 12\n"
    "s_add_u32 s63, s63, %7\n"
    "s_bfe_u32 s64, s49, 0x70018\n"
    "s_lshl_b32 s64, s64, 12\n"
    "s_add_u32 s64, s64, %7\n"
    "v_readlane_b32 s49, v169, 3\n"
    "s_nop 1\n"
    "s_bfe_u32 s65, s49, 0x70000\n"
    "s_lshl_b32 s65, s65, 12\n"
    "s_add_u32 s65, s65, %7\n"
    "s_bfe_u32 s66, s49, 0x70008\n"
    "s_lshl_b32 s66, s66, 12\n"
    "s_add_u32 s66, s66, %7\n"
    "s_bfe_u32 s67, s49, 0x70010\n"
    "s_lshl_b32 s67, s67, 12\n"
    "s_add_u32 s67, s67, %7\n"
    "s_bfe_u32 s68, s49, 0x70018\n"
    "s_lshl_b32 s68, s68, 12\n"
    "s_add_u32 s68, s68, %7\n"
    "v_readlane_b32 s49, v169, 4\n"
    "s_nop 1\n"
    "s_bfe_u32 s77, s49, 0x70000\n"
    "s_lshl_b32 s77, s77, 12\n"
    "s_add_u32 s77, s77, %7\n"
    "s_bfe_u32 s78, s49, 0x70008\n"
    "s_lshl_b32 s78, s78, 12\n"
    "s_add_u32 s78, s78, %7\n"
    "s_bfe_u32 s48, s49, 0x70010\n"
    "s_lshl_b32 s48, s48, 12\n"
    "s_add_u32 s48, s48, %7\n"
    "s_barrier\n"
    "s_memtime s[46:47]\n"
    "s_waitcnt lgkmcnt(0)\n"
    "s_mov_b32 s45, s46\n"
    "s_mov_b32 s51, s46\n"
    ".p2align 6\n"
    "s_nop 0\n"
    "LV1_%=_top:\n"
    "s_mov_b32 s50, 0\n"
    "s_memtime s[46:47]\n"
    "s_waitcnt lgkmcnt(0)\n"
    "s_sub_u32 s48, s45, s46\n"
    "s_cmp_lt_i32 s48, 28\n"
    "s_cbranch_scc1 LV1_%=_tw_go\n"
    "s_mul_i32 s48, s48, 2341\n"
    "s_lshr_b32 s48, s48, 16\n"
    "s_sub_u32 s48, s48, 1\n"
    "LV1_%=_tw_spin:\n"
    "s_sub_u32 s48, s48, 1\n"
    "s_cbranch_scc0 LV1_%=_tw_spin\n"
    "LV1_%=_tw_go:\n"
    "LV1_%=_mpoll:\n"
    "s_mov_b32 m0, 0x2000\n"
    "s_nop 0\n"
    "buffer_load_dwordx4 v0, s[40:43], s69 offen offset:0 sc1 lds\n"
    "s_mov_b32 m0, 0x2000\n"
    "s_nop 0\n"
    "buffer_load_dwordx4 v0, s[40:43], s69 offen offset:1024 sc1 lds\n"
    "v_mov_b32 v44, 0\n"
    "v_mov_b32 v45, 0\n"
    "v_mov_b32 v46, 0\n"
    "v_mov_b32 v47, 0\n"
    "v_mov_b32 v48, 0\n"
    "v_mov_b32 v49, 0\n"
    "v_mov_b32 v50, 0\n"
    "v_mov_b32 v51, 0\n"
    "v_mov_b32 v52, 0\n"
    "v_mov_b32 v53, 0\n"
    "v_mov_b32 v54, 0\n"
    "v_mov_b32 v55, 0\n"
    "v_mov_b32 v56, 0\n"
    "v_mov_b32 v57, 0\n"
    "v_mov_b32 v58, 0\n"
    "v_mov_b32 v59, 0\n"
    "s_mul_i32 s49, s44, 28\n"
    "s_add_u32 s49, s49, s72\n"
    "s_lshl_b32 s49, s49, 12\n"
    "s_add_u32 s49, s49, 0x4db7000\n"
    "s_cmp_eq_u32 s44, 0\n"
    "s_cselect_b32 s49, 0x7fff0000, s49\n"
    "buffer_store_dwordx4 v[64:67], v0, s[40:43], s49 offen offset:0 sc1\n"
    "buffer_store_dwordx4 v[68:71], v0, s[40:43], s49 offen offset:1024 sc1\n"
    "buffer_store_dwordx4 v[72:75], v0, s[40:43], s49 offen offset:2048 sc1\n"
    "buffer_store_dwordx4 v[76:79], v0, s[40:43], s49 offen offset:3072 sc1\n"
    "s_waitcnt vmcnt(4)\n"
    "s_barrier\n"
    "ds_read_b128 v[16:19], v0 offset:8192\n"
    "ds_read_b128 v[20:23], v0 offset:9216\n"
    "ds_read_b128 v[24:27], v0 offset:10240\n"
    "ds_read_b128 v[28:31], v0 offset:11264\n"
    "ds_read_b128 v[32:35], v0 offset:12288\n"
    "ds_read_b128 v[36:39], v0 offset:13312\n"
    "ds_read_b128 v[40:43], v0 offset:14336\n"
    "s_waitcnt lgkmcnt(6)\n"
    "v_smfmac_f32_16x16x64_bf16 v[44:47], v[16:19], a[0:7], v1\n"
    "s_waitcnt lgkmcnt(5)\n"
    "v_smfmac_f32_16x16x64_bf16 v[44:47], v[20:23], a[8:15], v1\n"
    "s_waitcnt lgkmcnt(4)\n"
    "v_smfmac_f32_16x16x64_bf16 v[44:47], v[24:27], a[16:23], v1\n"
    "s_waitcnt lgkmcnt(3)\n"
    "v_smfmac_f32_16x16x64_bf16 v[44:47], v[28:31], a[24:31], v1\n"
    "s_waitcnt lgkmcnt(2)\n"
    "v_smfmac_f32_16x16x64_bf16 v[44:47], v[32:35], a[32:39], v1\n"
    "s_waitcnt lgkmcnt(1)\n"
    "v_smfmac_f32_16x16x64_bf16 v[44:47], v[36:39], a[40:47], v1\n"
    "s_waitcnt lgkmcnt(0)\n"
    "v_smfmac_f32_16x16x64_bf16 v[44:47], v[40:43], a[48:55], v1\n"
    "v_smfmac_f32_16x16x64_bf16 v[48:51], v[16:19], a[56:63], v1\n"
    "v_smfmac_f32_16x16x64_bf16 v[48:51], v[20:23], a[64:71], v1\n"
    "v_smfmac_f32_16x16x64_bf16 v[48:51], v[24:27], a[72:79], v1\n"
    "v_add_f32 v44, v44, v46\n"
    "v_smfmac_f32_16x16x64_bf16 v[48:51], v[28:31], a[80:87], v1\n"
    "v_add_f32 v45, v45, v47\n"
    "v_smfmac_f32_16x16x64_bf16 v[48:51], v[32:35], a[88:95], v1\n"
    "v_cmp_u_f32 vcc, v44, v45\n"
    "v_smfmac_f32_16x16x64_bf16 v[48:51], v[36:39], a[96:103], v1\n"
    "s_cbranch_vccnz LV1_%=_mnan\n"
    "LV1_%=_mok:\n"
    "v_smfmac_f32_16x16x64_bf16 v[48:51], v[40:43], a[104:111], v1\n"
    "v_cvt_pk_bf16_f32 v44, v44, v45\n"
    "v_smfmac_f32_16x16x64_bf16 v[52:55], v[16:19], a[112:119], v1\n"
    "ds_write_b32 v3, v44 offset:0\n"
    "v_smfmac_f32_16x16x64_bf16 v[52:55], v[20:23], a[120:127], v1\n"
    "v_smfmac_f32_16x16x64_bf16 v[52:55], v[24:27], a[128:135], v1\n"
    "v_add_f32 v48, v48, v50\n"
    "v_smfmac_f32_16x16x64_bf16 v[52:55], v[28:31], a[136:143], v1\n"
    "v_add_f32 v49, v49, v51\n"
    "v_smfmac_f32_16x16x64_bf16 v[52:55], v[32:35], a[144:151], v1\n"
    "v_cvt_pk_bf16_f32 v48, v48, v49\n"
    "v_smfmac_f32_16x16x64_bf16 v[52:55], v[36:39], a[152:159], v1\n"
    "ds_write_b32 v3, v48 offset:4\n"
    "v_smfmac_f32_16x16x64_bf16 v[52:55], v[40:43], a[160:167], v1\n"
    "v_smfmac_f32_16x16x64_bf16 v[56:59], v[16:19], a[168:175], v1\n"
    "v_smfmac_f32_16x16x64_bf16 v[56:59], v[20:23], a[176:183], v1\n"
    "v_smfmac_f32_16x16x64_bf16 v[56:59], v[24:27], a[184:191], v1\n"
    "v_add_f32 v52, v52, v54\n"
    "v_smfmac_f32_16x16x64_bf16 v[56:59], v[28:31], a[192:199], v1\n"
    "v_add_f32 v53, v53, v55\n"
    "v_smfmac_f32_16x16x64_bf16 v[56:59], v[32:35], a[200:207], v1\n"
    "v_cvt_pk_bf16_f32 v52, v52, v53\n"
    "v_smfmac_f32_16x16x64_bf16 v[56:59], v[36:39], a[208:215], v1\n"
    "ds_write_b32 v3, v52 offset:512\n"
    "v_smfmac_f32_16x16x64_bf16 v[56:59], v[40:43], a[216:223], v1\n"
    "s_nop 7\n"
    "v_add_f32 v56, v56, v58\n"
    "v_add_f32 v57, v57, v59\n"
    "v_cvt_pk_bf16_f32 v56, v56, v57\n"
    "ds_write_b32 v3, v56 offset:516\n"
    "s_waitcnt lgkmcnt(0)\n"
    "s_barrier\n"
    "v_mov_b32 v11, s44\n"
    "s_waitcnt vmcnt(0)\n"
    "buffer_store_dword v11, v9, s[40:43], 0 offen sc1\n"
    "s_cmp_eq_u32 s44, 63\n"
    "s_cbranch_scc1 LV1_%=_last\n"
    "s_barrier\n"
    "s_memtime s[46:47]\n"
    "s_waitcnt lgkmcnt(0)\n"
    "s_mov_b32 s51, s46\n"
    "s_add_u32 s45, s46, s73\n"
    "ds_read_b128 v[64:67], v0 offset:0\n"
    "ds_read_b128 v[68:71], v0 offset:1024\n"
    "ds_read_b128 v[72:75], v0 offset:2048\n"
    "ds_read_b128 v[76:79], v0 offset:3072\n"
    "s_waitcnt lgkmcnt(0)\n"
    "s_add_u32 s44, s44, 1\n"
    "s_mov_b32 s49, s54\n"
    "s_mov_b32 s53, s55\n"
    "s_mov_b32 s54, s56\n"
    "s_mov_b32 s55, s57\n"
    "s_mov_b32 s56, s58\n"
    "s_mov_b32 s57, s59\n"
    "s_mov_b32 s58, s49\n"
    "s_mov_b32 s59, s53\n"
    "s_mov_b32 s69, s54\n"
    "s_mov_b32 s79, s55\n"
    "s_mov_b32 s49, s60\n"
    "s_mov_b32 s60, s63\n"
    "s_mov_b32 s63, s66\n"
    "s_mov_b32 s66, s49\n"
    "s_mov_b32 s49, s61\n"
    "s_mov_b32 s61, s64\n"
    "s_mov_b32 s64, s67\n"
    "s_mov_b32 s67, s49\n"
    "s_mov_b32 s49, s62\n"
    "s_mov_b32 s62, s65\n"
    "s_mov_b32 s65, s68\n"
    "s_mov_b32 s68, s49\n"
    "s_branch LV1_%=_top\n"
    "LV1_%=_last:\n"
    "s_waitcnt lgkmcnt(0)\n"
    "s_barrier\n"
    "ds_read_b128 v[64:67], v0 offset:0\n"
    "ds_read_b128 v[68:71], v0 offset:1024\n"
    "ds_read_b128 v[72:75], v0 offset:2048\n"
    "ds_read_b128 v[76:79], v0 offset:3072\n"
    "s_waitcnt lgkmcnt(0)\n"
    "s_mul_i32 s49, s44, 28\n"
    "s_add_u32 s49, s49, s72\n"
    "s_lshl_b32 s49, s49, 12\n"
    "s_add_u32 s49, s49, 0x4dd3000\n"
    "buffer_store_dwordx4 v[64:67], v0, s[40:43], s49 offen offset:0 sc1\n"
    "buffer_store_dwordx4 v[68:71], v0, s[40:43], s49 offen offset:1024 sc1\n"
    "buffer_store_dwordx4 v[72:75], v0, s[40:43], s49 offen offset:2048 sc1\n"
    "buffer_store_dwordx4 v[76:79], v0, s[40:43], s49 offen offset:3072 sc1\n"
    "v_mov_b32 v11, 64\n"
    "s_waitcnt vmcnt(0)\n"
    "buffer_store_dword v11, v9, s[40:43], 0 offen sc1\n"
    "s_branch LV_exit_%=\n"
    "LV1_%=_mnan:\n"
    "v_mov_b32 v2, 0\n"
    "v_max3_u32 v2, v16, v17, v2\n"
    "v_max3_u32 v2, v18, v19, v2\n"
    "v_max3_u32 v2, v20, v21, v2\n"
    "v_max3_u32 v2, v22, v23, v2\n"
    "v_max3_u32 v2, v24, v25, v2\n"
    "v_max3_u32 v2, v26, v27, v2\n"
    "v_max3_u32 v2, v28, v29, v2\n"
    "v_max3_u32 v2, v30, v31, v2\n"
    "v_max3_u32 v2, v32, v33, v2\n"
    "v_max3_u32 v2, v34, v35, v2\n"
    "v_max3_u32 v2, v36, v37, v2\n"
    "v_max3_u32 v2, v38, v39, v2\n"
    "v_max3_u32 v2, v40, v41, v2\n"
    "v_max3_u32 v2, v42, v43, v2\n"
    "v_cmp_eq_u32 vcc, -1, v2\n"
    "s_cbranch_vccz LV1_%=_mok\n"
    "s_add_u32 s50, s50, 1\n"
    "s_cmp_gt_u32 s50, 30000\n"
    "s_cbranch_scc1 LV_exit_%=\n"
    "s_barrier\n"
    "s_branch LV1_%=_mpoll\n"
    "LV2_%=_start:\n"
    "v_and_b32 v11, 15, v10\n"
    "v_lshrrev_b32 v12, 4, v10\n"
    "v_lshrrev_b32 v2, 1, v12\n"
    "v_lshlrev_b32 v3, 8, v2\n"
    "v_lshl_add_u32 v3, v11, 4, v3\n"
    "v_and_b32 v2, 1, v12\n"
    "v_lshl_add_u32 v3, v2, 3, v3\n"
    "v_add_u32 v3, 0x800, v3\n"
    "s_lshr_b32 s49, s72, 2\n"
    "s_lshl_b32 s49, s49, 9\n"
    "s_add_u32 s49, s49, 0xe00\n"
    "v_min_u32 v6, 3, v10\n"
    "v_lshlrev_b32 v6, 7, v6\n"
    "v_add_u32 v6, s49, v6\n"
    "s_mul_i32 s49, s72, 14336\n"
    "s_add_u32 s49, s49, 0xa03000\n"
    "buffer_load_dwordx4 a[0:3], v0, s[40:43], s49 offen offset:0\n"
    "buffer_load_dwordx4 a[4:7], v0, s[40:43], s49 offen offset:1024\n"
    "buffer_load_dwordx4 a[8:11], v0, s[40:43], s49 offen offset:2048\n"
    "buffer_load_dwordx4 a[12:15], v0, s[40:43], s49 offen offset:3072\n"
    "s_add_u32 s49, s49, 4096\n"
    "buffer_load_dwordx4 a[16:19], v0, s[40:43], s49 offen offset:0\n"
    "buffer_load_dwordx4 a[20:23], v0, s[40:43], s49 offen offset:1024\n"
    "buffer_load_dwordx4 a[24:27], v0, s[40:43], s49 offen offset:2048\n"
    "buffer_load_dwordx4 a[28:31], v0, s[40:43], s49 offen offset:3072\n"
    "s_add_u32 s49, s49, 4096\n"
    "buffer_load_dwordx4 a[32:35], v0, s[40:43], s49 offen offset:0\n"
    "buffer_load_dwordx4 a[36:39], v0, s[40:43], s49 offen offset:1024\n"
    "buffer_load_dwordx4 a[40:43], v0, s[40:43], s49 offen offset:2048\n"
    "buffer_load_dwordx4 a[44:47], v0, s[40:43], s49 offen offset:3072\n"
    "s_add_u32 s49, s49, 4096\n"
    "buffer_load_dwordx4 a[48:51], v0, s[40:43], s49 offen offset:0\n"
    "buffer_load_dwordx4 a[52:55], v0, s[40:43], s49 offen offset:1024\n"
    "s_mul_i32 s49, s72, 14336\n"
    "s_add_u32 s49, s49, 0xa65000\n"
    "buffer_load_dwordx4 a[56:59], v0, s[40:43], s49 offen offset:0\n"
    "buffer_load_dwordx4 a[60:63], v0, s[40:43], s49 offen offset:1024\n"
    "buffer_load_dwordx4 a[64:67], v0, s[40:43], s49 offen offset:2048\n"
    "buffer_load_dwordx4 a[68:71], v0, s[40:43], s49 offen offset:3072\n"
    "s_add_u32 s49, s49, 4096\n"
    "buffer_load_dwordx4 a[72:75], v0, s[40:43], s49 offen offset:0\n"
    "buffer_load_dwordx4 a[76:79], v0, s[40:43], s49 offen offset:1024\n"
    "buffer_load_dwordx4 a[80:83], v0, s[40:43], s49 offen offset:2048\n"
    "buffer_load_dwordx4 a[84:87], v0, s[40:43], s49 offen offset:3072\n"
    "s_add_u32 s49, s49, 4096\n"
    "buffer_load_dwordx4 a[88:91], v0, s[40:43], s49 offen offset:0\n"
    "buffer_load_dwordx4 a[92:95], v0, s[40:43], s49 offen offset:1024\n"
    "buffer_load_dwordx4 a[96:99], v0, s[40:43], s49 offen offset:2048\n"
    "buffer_load_dwordx4 a[100:103], v0, s[40:43], s49 offen offset:3072\n"
    "s_add_u32 s49, s49, 4096\n"
    "buffer_load_dwordx4 a[104:107], v0, s[40:43], s49 offen offset:0\n"
    "buffer_load_dwordx4 a[108:111], v0, s[40:43], s49 offen offset:1024\n"
    "s_mul_i32 s49, s72, 14336\n"
    "s_add_u32 s49, s49, 0xac7000\n"
    "buffer_load_dwordx4 a[112:115], v0, s[40:43], s49 offen offset:0\n"
    "buffer_load_dwordx4 a[116:119], v0, s[40:43], s49 offen offset:1024\n"
    "buffer_load_dwordx4 a[120:123], v0, s[40:43], s49 offen offset:2048\n"
    "buffer_load_dwordx4 a[124:127], v0, s[40:43], s49 offen offset:3072\n"
    "s_add_u32 s49, s49, 4096\n"
    "buffer_load_dwordx4 a[128:131], v0, s[40:43], s49 offen offset:0\n"
    "buffer_load_dwordx4 a[132:135], v0, s[40:43], s49 offen offset:1024\n"
    "buffer_load_dwordx4 a[136:139], v0, s[40:43], s49 offen offset:2048\n"
    "buffer_load_dwordx4 a[140:143], v0, s[40:43], s49 offen offset:3072\n"
    "s_add_u32 s49, s49, 4096\n"
    "buffer_load_dwordx4 a[144:147], v0, s[40:43], s49 offen offset:0\n"
    "buffer_load_dwordx4 a[148:151], v0, s[40:43], s49 offen offset:1024\n"
    "buffer_load_dwordx4 a[152:155], v0, s[40:43], s49 offen offset:2048\n"
    "buffer_load_dwordx4 a[156:159], v0, s[40:43], s49 offen offset:3072\n"
    "s_add_u32 s49, s49, 4096\n"
    "buffer_load_dwordx4 a[160:163], v0, s[40:43], s49 offen offset:0\n"
    "buffer_load_dwordx4 a[164:167], v0, s[40:43], s49 offen offset:1024\n"
    "s_mul_i32 s49, s72, 14336\n"
    "s_add_u32 s49, s49, 0xb29000\n"
    "buffer_load_dwordx4 a[168:171], v0, s[40:43], s49 offen offset:0\n"
    "buffer_load_dwordx4 a[172:175], v0, s[40:43], s49 offen offset:1024\n"
    "buffer_load_dwordx4 a[176:179], v0, s[40:43], s49 offen offset:2048\n"
    "buffer_load_dwordx4 a[180:183], v0, s[40:43], s49 offen offset:3072\n"
    "s_add_u32 s49, s49, 4096\n"
    "buffer_load_dwordx4 a[184:187], v0, s[40:43], s49 offen offset:0\n"
    "buffer_load_dwordx4 a[188:191], v0, s[40:43], s49 offen offset:1024\n"
    "buffer_load_dwordx4 a[192:195], v0, s[40:43], s49 offen offset:2048\n"
    "buffer_load_dwordx4 a[196:199], v0, s[40:43], s49 offen offset:3072\n"
    "s_add_u32 s49, s49, 4096\n"
    "buffer_load_dwordx4 a[200:203], v0, s[40:43], s49 offen offset:0\n"
    "buffer_load_dwordx4 a[204:207], v0, s[40:43], s49 offen offset:1024\n"
    "buffer_load_dwordx4 a[208:211], v0, s[40:43], s49 offen offset:2048\n"
    "buffer_load_dwordx4 a[212:215], v0, s[40:43], s49 offen offset:3072\n"
    "s_add_u32 s49, s49, 4096\n"
    "buffer_load_dwordx4 a[216:219], v0, s[40:43], s49 offen offset:0\n"
    "buffer_load_dwordx4 a[220:223], v0, s[40:43], s49 offen offset:1024\n"
    "v_min_u32 v168, 4, v10\n"
    "v_lshlrev_b32 v168, 2, v168\n"
    "v_add_u32 v168, 0xf800, v168\n"
    "s_mov_b32 s50, 0\n"
    "LV2_%=_pt:\n"
    "buffer_load_dword v169, v168, s[40:43], 0 offen sc1\n"
    "s_add_u32 s50, s50, 1\n"
    "s_cmp_gt_u32 s50, 30000\n"
    "s_cbranch_scc1 LV_exit_%=\n"
    "s_waitcnt vmcnt(0)\n"
    "v_cmp_eq_u32 vcc, -1, v169\n"
    "s_cbranch_vccnz LV2_%=_pt\n"
    "v_readlane_b32 s49, v169, 0\n"
    "s_nop 1\n"
    "s_bfe_u32 s39, s49, 0x70000\n"
    "s_lshl_b32 s39, s39, 12\n"
    "s_add_u32 s39, s39, %7\n"
    "s_bfe_u32 s54, s49, 0x70008\n"
    "s_lshl_b32 s54, s54, 12\n"
    "s_add_u32 s54, s54, %7\n"
    "s_bfe_u32 s55, s49, 0x70010\n"
    "s_lshl_b32 s55, s55, 12\n"
    "s_add_u32 s55, s55, %7\n"
    "s_bfe_u32 s56, s49, 0x70018\n"
    "s_lshl_b32 s56, s56, 12\n"
    "s_add_u32 s56, s56, %7\n"
    "v_readlane_b32 s49, v169, 1\n"
    "s_nop 1\n"
    "s_bfe_u32 s57, s49, 0x70000\n"
    "s_lshl_b32 s57, s57, 12\n"
    "s_add_u32 s57, s57, %7\n"
    "s_bfe_u32 s58, s49, 0x70008\n"
    "s_lshl_b32 s58, s58, 12\n"
    "s_add_u32 s58, s58, %7\n"
    "s_bfe_u32 s59, s49, 0x70010\n"
    "s_lshl_b32 s59, s59, 12\n"
    "s_add_u32 s59, s59, %7\n"
    "s_bfe_u32 s60, s49, 0x70018\n"
    "s_lshl_b32 s60, s60, 12\n"
    "s_add_u32 s60, s60, %7\n"
    "v_readlane_b32 s49, v169, 2\n"
    "s_nop 1\n"
    "s_bfe_u32 s61, s49, 0x70000\n"
    "s_lshl_b32 s61, s61, 12\n"
    "s_add_u32 s61, s61, %7\n"
    "s_bfe_u32 s62, s49, 0x70008\n"
    "s_lshl_b32 s62, s62, 12\n"
    "s_add_u32 s62, s62, %7\n"
    "s_bfe_u32 s63, s49, 0x70010\n"
    "s_lshl_b32 s63, s63, 12\n"
    "s_add_u32 s63, s63, %7\n"
    "s_bfe_u32 s64, s49, 0x70018\n"
    "s_lshl_b32 s64, s64, 12\n"
    "s_add_u32 s64, s64, %7\n"
    "v_readlane_b32 s49, v169, 3\n"
    "s_nop 1\n"
    "s_bfe_u32 s65, s49, 0x70000\n"
    "s_lshl_b32 s65, s65, 12\n"
    "s_add_u32 s65, s65, %7\n"
    "s_bfe_u32 s66, s49, 0x70008\n"
    "s_lshl_b32 s66, s66, 12\n"
    "s_add_u32 s66, s66, %7\n"
    "s_bfe_u32 s67, s49, 0x70010\n"
    "s_lshl_b32 s67, s67, 12\n"
    "s_add_u32 s67, s67, %7\n"
    "s_bfe_u32 s68, s49, 0x70018\n"
    "s_lshl_b32 s68, s68, 12\n"
    "s_add_u32 s68, s68, %7\n"
    "v_readlane_b32 s49, v169, 4\n"
    "s_nop 1\n"
    "s_bfe_u32 s77, s49, 0x70000\n"
    "s_lshl_b32 s77, s77, 12\n"
    "s_add_u32 s77, s77, %7\n"
    "s_bfe_u32 s78, s49, 0x70008\n"
    "s_lshl_b32 s78, s78, 12\n"
    "s_add_u32 s78, s78, %7\n"
    "s_bfe_u32 s48, s49, 0x70010\n"
    "s_lshl_b32 s48, s48, 12\n"
    "s_add_u32 s48, s48, %7\n"
    "s_mov_b32 s96, 0\n"
    "s_min_u32 s53, s96, 63\n"
    "s_add_u32 s52, s53, 1\n"
    "s_mov_b32 s50, 0\n"
    "LV2_%=_pf_poll:\n"
    "buffer_load_dword v7, v6, s[40:43], 0 offen sc1\n"
    "s_add_u32 s50, s50, 1\n"
    "s_cmp_gt_u32 s50, 30000\n"
    "s_cbranch_scc1 LV_exit_%=\n"
    "s_waitcnt vmcnt(0)\n"
    "v_cmp_gt_u32 vcc, s52, v7\n"
    "s_cbranch_vccnz LV2_%=_pf_poll\n"
    "LV2_%=_pf_ok:\n"
    "s_mul_i32 s97, s53, 28\n"
    "s_add_u32 s97, s97, s72\n"
    "s_lshl_b32 s97, s97, 14\n"
    "s_add_u32 s97, s97, 0x2ad3000\n"
    "s_and_b32 s99, s96, 1\n"
    "s_lshl_b32 s99, s99, 14\n"
    "s_add_u32 s99, s99, 0x8000\n"
    "s_add_u32 s96, s96, 1\n"
    "s_add_u32 m0, s99, 0\n"
    "s_add_u32 s98, s97, 0\n"
    "buffer_load_dwordx4 v0, s[40:43], s98 offen offset:0 sc1 lds\n"
    "buffer_load_dwordx4 v0, s[40:43], s98 offen offset:1024 sc1 lds\n"
    "buffer_load_dwordx4 v0, s[40:43], s98 offen offset:2048 sc1 lds\n"
    "buffer_load_dwordx4 v0, s[40:43], s98 offen offset:3072 sc1 lds\n"
    "s_add_u32 m0, s99, 4096\n"
    "s_add_u32 s98, s97, 4096\n"
    "buffer_load_dwordx4 v0, s[40:43], s98 offen offset:0 sc1 lds\n"
    "buffer_load_dwordx4 v0, s[40:43], s98 offen offset:1024 sc1 lds\n"
    "buffer_load_dwordx4 v0, s[40:43], s98 offen offset:2048 sc1 lds\n"
    "buffer_load_dwordx4 v0, s[40:43], s98 offen offset:3072 sc1 lds\n"
    "buffer_load_dword v7, v6, s[40:43], 0 offen sc1\n"
    "s_waitcnt vmcnt(0)\n"
    "s_barrier\n"
    "s_memtime s[46:47]\n"
    "s_waitcnt lgkmcnt(0)\n"
    "s_mov_b32 s45, s46\n"
    "s_mov_b32 s51, s46\n"
    ".p2align 6\n"
    "s_nop 0\n"
    "LV2_%=_top:\n"
    "s_min_u32 s53, s96, 63\n"
    "s_add_u32 s52, s53, 1\n"
    "v_cmp_gt_u32 vcc, s52, v7\n"
    "s_cbranch_vccz LV2_%=_f_ok\n"
    "s_mov_b32 s50, 0\n"
    "LV2_%=_f_poll:\n"
    "buffer_load_dword v7, v6, s[40:43], 0 offen sc1\n"
    "s_add_u32 s50, s50, 1\n"
    "s_cmp_gt_u32 s50, 30000\n"
    "s_cbranch_scc1 LV_exit_%=\n"
    "s_waitcnt vmcnt(0)\n"
    "v_cmp_gt_u32 vcc, s52, v7\n"
    "s_cbranch_vccnz LV2_%=_f_poll\n"
    "LV2_%=_f_ok:\n"
    "s_mul_i32 s97, s53, 28\n"
    "s_add_u32 s97, s97, s72\n"
    "s_lshl_b32 s97, s97, 14\n"
    "s_add_u32 s97, s97, 0x2ad3000\n"
    "s_and_b32 s99, s96, 1\n"
    "s_lshl_b32 s99, s99, 14\n"
    "s_add_u32 s99, s99, 0x8000\n"
    "s_add_u32 s96, s96, 1\n"
    "s_mov_b32 s50, 0\n"
    "s_memtime s[46:47]\n"
    "s_waitcnt lgkmcnt(0)\n"
    "s_sub_u32 s48, s45, s46\n"
    "s_cmp_lt_i32 s48, 28\n"
    "s_cbranch_scc1 LV2_%=_tw_go\n"
    "s_mul_i32 s48, s48, 2341\n"
    "s_lshr_b32 s48, s48, 16\n"
    "s_sub_u32 s48, s48, 1\n"
    "LV2_%=_tw_spin:\n"
    "s_sub_u32 s48, s48, 1\n"
    "s_cbranch_scc0 LV2_%=_tw_spin\n"
    "LV2_%=_tw_go:\n"
    "LV2_%=_mpoll:\n"
    "s_mov_b32 m0, 0x2000\n"
    "s_nop 0\n"
    "buffer_load_dwordx4 v0, s[40:43], s69 offen offset:2048 sc1 lds\n"
    "s_mov_b32 m0, 0x2000\n"
    "s_nop 0\n"
    "buffer_load_dwordx4 v0, s[40:43], s69 offen offset:3072 sc1 lds\n"
    "s_mov_b32 m0, 0x3000\n"
    "s_nop 0\n"
    "buffer_load_dwordx4 v0, s[40:43], s79 offen offset:0 sc1 lds\n"
    "v_mov_b32 v44, 0\n"
    "v_mov_b32 v45, 0\n"
    "v_mov_b32 v46, 0\n"
    "v_mov_b32 v47, 0\n"
    "v_mov_b32 v48, 0\n"
    "v_mov_b32 v49, 0\n"
    "v_mov_b32 v50, 0\n"
    "v_mov_b32 v51, 0\n"
    "v_mov_b32 v52, 0\n"
    "v_mov_b32 v53, 0\n"
    "v_mov_b32 v54, 0\n"
    "v_mov_b32 v55, 0\n"
    "v_mov_b32 v56, 0\n"
    "v_mov_b32 v57, 0\n"
    "v_mov_b32 v58, 0\n"
    "v_mov_b32 v59, 0\n"
    "s_add_u32 m0, s99, 0\n"
    "s_add_u32 s98, s97, 0\n"
    "buffer_load_dwordx4 v0, s[40:43], s98 offen offset:0 sc1 lds\n"
    "buffer_load_dwordx4 v0, s[40:43], s98 offen offset:1024 sc1 lds\n"
    "buffer_load_dwordx4 v0, s[40:43], s98 offen offset:2048 sc1 lds\n"
    "buffer_load_dwordx4 v0, s[40:43], s98 offen offset:3072 sc1 lds\n"
    "s_add_u32 m0, s99, 4096\n"
    "s_add_u32 s98, s97, 4096\n"
    "buffer_load_dwordx4 v0, s[40:43], s98 offen offset:0 sc1 lds\n"
    "buffer_load_dwordx4 v0, s[40:43], s98 offen offset:1024 sc1 lds\n"
    "buffer_load_dwordx4 v0, s[40:43], s98 offen offset:2048 sc1 lds\n"
    "buffer_load_dwordx4 v0, s[40:43], s98 offen offset:3072 sc1 lds\n"
    "buffer_load_dword v7, v6, s[40:43], 0 offen sc1\n"
    "s_waitcnt vmcnt(9)\n"
    "s_barrier\n"
    "ds_read_b128 v[16:19], v0 offset:8192\n"
    "ds_read_b128 v[20:23], v0 offset:9216\n"
    "ds_read_b128 v[24:27], v0 offset:10240\n"
    "ds_read_b128 v[28:31], v0 offset:11264\n"
    "ds_read_b128 v[32:35], v0 offset:12288\n"
    "ds_read_b128 v[36:39], v0 offset:13312\n"
    "ds_read_b128 v[40:43], v0 offset:14336\n"
    "s_waitcnt lgkmcnt(6)\n"
    "v_smfmac_f32_16x16x64_bf16 v[44:47], v[16:19], a[0:7], v1\n"
    "s_waitcnt lgkmcnt(5)\n"
    "v_smfmac_f32_16x16x64_bf16 v[44:47], v[20:23], a[8:15], v1\n"
    "s_waitcnt lgkmcnt(4)\n"
    "v_smfmac_f32_16x16x64_bf16 v[44:47], v[24:27], a[16:23], v1\n"
    "s_waitcnt lgkmcnt(3)\n"
    "v_smfmac_f32_16x16x64_bf16 v[44:47], v[28:31], a[24:31], v1\n"
    "s_waitcnt lgkmcnt(2)\n"
    "v_smfmac_f32_16x16x64_bf16 v[44:47], v[32:35], a[32:39], v1\n"
    "s_waitcnt lgkmcnt(1)\n"
    "v_smfmac_f32_16x16x64_bf16 v[44:47], v[36:39], a[40:47], v1\n"
    "s_waitcnt lgkmcnt(0)\n"
    "v_smfmac_f32_16x16x64_bf16 v[44:47], v[40:43], a[48:55], v1\n"
    "v_smfmac_f32_16x16x64_bf16 v[48:51], v[16:19], a[56:63], v1\n"
    "v_smfmac_f32_16x16x64_bf16 v[48:51], v[20:23], a[64:71], v1\n"
    "v_smfmac_f32_16x16x64_bf16 v[48:51], v[24:27], a[72:79], v1\n"
    "v_add_f32 v44, v44, v46\n"
    "v_smfmac_f32_16x16x64_bf16 v[48:51], v[28:31], a[80:87], v1\n"
    "v_add_f32 v45, v45, v47\n"
    "v_smfmac_f32_16x16x64_bf16 v[48:51], v[32:35], a[88:95], v1\n"
    "v_cmp_u_f32 vcc, v44, v45\n"
    "v_smfmac_f32_16x16x64_bf16 v[48:51], v[36:39], a[96:103], v1\n"
    "s_cbranch_vccnz LV2_%=_mnan\n"
    "LV2_%=_mok:\n"
    "v_smfmac_f32_16x16x64_bf16 v[48:51], v[40:43], a[104:111], v1\n"
    "v_cvt_pk_bf16_f32 v44, v44, v45\n"
    "v_smfmac_f32_16x16x64_bf16 v[52:55], v[16:19], a[112:119], v1\n"
    "ds_write_b32 v3, v44 offset:0\n"
    "v_smfmac_f32_16x16x64_bf16 v[52:55], v[20:23], a[120:127], v1\n"
    "v_smfmac_f32_16x16x64_bf16 v[52:55], v[24:27], a[128:135], v1\n"
    "v_add_f32 v48, v48, v50\n"
    "v_smfmac_f32_16x16x64_bf16 v[52:55], v[28:31], a[136:143], v1\n"
    "v_add_f32 v49, v49, v51\n"
    "v_smfmac_f32_16x16x64_bf16 v[52:55], v[32:35], a[144:151], v1\n"
    "v_cvt_pk_bf16_f32 v48, v48, v49\n"
    "v_smfmac_f32_16x16x64_bf16 v[52:55], v[36:39], a[152:159], v1\n"
    "ds_write_b32 v3, v48 offset:4\n"
    "v_smfmac_f32_16x16x64_bf16 v[52:55], v[40:43], a[160:167], v1\n"
    "v_smfmac_f32_16x16x64_bf16 v[56:59], v[16:19], a[168:175], v1\n"
    "v_smfmac_f32_16x16x64_bf16 v[56:59], v[20:23], a[176:183], v1\n"
    "v_smfmac_f32_16x16x64_bf16 v[56:59], v[24:27], a[184:191], v1\n"
    "v_add_f32 v52, v52, v54\n"
    "v_smfmac_f32_16x16x64_bf16 v[56:59], v[28:31], a[192:199], v1\n"
    "v_add_f32 v53, v53, v55\n"
    "v_smfmac_f32_16x16x64_bf16 v[56:59], v[32:35], a[200:207], v1\n"
    "v_cvt_pk_bf16_f32 v52, v52, v53\n"
    "v_smfmac_f32_16x16x64_bf16 v[56:59], v[36:39], a[208:215], v1\n"
    "ds_write_b32 v3, v52 offset:512\n"
    "v_smfmac_f32_16x16x64_bf16 v[56:59], v[40:43], a[216:223], v1\n"
    "s_nop 7\n"
    "v_add_f32 v56, v56, v58\n"
    "v_add_f32 v57, v57, v59\n"
    "v_cvt_pk_bf16_f32 v56, v56, v57\n"
    "ds_write_b32 v3, v56 offset:516\n"
    "s_waitcnt lgkmcnt(0)\n"
    "s_barrier\n"
    "s_cmp_eq_u32 s44, 63\n"
    "s_cbranch_scc1 LV2_%=_last\n"
    "s_waitcnt vmcnt(0)\n"
    "s_barrier\n"
    "s_memtime s[46:47]\n"
    "s_waitcnt lgkmcnt(0)\n"
    "s_mov_b32 s51, s46\n"
    "s_add_u32 s45, s46, s73\n"
    "s_add_u32 s44, s44, 1\n"
    "s_mov_b32 s49, s54\n"
    "s_mov_b32 s53, s55\n"
    "s_mov_b32 s54, s56\n"
    "s_mov_b32 s55, s57\n"
    "s_mov_b32 s56, s58\n"
    "s_mov_b32 s57, s59\n"
    "s_mov_b32 s58, s49\n"
    "s_mov_b32 s59, s53\n"
    "s_mov_b32 s69, s54\n"
    "s_mov_b32 s79, s55\n"
    "s_mov_b32 s49, s60\n"
    "s_mov_b32 s60, s63\n"
    "s_mov_b32 s63, s66\n"
    "s_mov_b32 s66, s49\n"
    "s_mov_b32 s49, s61\n"
    "s_mov_b32 s61, s64\n"
    "s_mov_b32 s64, s67\n"
    "s_mov_b32 s67, s49\n"
    "s_mov_b32 s49, s62\n"
    "s_mov_b32 s62, s65\n"
    "s_mov_b32 s65, s68\n"
    "s_mov_b32 s68, s49\n"
    "s_branch LV2_%=_top\n"
    "LV2_%=_last:\n"
    "s_waitcnt lgkmcnt(0)\n"
    "s_barrier\n"
    "s_branch LV_exit_%=\n"
    "LV2_%=_mnan:\n"
    "v_mov_b32 v2, 0\n"
    "v_max3_u32 v2, v16, v17, v2\n"
    "v_max3_u32 v2, v18, v19, v2\n"
    "v_max3_u32 v2, v20, v21, v2\n"
    "v_max3_u32 v2, v22, v23, v2\n"
    "v_max3_u32 v2, v24, v25, v2\n"
    "v_max3_u32 v2, v26, v27, v2\n"
    "v_max3_u32 v2, v28, v29, v2\n"
    "v_max3_u32 v2, v30, v31, v2\n"
    "v_max3_u32 v2, v32, v33, v2\n"
    "v_max3_u32 v2, v34, v35, v2\n"
    "v_max3_u32 v2, v36, v37, v2\n"
    "v_max3_u32 v2, v38, v39, v2\n"
    "v_max3_u32 v2, v40, v41, v2\n"
    "v_max3_u32 v2, v42, v43, v2\n"
    "v_cmp_eq_u32 vcc, -1, v2\n"
    "s_cbranch_vccz LV2_%=_mok\n"
    "s_add_u32 s50, s50, 1\n"
    "s_cmp_gt_u32 s50, 30000\n"
    "s_cbranch_scc1 LV_exit_%=\n"
    "s_barrier\n"
    "s_branch LV2_%=_mpoll\n"
    "LV3_%=_start:\n"
    "v_and_b32 v11, 15, v10\n"
    "v_lshrrev_b32 v12, 4, v10\n"
    "v_lshrrev_b32 v2, 1, v12\n"
    "v_lshlrev_b32 v3, 8, v2\n"
    "v_lshl_add_u32 v3, v11, 4, v3\n"
    "v_and_b32 v2, 1, v12\n"
    "v_lshl_add_u32 v3, v2, 3, v3\n"
    "v_add_u32 v3, 0xc00, v3\n"
    "s_lshr_b32 s49, s72, 2\n"
    "s_lshl_b32 s49, s49, 9\n"
    "s_add_u32 s49, s49, 0xe00\n"
    "v_min_u32 v6, 3, v10\n"
    "v_lshlrev_b32 v6, 7, v6\n"
    "v_add_u32 v6, s49, v6\n"
    "s_mul_i32 s49, s72, 14336\n"
    "s_add_u32 s49, s49, 0xb8b000\n"
    "buffer_load_dwordx4 a[0:3], v0, s[40:43], s49 offen offset:0\n"
    "buffer_load_dwordx4 a[4:7], v0, s[40:43], s49 offen offset:1024\n"
    "buffer_load_dwordx4 a[8:11], v0, s[40:43], s49 offen offset:2048\n"
    "buffer_load_dwordx4 a[12:15], v0, s[40:43], s49 offen offset:3072\n"
    "s_add_u32 s49, s49, 4096\n"
    "buffer_load_dwordx4 a[16:19], v0, s[40:43], s49 offen offset:0\n"
    "buffer_load_dwordx4 a[20:23], v0, s[40:43], s49 offen offset:1024\n"
    "buffer_load_dwordx4 a[24:27], v0, s[40:43], s49 offen offset:2048\n"
    "buffer_load_dwordx4 a[28:31], v0, s[40:43], s49 offen offset:3072\n"
    "s_add_u32 s49, s49, 4096\n"
    "buffer_load_dwordx4 a[32:35], v0, s[40:43], s49 offen offset:0\n"
    "buffer_load_dwordx4 a[36:39], v0, s[40:43], s49 offen offset:1024\n"
    "buffer_load_dwordx4 a[40:43], v0, s[40:43], s49 offen offset:2048\n"
    "buffer_load_dwordx4 a[44:47], v0, s[40:43], s49 offen offset:3072\n"
    "s_add_u32 s49, s49, 4096\n"
    "buffer_load_dwordx4 a[48:51], v0, s[40:43], s49 offen offset:0\n"
    "buffer_load_dwordx4 a[52:55], v0, s[40:43], s49 offen offset:1024\n"
    "s_mul_i32 s49, s72, 14336\n"
    "s_add_u32 s49, s49, 0xbed000\n"
    "buffer_load_dwordx4 a[56:59], v0, s[40:43], s49 offen offset:0\n"
    "buffer_load_dwordx4 a[60:63], v0, s[40:43], s49 offen offset:1024\n"
    "buffer_load_dwordx4 a[64:67], v0, s[40:43], s49 offen offset:2048\n"
    "buffer_load_dwordx4 a[68:71], v0, s[40:43], s49 offen offset:3072\n"
    "s_add_u32 s49, s49, 4096\n"
    "buffer_load_dwordx4 a[72:75], v0, s[40:43], s49 offen offset:0\n"
    "buffer_load_dwordx4 a[76:79], v0, s[40:43], s49 offen offset:1024\n"
    "buffer_load_dwordx4 a[80:83], v0, s[40:43], s49 offen offset:2048\n"
    "buffer_load_dwordx4 a[84:87], v0, s[40:43], s49 offen offset:3072\n"
    "s_add_u32 s49, s49, 4096\n"
    "buffer_load_dwordx4 a[88:91], v0, s[40:43], s49 offen offset:0\n"
    "buffer_load_dwordx4 a[92:95], v0, s[40:43], s49 offen offset:1024\n"
    "buffer_load_dwordx4 a[96:99], v0, s[40:43], s49 offen offset:2048\n"
    "buffer_load_dwordx4 a[100:103], v0, s[40:43], s49 offen offset:3072\n"
    "s_add_u32 s49, s49, 4096\n"
    "buffer_load_dwordx4 a[104:107], v0, s[40:43], s49 offen offset:0\n"
    "buffer_load_dwordx4 a[108:111], v0, s[40:43], s49 offen offset:1024\n"
    "s_mul_i32 s49, s72, 14336\n"
    "s_add_u32 s49, s49, 0xc4f000\n"
    "buffer_load_dwordx4 a[112:115], v0, s[40:43], s49 offen offset:0\n"
    "buffer_load_dwordx4 a[116:119], v0, s[40:43], s49 offen offset:1024\n"
    "buffer_load_dwordx4 a[120:123], v0, s[40:43], s49 offen offset:2048\n"
    "buffer_load_dwordx4 a[124:127], v0, s[40:43], s49 offen offset:3072\n"
    "s_add_u32 s49, s49, 4096\n"
    "buffer_load_dwordx4 a[128:131], v0, s[40:43], s49 offen offset:0\n"
    "buffer_load_dwordx4 a[132:135], v0, s[40:43], s49 offen offset:1024\n"
    "buffer_load_dwordx4 a[136:139], v0, s[40:43], s49 offen offset:2048\n"
    "buffer_load_dwordx4 a[140:143], v0, s[40:43], s49 offen offset:3072\n"
    "s_add_u32 s49, s49, 4096\n"
    "buffer_load_dwordx4 a[144:147], v0, s[40:43], s49 offen offset:0\n"
    "buffer_load_dwordx4 a[148:151], v0, s[40:43], s49 offen offset:1024\n"
    "buffer_load_dwordx4 a[152:155], v0, s[40:43], s49 offen offset:2048\n"
    "buffer_load_dwordx4 a[156:159], v0, s[40:43], s49 offen offset:3072\n"
    "s_add_u32 s49, s49, 4096\n"
    "buffer_load_dwordx4 a[160:163], v0, s[40:43], s49 offen offset:0\n"
    "buffer_load_dwordx4 a[164:167], v0, s[40:43], s49 offen offset:1024\n"
    "s_mul_i32 s49, s72, 14336\n"
    "s_add_u32 s49, s49, 0xcb1000\n"
    "buffer_load_dwordx4 a[168:171], v0, s[40:43], s49 offen offset:0\n"
    "buffer_load_dwordx4 a[172:175], v0, s[40:43], s49 offen offset:1024\n"
    "buffer_load_dwordx4 a[176:179], v0, s[40:43], s49 offen offset:2048\n"
    "buffer_load_dwordx4 a[180:183], v0, s[40:43], s49 offen offset:3072\n"
    "s_add_u32 s49, s49, 4096\n"
    "buffer_load_dwordx4 a[184:187], v0, s[40:43], s49 offen offset:0\n"
    "buffer_load_dwordx4 a[188:191], v0, s[40:43], s49 offen offset:1024\n"
    "buffer_load_dwordx4 a[192:195], v0, s[40:43], s49 offen offset:2048\n"
    "buffer_load_dwordx4 a[196:199], v0, s[40:43], s49 offen offset:3072\n"
    "s_add_u32 s49, s49, 4096\n"
    "buffer_load_dwordx4 a[200:203], v0, s[40:43], s49 offen offset:0\n"
    "buffer_load_dwordx4 a[204:207], v0, s[40:43], s49 offen offset:1024\n"
    "buffer_load_dwordx4 a[208:211], v0, s[40:43], s49 offen offset:2048\n"
    "buffer_load_dwordx4 a[212:215], v0, s[40:43], s49 offen offset:3072\n"
    "s_add_u32 s49, s49, 4096\n"
    "buffer_load_dwordx4 a[216:219], v0, s[40:43], s49 offen offset:0\n"
    "buffer_load_dwordx4 a[220:223], v0, s[40:43], s49 offen offset:1024\n"
    "v_min_u32 v168, 4, v10\n"
    "v_lshlrev_b32 v168, 2, v168\n"
    "v_add_u32 v168, 0xf800, v168\n"
    "s_mov_b32 s50, 0\n"
    "LV3_%=_pt:\n"
    "buffer_load_dword v169, v168, s[40:43], 0 offen sc1\n"
    "s_add_u32 s50, s50, 1\n"
    "s_cmp_gt_u32 s50, 30000\n"
    "s_cbranch_scc1 LV_exit_%=\n"
    "s_waitcnt vmcnt(0)\n"
    "v_cmp_eq_u32 vcc, -1, v169\n"
    "s_cbranch_vccnz LV3_%=_pt\n"
    "v_readlane_b32 s49, v169, 0\n"
    "s_nop 1\n"
    "s_bfe_u32 s39, s49, 0x70000\n"
    "s_lshl_b32 s39, s39, 12\n"
    "s_add_u32 s39, s39, %7\n"
    "s_bfe_u32 s54, s49, 0x70008\n"
    "s_lshl_b32 s54, s54, 12\n"
    "s_add_u32 s54, s54, %7\n"
    "s_bfe_u32 s55, s49, 0x70010\n"
    "s_lshl_b32 s55, s55, 12\n"
    "s_add_u32 s55, s55, %7\n"
    "s_bfe_u32 s56, s49, 0x70018\n"
    "s_lshl_b32 s56, s56, 12\n"
    "s_add_u32 s56, s56, %7\n"
    "v_readlane_b32 s49, v169, 1\n"
    "s_nop 1\n"
    "s_bfe_u32 s57, s49, 0x70000\n"
    "s_lshl_b32 s57, s57, 12\n"
    "s_add_u32 s57, s57, %7\n"
    "s_bfe_u32 s58, s49, 0x70008\n"
    "s_lshl_b32 s58, s58, 12\n"
    "s_add_u32 s58, s58, %7\n"
    "s_bfe_u32 s59, s49, 0x70010\n"
    "s_lshl_b32 s59, s59, 12\n"
    "s_add_u32 s59, s59, %7\n"
    "s_bfe_u32 s60, s49, 0x70018\n"
    "s_lshl_b32 s60, s60, 12\n"
    "s_add_u32 s60, s60, %7\n"
    "v_readlane_b32 s49, v169, 2\n"
    "s_nop 1\n"
    "s_bfe_u32 s61, s49, 0x70000\n"
    "s_lshl_b32 s61, s61, 12\n"
    "s_add_u32 s61, s61, %7\n"
    "s_bfe_u32 s62, s49, 0x70008\n"
    "s_lshl_b32 s62, s62, 12\n"
    "s_add_u32 s62, s62, %7\n"
    "s_bfe_u32 s63, s49, 0x70010\n"
    "s_lshl_b32 s63, s63, 12\n"
    "s_add_u32 s63, s63, %7\n"
    "s_bfe_u32 s64, s49, 0x70018\n"
    "s_lshl_b32 s64, s64, 12\n"
    "s_add_u32 s64, s64, %7\n"
    "v_readlane_b32 s49, v169, 3\n"
    "s_nop 1\n"
    "s_bfe_u32 s65, s49, 0x70000\n"
    "s_lshl_b32 s65, s65, 12\n"
    "s_add_u32 s65, s65, %7\n"
    "s_bfe_u32 s66, s49, 0x70008\n"
    "s_lshl_b32 s66, s66, 12\n"
    "s_add_u32 s66, s66, %7\n"
    "s_bfe_u32 s67, s49, 0x70010\n"
    "s_lshl_b32 s67, s67, 12\n"
    "s_add_u32 s67, s67, %7\n"
    "s_bfe_u32 s68, s49, 0x70018\n"
    "s_lshl_b32 s68, s68, 12\n"
    "s_add_u32 s68, s68, %7\n"
    "v_readlane_b32 s49, v169, 4\n"
    "s_nop 1\n"
    "s_bfe_u32 s77, s49, 0x70000\n"
    "s_lshl_b32 s77, s77, 12\n"
    "s_add_u32 s77, s77, %7\n"
    "s_bfe_u32 s78, s49, 0x70008\n"
    "s_lshl_b32 s78, s78, 12\n"
    "s_add_u32 s78, s78, %7\n"
    "s_bfe_u32 s48, s49, 0x70010\n"
    "s_lshl_b32 s48, s48, 12\n"
    "s_add_u32 s48, s48, %7\n"
    "s_mov_b32 s96, 0\n"
    "s_min_u32 s53, s96, 63\n"
    "s_add_u32 s52, s53, 1\n"
    "s_mov_b32 s50, 0\n"
    "LV3_%=_pf_poll:\n"
    "buffer_load_dword v7, v6, s[40:43], 0 offen sc1\n"
    "s_add_u32 s50, s50, 1\n"
    "s_cmp_gt_u32 s50, 30000\n"
    "s_cbranch_scc1 LV_exit_%=\n"
    "s_waitcnt vmcnt(0)\n"
    "v_cmp_gt_u32 vcc, s52, v7\n"
    "s_cbranch_vccnz LV3_%=_pf_poll\n"
    "LV3_%=_pf_ok:\n"
    "s_mul_i32 s97, s53, 28\n"
    "s_add_u32 s97, s97, s72\n"
    "s_lshl_b32 s97, s97, 14\n"
    "s_add_u32 s97, s97, 0x2ad5000\n"
    "s_and_b32 s99, s96, 1\n"
    "s_lshl_b32 s99, s99, 14\n"
    "s_add_u32 s99, s99, 0xa000\n"
    "s_add_u32 s96, s96, 1\n"
    "s_add_u32 m0, s99, 0\n"
    "s_add_u32 s98, s97, 0\n"
    "buffer_load_dwordx4 v0, s[40:43], s98 offen offset:0 sc1 lds\n"
    "buffer_load_dwordx4 v0, s[40:43], s98 offen offset:1024 sc1 lds\n"
    "buffer_load_dwordx4 v0, s[40:43], s98 offen offset:2048 sc1 lds\n"
    "buffer_load_dwordx4 v0, s[40:43], s98 offen offset:3072 sc1 lds\n"
    "s_add_u32 m0, s99, 4096\n"
    "s_add_u32 s98, s97, 4096\n"
    "buffer_load_dwordx4 v0, s[40:43], s98 offen offset:0 sc1 lds\n"
    "buffer_load_dwordx4 v0, s[40:43], s98 offen offset:1024 sc1 lds\n"
    "buffer_load_dwordx4 v0, s[40:43], s98 offen offset:2048 sc1 lds\n"
    "buffer_load_dwordx4 v0, s[40:43], s98 offen offset:3072 sc1 lds\n"
    "buffer_load_dword v7, v6, s[40:43], 0 offen sc1\n"
    "s_waitcnt vmcnt(0)\n"
    "s_barrier\n"
    "s_memtime s[46:47]\n"
    "s_waitcnt lgkmcnt(0)\n"
    "s_mov_b32 s45, s46\n"
    "s_mov_b32 s51, s46\n"
    ".p2align 6\n"
    "s_nop 0\n"
    "LV3_%=_top:\n"
    "s_min_u32 s53, s96, 63\n"
    "s_add_u32 s52, s53, 1\n"
    "v_cmp_gt_u32 vcc, s52, v7\n"
    "s_cbranch_vccz LV3_%=_f_ok\n"
    "s_mov_b32 s50, 0\n"
    "LV3_%=_f_poll:\n"
    "buffer_load_dword v7, v6, s[40:43], 0 offen sc1\n"
    "s_add_u32 s50, s50, 1\n"
    "s_cmp_gt_u32 s50, 30000\n"
    "s_cbranch_scc1 LV_exit_%=\n"
    "s_waitcnt vmcnt(0)\n"
    "v_cmp_gt_u32 vcc, s52, v7\n"
    "s_cbranch_vccnz LV3_%=_f_poll\n"
    "LV3_%=_f_ok:\n"
    "s_mul_i32 s97, s53, 28\n"
    "s_add_u32 s97, s97, s72\n"
    "s_lshl_b32 s97, s97, 14\n"
    "s_add_u32 s97, s97, 0x2ad5000\n"
    "s_and_b32 s99, s96, 1\n"
    "s_lshl_b32 s99, s99, 14\n"
    "s_add_u32 s99, s99, 0xa000\n"
    "s_add_u32 s96, s96, 1\n"
    "s_mov_b32 s50, 0\n"
    "s_memtime s[46:47]\n"
    "s_waitcnt lgkmcnt(0)\n"
    "s_sub_u32 s48, s45, s46\n"
    "s_cmp_lt_i32 s48, 28\n"
    "s_cbranch_scc1 LV3_%=_tw_go\n"
    "s_mul_i32 s48, s48, 2341\n"
    "s_lshr_b32 s48, s48, 16\n"
    "s_sub_u32 s48, s48, 1\n"
    "LV3_%=_tw_spin:\n"
    "s_sub_u32 s48, s48, 1\n"
    "s_cbranch_scc0 LV3_%=_tw_spin\n"
    "LV3_%=_tw_go:\n"
    "LV3_%=_mpoll:\n"
    "s_mov_b32 m0, 0x3000\n"
    "s_nop 0\n"
    "buffer_load_dwordx4 v0, s[40:43], s79 offen offset:1024 sc1 lds\n"
    "s_mov_b32 m0, 0x3000\n"
    "s_nop 0\n"
    "buffer_load_dwordx4 v0, s[40:43], s79 offen offset:2048 sc1 lds\n"
    "v_mov_b32 v44, 0\n"
    "v_mov_b32 v45, 0\n"
    "v_mov_b32 v46, 0\n"
    "v_mov_b32 v47, 0\n"
    "v_mov_b32 v48, 0\n"
    "v_mov_b32 v49, 0\n"
    "v_mov_b32 v50, 0\n"
    "v_mov_b32 v51, 0\n"
    "v_mov_b32 v52, 0\n"
    "v_mov_b32 v53, 0\n"
    "v_mov_b32 v54, 0\n"
    "v_mov_b32 v55, 0\n"
    "v_mov_b32 v56, 0\n"
    "v_mov_b32 v57, 0\n"
    "v_mov_b32 v58, 0\n"
    "v_mov_b32 v59, 0\n"
    "s_add_u32 m0, s99, 0\n"
    "s_add_u32 s98, s97, 0\n"
    "buffer_load_dwordx4 v0, s[40:43], s98 offen offset:0 sc1 lds\n"
    "buffer_load_dwordx4 v0, s[40:43], s98 offen offset:1024 sc1 lds\n"
    "buffer_load_dwordx4 v0, s[40:43], s98 offen offset:2048 sc1 lds\n"
    "buffer_load_dwordx4 v0, s[40:43], s98 offen offset:3072 sc1 lds\n"
    "s_add_u32 m0, s99, 4096\n"
    "s_add_u32 s98, s97, 4096\n"
    "buffer_load_dwordx4 v0, s[40:43], s98 offen offset:0 sc1 lds\n"
    "buffer_load_dwordx4 v0, s[40:43], s98 offen offset:1024 sc1 lds\n"
    "buffer_load_dwordx4 v0, s[40:43], s98 offen offset:2048 sc1 lds\n"
    "buffer_load_dwordx4 v0, s[40:43], s98 offen offset:3072 sc1 lds\n"
    "buffer_load_dword v7, v6, s[40:43], 0 offen sc1\n"
    "s_waitcnt vmcnt(9)\n"
    "s_barrier\n"
    "ds_read_b128 v[16:19], v0 offset:8192\n"
    "ds_read_b128 v[20:23], v0 offset:9216\n"
    "ds_read_b128 v[24:27], v0 offset:10240\n"
    "ds_read_b128 v[28:31], v0 offset:11264\n"
    "ds_read_b128 v[32:35], v0 offset:12288\n"
    "ds_read_b128 v[36:39], v0 offset:13312\n"
    "ds_read_b128 v[40:43], v0 offset:14336\n"
    "s_waitcnt lgkmcnt(6)\n"
    "v_smfmac_f32_16x16x64_bf16 v[44:47], v[16:19], a[0:7], v1\n"
    "s_waitcnt lgkmcnt(5)\n"
    "v_smfmac_f32_16x16x64_bf16 v[44:47], v[20:23], a[8:15], v1\n"
    "s_waitcnt lgkmcnt(4)\n"
    "v_smfmac_f32_16x16x64_bf16 v[44:47], v[24:27], a[16:23], v1\n"
    "s_waitcnt lgkmcnt(3)\n"
    "v_smfmac_f32_16x16x64_bf16 v[44:47], v[28:31], a[24:31], v1\n"
    "s_waitcnt lgkmcnt(2)\n"
    "v_smfmac_f32_16x16x64_bf16 v[44:47], v[32:35], a[32:39], v1\n"
    "s_waitcnt lgkmcnt(1)\n"
    "v_smfmac_f32_16x16x64_bf16 v[44:47], v[36:39], a[40:47], v1\n"
    "s_waitcnt lgkmcnt(0)\n"
    "v_smfmac_f32_16x16x64_bf16 v[44:47], v[40:43], a[48:55], v1\n"
    "v_smfmac_f32_16x16x64_bf16 v[48:51], v[16:19], a[56:63], v1\n"
    "v_smfmac_f32_16x16x64_bf16 v[48:51], v[20:23], a[64:71], v1\n"
    "v_smfmac_f32_16x16x64_bf16 v[48:51], v[24:27], a[72:79], v1\n"
    "v_add_f32 v44, v44, v46\n"
    "v_smfmac_f32_16x16x64_bf16 v[48:51], v[28:31], a[80:87], v1\n"
    "v_add_f32 v45, v45, v47\n"
    "v_smfmac_f32_16x16x64_bf16 v[48:51], v[32:35], a[88:95], v1\n"
    "v_cmp_u_f32 vcc, v44, v45\n"
    "v_smfmac_f32_16x16x64_bf16 v[48:51], v[36:39], a[96:103], v1\n"
    "s_cbranch_vccnz LV3_%=_mnan\n"
    "LV3_%=_mok:\n"
    "v_smfmac_f32_16x16x64_bf16 v[48:51], v[40:43], a[104:111], v1\n"
    "v_cvt_pk_bf16_f32 v44, v44, v45\n"
    "v_smfmac_f32_16x16x64_bf16 v[52:55], v[16:19], a[112:119], v1\n"
    "ds_write_b32 v3, v44 offset:0\n"
    "v_smfmac_f32_16x16x64_bf16 v[52:55], v[20:23], a[120:127], v1\n"
    "v_smfmac_f32_16x16x64_bf16 v[52:55], v[24:27], a[128:135], v1\n"
    "v_add_f32 v48, v48, v50\n"
    "v_smfmac_f32_16x16x64_bf16 v[52:55], v[28:31], a[136:143], v1\n"
    "v_add_f32 v49, v49, v51\n"
    "v_smfmac_f32_16x16x64_bf16 v[52:55], v[32:35], a[144:151], v1\n"
    "v_cvt_pk_bf16_f32 v48, v48, v49\n"
    "v_smfmac_f32_16x16x64_bf16 v[52:55], v[36:39], a[152:159], v1\n"
    "ds_write_b32 v3, v48 offset:4\n"
    "v_smfmac_f32_16x16x64_bf16 v[52:55], v[40:43], a[160:167], v1\n"
    "v_smfmac_f32_16x16x64_bf16 v[56:59], v[16:19], a[168:175], v1\n"
    "v_smfmac_f32_16x16x64_bf16 v[56:59], v[20:23], a[176:183], v1\n"
    "v_smfmac_f32_16x16x64_bf16 v[56:59], v[24:27], a[184:191], v1\n"
    "v_add_f32 v52, v52, v54\n"
    "v_smfmac_f32_16x16x64_bf16 v[56:59], v[28:31], a[192:199], v1\n"
    "v_add_f32 v53, v53, v55\n"
    "v_smfmac_f32_16x16x64_bf16 v[56:59], v[32:35], a[200:207], v1\n"
    "v_cvt_pk_bf16_f32 v52, v52, v53\n"
    "v_smfmac_f32_16x16x64_bf16 v[56:59], v[36:39], a[208:215], v1\n"
    "ds_write_b32 v3, v52 offset:512\n"
    "v_smfmac_f32_16x16x64_bf16 v[56:59], v[40:43], a[216:223], v1\n"
    "s_nop 7\n"
    "v_add_f32 v56, v56, v58\n"
    "v_add_f32 v57, v57, v59\n"
    "v_cvt_pk_bf16_f32 v56, v56, v57\n"
    "ds_write_b32 v3, v56 offset:516\n"
    "s_waitcnt lgkmcnt(0)\n"
    "s_barrier\n"
    "s_cmp_eq_u32 s44, 63\n"
    "s_cbranch_scc1 LV3_%=_last\n"
    "s_waitcnt vmcnt(0)\n"
    "s_barrier\n"
    "s_memtime s[46:47]\n"
    "s_waitcnt lgkmcnt(0)\n"
    "s_mov_b32 s51, s46\n"
    "s_add_u32 s45, s46, s73\n"
    "s_add_u32 s44, s44, 1\n"
    "s_mov_b32 s49, s54\n"
    "s_mov_b32 s53, s55\n"
    "s_mov_b32 s54, s56\n"
    "s_mov_b32 s55, s57\n"
    "s_mov_b32 s56, s58\n"
    "s_mov_b32 s57, s59\n"
    "s_mov_b32 s58, s49\n"
    "s_mov_b32 s59, s53\n"
    "s_mov_b32 s69, s54\n"
    "s_mov_b32 s79, s55\n"
    "s_mov_b32 s49, s60\n"
    "s_mov_b32 s60, s63\n"
    "s_mov_b32 s63, s66\n"
    "s_mov_b32 s66, s49\n"
    "s_mov_b32 s49, s61\n"
    "s_mov_b32 s61, s64\n"
    "s_mov_b32 s64, s67\n"
    "s_mov_b32 s67, s49\n"
    "s_mov_b32 s49, s62\n"
    "s_mov_b32 s62, s65\n"
    "s_mov_b32 s65, s68\n"
    "s_mov_b32 s68, s49\n"
    "s_branch LV3_%=_top\n"
    "LV3_%=_last:\n"
    "s_waitcnt lgkmcnt(0)\n"
    "s_barrier\n"
    "s_branch LV_exit_%=\n"
    "LV3_%=_mnan:\n"
    "v_mov_b32 v2, 0\n"
    "v_max3_u32 v2, v16, v17, v2\n"
    "v_max3_u32 v2, v18, v19, v2\n"
    "v_max3_u32 v2, v20, v21, v2\n"
    "v_max3_u32 v2, v22, v23, v2\n"
    "v_max3_u32 v2, v24, v25, v2\n"
    "v_max3_u32 v2, v26, v27, v2\n"
    "v_max3_u32 v2, v28, v29, v2\n"
    "v_max3_u32 v2, v30, v31, v2\n"
    "v_max3_u32 v2, v32, v33, v2\n"
    "v_max3_u32 v2, v34, v35, v2\n"
    "v_max3_u32 v2, v36, v37, v2\n"
    "v_max3_u32 v2, v38, v39, v2\n"
    "v_max3_u32 v2, v40, v41, v2\n"
    "v_max3_u32 v2, v42, v43, v2\n"
    "v_cmp_eq_u32 vcc, -1, v2\n"
    "s_cbranch_vccz LV3_%=_mok\n"
    "s_add_u32 s50, s50, 1\n"
    "s_cmp_gt_u32 s50, 30000\n"
    "s_cbranch_scc1 LV_exit_%=\n"
    "s_barrier\n"
    "s_branch LV3_%=_mpoll\n"
    "LV_exit_%=:\n"
    "s_endpgm\n"
            :: "s"(wlo), "s"(whi), "s"(r), "s"(w), "s"(p.wm), "s"(p.wv_t),
               "s"(p.ws_bytes), "s"(ring)
            : CHAIN_CLOBBERS);
            __builtin_unreachable();
        }
        return;
    }
    const int x6 = x - (x > 0) - (x > 2);
    int role, id;
    if (x6 + 6 * r < 56) { id = x6 + 6 * r; role = id >= 28; id -= 28 * role; }
    else {
        id = x6 * 23 - min(x6, 2) + r - (x6 < 2 ? 10 : 9);
        role = id < 24 ? 2 : id < 52 ? 3 : id < 108 ? 4 : 5;
        id -= role == 2 ? 0 : role == 3 ? 24 : role == 4 ? 52 : 108;
    }
    role = __builtin_amdgcn_readfirstlane(role); id = __builtin_amdgcn_readfirstlane(id);
    const int j = role * 32 + (id & 31);
    if (role < 2) {
        const Params p = args();
        const int a = id & 3, pr = id >> 2, h = 4 * a + w;
        const u32 wlo = (u32)(size_t)p.ws, whi = (u32)((size_t)p.ws >> 32);
        const u32 wo = WS_WIMG + ((role * 16 + h) * 28 + 4 * pr) * 14336;
        if (role == 0) {
            asm volatile(
    "s_mov_b32 s40, %0\n"
    "s_and_b32 s41, %1, 0xffff\n"
    "s_mov_b32 s42, %2\n"
    "s_mov_b32 s43, 0x00020000\n"
    "v_mbcnt_lo_u32_b32 v10, -1, 0\n"
    "v_mbcnt_hi_u32_b32 v10, -1, v10\n"
    "v_lshlrev_b32 v0, 4, v10\n"
    "s_cmp_eq_u32 %3, 0\n"
    "s_cselect_b32 s49, %6, 0x7fffff00\n"
    "v_mov_b32 v3, 0x7fffff00\n"
    "v_mov_b32 v2, s49\n"
    "v_cmp_eq_u32 vcc, 0, v10\n"
    "s_nop 1\n"
    "v_cndmask_b32 v3, v3, v2, vcc\n"
    "s_mul_i32 s59, %3, 7168\n"
    "s_add_u32 s57, s59, 0xd13000\n"
    "s_mov_b32 s58, s59\n"
    "s_mov_b32 s55, %5\n"
    "s_add_u32 m0, s58, 0\n"
    "s_add_u32 s53, s57, 0\n"
    "buffer_load_dwordx4 v0, s[40:43], s53 offen offset:0 lds\n"
    "buffer_load_dwordx4 v0, s[40:43], s53 offen offset:1024 lds\n"
    "buffer_load_dwordx4 v0, s[40:43], s53 offen offset:2048 lds\n"
    "buffer_load_dwordx4 v0, s[40:43], s53 offen offset:3072 lds\n"
    "s_add_u32 m0, s58, 4096\n"
    "s_add_u32 s53, s57, 4096\n"
    "buffer_load_dwordx4 v0, s[40:43], s53 offen offset:0 lds\n"
    "buffer_load_dwordx4 v0, s[40:43], s53 offen offset:1024 lds\n"
    "buffer_load_dwordx4 v0, s[40:43], s53 offen offset:2048 lds\n"
    "s_add_u32 s57, s57, 28672\n"
    "s_add_u32 s58, s58, 28672\n"
    "s_cmp_ge_u32 s58, 86016\n"
    "s_cselect_b32 s58, s59, s58\n"
    "s_add_u32 m0, s58, 0\n"
    "s_add_u32 s53, s57, 0\n"
    "buffer_load_dwordx4 v0, s[40:43], s53 offen offset:0 lds\n"
    "buffer_load_dwordx4 v0, s[40:43], s53 offen offset:1024 lds\n"
    "buffer_load_dwordx4 v0, s[40:43], s53 offen offset:2048 lds\n"
    "buffer_load_dwordx4 v0, s[40:43], s53 offen offset:3072 lds\n"
    "s_add_u32 m0, s58, 4096\n"
    "s_add_u32 s53, s57, 4096\n"
    "buffer_load_dwordx4 v0, s[40:43], s53 offen offset:0 lds\n"
    "buffer_load_dwordx4 v0, s[40:43], s53 offen offset:1024 lds\n"
    "buffer_load_dwordx4 v0, s[40:43], s53 offen offset:2048 lds\n"
    "s_add_u32 s57, s57, 28672\n"
    "s_add_u32 s58, s58, 28672\n"
    "s_cmp_ge_u32 s58, 86016\n"
    "s_cselect_b32 s58, s59, s58\n"
    "s_mov_b32 s54, %4\n"
    "buffer_load_dwordx4 a[0:3], v0, s[40:43], s54 offen offset:0\n"
    "buffer_load_dwordx4 a[4:7], v0, s[40:43], s54 offen offset:1024\n"
    "buffer_load_dwordx4 a[8:11], v0, s[40:43], s54 offen offset:2048\n"
    "buffer_load_dwordx4 a[12:15], v0, s[40:43], s54 offen offset:3072\n"
    "s_add_u32 s54, s54, 4096\n"
    "buffer_load_dwordx4 a[16:19], v0, s[40:43], s54 offen offset:0\n"
    "buffer_load_dwordx4 a[20:23], v0, s[40:43], s54 offen offset:1024\n"
    "buffer_load_dwordx4 a[24:27], v0, s[40:43], s54 offen offset:2048\n"
    "buffer_load_dwordx4 a[28:31], v0, s[40:43], s54 offen offset:3072\n"
    "s_add_u32 s54, s54, 4096\n"
    "buffer_load_dwordx4 a[32:35], v0, s[40:43], s54 offen offset:0\n"
    "buffer_load_dwordx4 a[36:39], v0, s[40:43], s54 offen offset:1024\n"
    "buffer_load_dwordx4 a[40:43], v0, s[40:43], s54 offen offset:2048\n"
    "buffer_load_dwordx4 a[44:47], v0, s[40:43], s54 offen offset:3072\n"
    "s_add_u32 s54, s54, 4096\n"
    "buffer_load_dwordx4 a[48:51], v0, s[40:43], s54 offen offset:0\n"
    "buffer_load_dwordx4 a[52:55], v0, s[40:43], s54 offen offset:1024\n"
    "buffer_load_dwordx4 a[56:59], v0, s[40:43], s54 offen offset:2048\n"
    "buffer_load_dwordx4 a[60:63], v0, s[40:43], s54 offen offset:3072\n"
    "s_add_u32 s54, s54, 4096\n"
    "buffer_load_dwordx4 a[64:67], v0, s[40:43], s54 offen offset:0\n"
    "buffer_load_dwordx4 a[68:71], v0, s[40:43], s54 offen offset:1024\n"
    "buffer_load_dwordx4 a[72:75], v0, s[40:43], s54 offen offset:2048\n"
    "buffer_load_dwordx4 a[76:79], v0, s[40:43], s54 offen offset:3072\n"
    "s_add_u32 s54, s54, 4096\n"
    "buffer_load_dwordx4 a[80:83], v0, s[40:43], s54 offen offset:0\n"
    "buffer_load_dwordx4 a[84:87], v0, s[40:43], s54 offen offset:1024\n"
    "buffer_load_dwordx4 a[88:91], v0, s[40:43], s54 offen offset:2048\n"
    "buffer_load_dwordx4 a[92:95], v0, s[40:43], s54 offen offset:3072\n"
    "s_add_u32 s54, s54, 4096\n"
    "buffer_load_dwordx4 a[96:99], v0, s[40:43], s54 offen offset:0\n"
    "buffer_load_dwordx4 a[100:103], v0, s[40:43], s54 offen offset:1024\n"
    "buffer_load_dwordx4 a[104:107], v0, s[40:43], s54 offen offset:2048\n"
    "buffer_load_dwordx4 a[108:111], v0, s[40:43], s54 offen offset:3072\n"
    "s_add_u32 s54, s54, 4096\n"
    "buffer_load_dwordx4 a[112:115], v0, s[40:43], s54 offen offset:0\n"
    "buffer_load_dwordx4 a[116:119], v0, s[40:43], s54 offen offset:1024\n"
    "buffer_load_dwordx4 a[120:123], v0, s[40:43], s54 offen offset:2048\n"
    "buffer_load_dwordx4 a[124:127], v0, s[40:43], s54 offen offset:3072\n"
    "s_add_u32 s54, s54, 4096\n"
    "buffer_load_dwordx4 a[128:131], v0, s[40:43], s54 offen offset:0\n"
    "buffer_load_dwordx4 a[132:135], v0, s[40:43], s54 offen offset:1024\n"
    "buffer_load_dwordx4 a[136:139], v0, s[40:43], s54 offen offset:2048\n"
    "buffer_load_dwordx4 a[140:143], v0, s[40:43], s54 offen offset:3072\n"
    "s_add_u32 s54, s54, 4096\n"
    "buffer_load_dwordx4 a[144:147], v0, s[40:43], s54 offen offset:0\n"
    "buffer_load_dwordx4 a[148:151], v0, s[40:43], s54 offen offset:1024\n"
    "buffer_load_dwordx4 a[152:155], v0, s[40:43], s54 offen offset:2048\n"
    "buffer_load_dwordx4 a[156:159], v0, s[40:43], s54 offen offset:3072\n"
    "s_add_u32 s54, s54, 4096\n"
    "buffer_load_dwordx4 a[160:163], v0, s[40:43], s54 offen offset:0\n"
    "buffer_load_dwordx4 a[164:167], v0, s[40:43], s54 offen offset:1024\n"
    "buffer_load_dwordx4 a[168:171], v0, s[40:43], s54 offen offset:2048\n"
    "buffer_load_dwordx4 a[172:175], v0, s[40:43], s54 offen offset:3072\n"
    "s_add_u32 s54, s54, 4096\n"
    "s_waitcnt vmcnt(20)\n"
    "buffer_load_dwordx4 a[176:179], v0, s[40:43], s54 offen offset:0\n"
    "buffer_load_dwordx4 a[180:183], v0, s[40:43], s54 offen offset:1024\n"
    "buffer_load_dwordx4 a[184:187], v0, s[40:43], s54 offen offset:2048\n"
    "buffer_load_dwordx4 a[188:191], v0, s[40:43], s54 offen offset:3072\n"
    "s_add_u32 s54, s54, 4096\n"
    "buffer_load_dwordx4 a[192:195], v0, s[40:43], s54 offen offset:0\n"
    "buffer_load_dwordx4 a[196:199], v0, s[40:43], s54 offen offset:1024\n"
    "buffer_load_dwordx4 a[200:203], v0, s[40:43], s54 offen offset:2048\n"
    "buffer_load_dwordx4 a[204:207], v0, s[40:43], s54 offen offset:3072\n"
    "s_add_u32 s54, s54, 4096\n"
    "buffer_load_dwordx4 a[208:211], v0, s[40:43], s54 offen offset:0\n"
    "buffer_load_dwordx4 a[212:215], v0, s[40:43], s54 offen offset:1024\n"
    "buffer_load_dwordx4 a[216:219], v0, s[40:43], s54 offen offset:2048\n"
    "buffer_load_dwordx4 a[220:223], v0, s[40:43], s54 offen offset:3072\n"
    "s_mov_b32 s44, 0\n"
    "v_mov_b32 v1, v0\n"
    "s_waitcnt vmcnt(0)\n"
    "s_barrier\n"
    "ds_read_b128 v[8:11], v1 offset:0\n"
    "ds_read_b128 v[12:15], v1 offset:14336\n"
    "ds_read_b128 v[16:19], v1 offset:1024\n"
    "ds_read_b128 v[20:23], v1 offset:15360\n"
    "ds_read_b128 v[24:27], v1 offset:2048\n"
    "ds_read_b128 v[28:31], v1 offset:16384\n"
    "s_waitcnt lgkmcnt(5)\n"
    "v_mfma_f32_16x16x32_bf16 v[48:51], a[0:3], v[8:11], 0\n"
    "ds_read_b128 v[32:35], v1 offset:3072\n"
    "v_mfma_f32_16x16x32_bf16 v[56:59], a[56:59], v[8:11], 0\n"
    "s_add_u32 m0, s58, 0\n"
    "s_add_u32 s53, s57, 0\n"
    "buffer_load_dwordx4 v0, s[40:43], s53 offen offset:0 lds\n"
    "v_mfma_f32_16x16x32_bf16 v[64:67], a[112:115], v[8:11], 0\n"
    "v_mfma_f32_16x16x32_bf16 v[72:75], a[168:171], v[8:11], 0\n"
    "buffer_load_dwordx4 v0, s[40:43], s53 offen offset:1024 lds\n"
    "s_waitcnt lgkmcnt(5)\n"
    "v_mfma_f32_16x16x32_bf16 v[52:55], a[0:3], v[12:15], 0\n"
    "ds_read_b128 v[36:39], v1 offset:17408\n"
    "v_mfma_f32_16x16x32_bf16 v[60:63], a[56:59], v[12:15], 0\n"
    "buffer_load_dwordx4 v0, s[40:43], s53 offen offset:2048 lds\n"
    "v_mfma_f32_16x16x32_bf16 v[68:71], a[112:115], v[12:15], 0\n"
    "v_mfma_f32_16x16x32_bf16 v[76:79], a[168:171], v[12:15], 0\n"
    "buffer_load_dwordx4 v0, s[40:43], s53 offen offset:3072 lds\n"
    "s_waitcnt lgkmcnt(5)\n"
    "v_mfma_f32_16x16x32_bf16 v[48:51], a[4:7], v[16:19], v[48:51]\n"
    "ds_read_b128 v[8:11], v1 offset:4096\n"
    "v_mfma_f32_16x16x32_bf16 v[56:59], a[60:63], v[16:19], v[56:59]\n"
    "s_add_u32 m0, s58, 4096\n"
    "s_add_u32 s53, s57, 4096\n"
    "buffer_load_dwordx4 v0, s[40:43], s53 offen offset:0 lds\n"
    "v_mfma_f32_16x16x32_bf16 v[64:67], a[116:119], v[16:19], v[64:67]\n"
    "v_mfma_f32_16x16x32_bf16 v[72:75], a[172:175], v[16:19], v[72:75]\n"
    "buffer_load_dwordx4 v0, s[40:43], s53 offen offset:1024 lds\n"
    "s_waitcnt lgkmcnt(5)\n"
    "v_mfma_f32_16x16x32_bf16 v[52:55], a[4:7], v[20:23], v[52:55]\n"
    "ds_read_b128 v[12:15], v1 offset:18432\n"
    "v_mfma_f32_16x16x32_bf16 v[60:63], a[60:63], v[20:23], v[60:63]\n"
    "buffer_load_dwordx4 v0, s[40:43], s53 offen offset:2048 lds\n"
    "v_mfma_f32_16x16x32_bf16 v[68:71], a[116:119], v[20:23], v[68:71]\n"
    "v_mfma_f32_16x16x32_bf16 v[76:79], a[172:175], v[20:23], v[76:79]\n"
    "s_add_u32 s57, s57, 28672\n"
    "s_add_u32 s58, s58, 28672\n"
    "s_cmp_ge_u32 s58, 86016\n"
    "s_cselect_b32 s58, s59, s58\n"
    "s_waitcnt lgkmcnt(5)\n"
    "v_mfma_f32_16x16x32_bf16 v[48:51], a[8:11], v[24:27], v[48:51]\n"
    "ds_read_b128 v[16:19], v1 offset:5120\n"
    "v_mfma_f32_16x16x32_bf16 v[56:59], a[64:67], v[24:27], v[56:59]\n"
    "v_mfma_f32_16x16x32_bf16 v[64:67], a[120:123], v[24:27], v[64:67]\n"
    "v_mfma_f32_16x16x32_bf16 v[72:75], a[176:179], v[24:27], v[72:75]\n"
    "s_waitcnt lgkmcnt(5)\n"
    "v_mfma_f32_16x16x32_bf16 v[52:55], a[8:11], v[28:31], v[52:55]\n"
    "ds_read_b128 v[20:23], v1 offset:19456\n"
    "v_mfma_f32_16x16x32_bf16 v[60:63], a[64:67], v[28:31], v[60:63]\n"
    "v_mfma_f32_16x16x32_bf16 v[68:71], a[120:123], v[28:31], v[68:71]\n"
    "v_mfma_f32_16x16x32_bf16 v[76:79], a[176:179], v[28:31], v[76:79]\n"
    "s_waitcnt lgkmcnt(5)\n"
    "v_mfma_f32_16x16x32_bf16 v[48:51], a[12:15], v[32:35], v[48:51]\n"
    "ds_read_b128 v[24:27], v1 offset:6144\n"
    "v_mfma_f32_16x16x32_bf16 v[56:59], a[68:71], v[32:35], v[56:59]\n"
    "v_mfma_f32_16x16x32_bf16 v[64:67], a[124:127], v[32:35], v[64:67]\n"
    "v_mfma_f32_16x16x32_bf16 v[72:75], a[180:183], v[32:35], v[72:75]\n"
    "s_waitcnt lgkmcnt(5)\n"
    "v_mfma_f32_16x16x32_bf16 v[52:55], a[12:15], v[36:39], v[52:55]\n"
    "ds_read_b128 v[28:31], v1 offset:20480\n"
    "v_mfma_f32_16x16x32_bf16 v[60:63], a[68:71], v[36:39], v[60:63]\n"
    "v_mfma_f32_16x16x32_bf16 v[68:71], a[124:127], v[36:39], v[68:71]\n"
    "v_mfma_f32_16x16x32_bf16 v[76:79], a[180:183], v[36:39], v[76:79]\n"
    "s_waitcnt lgkmcnt(5)\n"
    "v_mfma_f32_16x16x32_bf16 v[48:51], a[16:19], v[8:11], v[48:51]\n"
    "ds_read_b128 v[32:35], v1 offset:7168\n"
    "v_mfma_f32_16x16x32_bf16 v[56:59], a[72:75], v[8:11], v[56:59]\n"
    "v_mfma_f32_16x16x32_bf16 v[64:67], a[128:131], v[8:11], v[64:67]\n"
    "v_mfma_f32_16x16x32_bf16 v[72:75], a[184:187], v[8:11], v[72:75]\n"
    "s_waitcnt lgkmcnt(5)\n"
    "v_mfma_f32_16x16x32_bf16 v[52:55], a[16:19], v[12:15], v[52:55]\n"
    "ds_read_b128 v[36:39], v1 offset:21504\n"
    "v_mfma_f32_16x16x32_bf16 v[60:63], a[72:75], v[12:15], v[60:63]\n"
    "v_mfma_f32_16x16x32_bf16 v[68:71], a[128:131], v[12:15], v[68:71]\n"
    "v_mfma_f32_16x16x32_bf16 v[76:79], a[184:187], v[12:15], v[76:79]\n"
    "s_waitcnt lgkmcnt(5)\n"
    "v_mfma_f32_16x16x32_bf16 v[48:51], a[20:23], v[16:19], v[48:51]\n"
    "ds_read_b128 v[8:11], v1 offset:8192\n"
    "v_mfma_f32_16x16x32_bf16 v[56:59], a[76:79], v[16:19], v[56:59]\n"
    "v_mfma_f32_16x16x32_bf16 v[64:67], a[132:135], v[16:19], v[64:67]\n"
    "v_mfma_f32_16x16x32_bf16 v[72:75], a[188:191], v[16:19], v[72:75]\n"
    "s_waitcnt lgkmcnt(5)\n"
    "v_mfma_f32_16x16x32_bf16 v[52:55], a[20:23], v[20:23], v[52:55]\n"
    "ds_read_b128 v[12:15], v1 offset:22528\n"
    "v_mfma_f32_16x16x32_bf16 v[60:63], a[76:79], v[20:23], v[60:63]\n"
    "v_mfma_f32_16x16x32_bf16 v[68:71], a[132:135], v[20:23], v[68:71]\n"
    "v_mfma_f32_16x16x32_bf16 v[76:79], a[188:191], v[20:23], v[76:79]\n"
    "s_waitcnt lgkmcnt(5)\n"
    "v_mfma_f32_16x16x32_bf16 v[48:51], a[24:27], v[24:27], v[48:51]\n"
    "ds_read_b128 v[16:19], v1 offset:9216\n"
    "v_mfma_f32_16x16x32_bf16 v[56:59], a[80:83], v[24:27], v[56:59]\n"
    "v_mfma_f32_16x16x32_bf16 v[64:67], a[136:139], v[24:27], v[64:67]\n"
    "v_mfma_f32_16x16x32_bf16 v[72:75], a[192:195], v[24:27], v[72:75]\n"
    "s_waitcnt lgkmcnt(5)\n"
    "v_mfma_f32_16x16x32_bf16 v[52:55], a[24:27], v[28:31], v[52:55]\n"
    "ds_read_b128 v[20:23], v1 offset:23552\n"
    "v_mfma_f32_16x16x32_bf16 v[60:63], a[80:83], v[28:31], v[60:63]\n"
    "v_mfma_f32_16x16x32_bf16 v[68:71], a[136:139], v[28:31], v[68:71]\n"
    "v_mfma_f32_16x16x32_bf16 v[76:79], a[192:195], v[28:31], v[76:79]\n"
    "s_waitcnt lgkmcnt(5)\n"
    "v_mfma_f32_16x16x32_bf16 v[48:51], a[28:31], v[32:35], v[48:51]\n"
    "ds_read_b128 v[24:27], v1 offset:10240\n"
    "v_mfma_f32_16x16x32_bf16 v[56:59], a[84:87], v[32:35], v[56:59]\n"
    "v_mfma_f32_16x16x32_bf16 v[64:67], a[140:143], v[32:35], v[64:67]\n"
    "v_mfma_f32_16x16x32_bf16 v[72:75], a[196:199], v[32:35], v[72:75]\n"
    "s_waitcnt lgkmcnt(5)\n"
    "v_mfma_f32_16x16x32_bf16 v[52:55], a[28:31], v[36:39], v[52:55]\n"
    "ds_read_b128 v[28:31], v1 offset:24576\n"
    "v_mfma_f32_16x16x32_bf16 v[60:63], a[84:87], v[36:39], v[60:63]\n"
    "v_mfma_f32_16x16x32_bf16 v[68:71], a[140:143], v[36:39], v[68:71]\n"
    "v_mfma_f32_16x16x32_bf16 v[76:79], a[196:199], v[36:39], v[76:79]\n"
    "s_waitcnt lgkmcnt(5)\n"
    "v_mfma_f32_16x16x32_bf16 v[48:51], a[32:35], v[8:11], v[48:51]\n"
    "ds_read_b128 v[32:35], v1 offset:11264\n"
    "v_mfma_f32_16x16x32_bf16 v[56:59], a[88:91], v[8:11], v[56:59]\n"
    "v_mfma_f32_16x16x32_bf16 v[64:67], a[144:147], v[8:11], v[64:67]\n"
    "v_mfma_f32_16x16x32_bf16 v[72:75], a[200:203], v[8:11], v[72:75]\n"
    "s_waitcnt lgkmcnt(5)\n"
    "v_mfma_f32_16x16x32_bf16 v[52:55], a[32:35], v[12:15], v[52:55]\n"
    "ds_read_b128 v[36:39], v1 offset:25600\n"
    "v_mfma_f32_16x16x32_bf16 v[60:63], a[88:91], v[12:15], v[60:63]\n"
    "v_mfma_f32_16x16x32_bf16 v[68:71], a[144:147], v[12:15], v[68:71]\n"
    "v_mfma_f32_16x16x32_bf16 v[76:79], a[200:203], v[12:15], v[76:79]\n"
    "s_waitcnt lgkmcnt(5)\n"
    "v_mfma_f32_16x16x32_bf16 v[48:51], a[36:39], v[16:19], v[48:51]\n"
    "ds_read_b128 v[8:11], v1 offset:12288\n"
    "v_mfma_f32_16x16x32_bf16 v[56:59], a[92:95], v[16:19], v[56:59]\n"
    "v_mfma_f32_16x16x32_bf16 v[64:67], a[148:151], v[16:19], v[64:67]\n"
    "v_mfma_f32_16x16x32_bf16 v[72:75], a[204:207], v[16:19], v[72:75]\n"
    "s_waitcnt lgkmcnt(5)\n"
    "v_mfma_f32_16x16x32_bf16 v[52:55], a[36:39], v[20:23], v[52:55]\n"
    "ds_read_b128 v[12:15], v1 offset:26624\n"
    "v_mfma_f32_16x16x32_bf16 v[60:63], a[92:95], v[20:23], v[60:63]\n"
    "v_mfma_f32_16x16x32_bf16 v[68:71], a[148:151], v[20:23], v[68:71]\n"
    "v_mfma_f32_16x16x32_bf16 v[76:79], a[204:207], v[20:23], v[76:79]\n"
    "s_waitcnt vmcnt(7)\n"
    "s_barrier\n"
    "s_waitcnt lgkmcnt(5)\n"
    "v_mfma_f32_16x16x32_bf16 v[48:51], a[40:43], v[24:27], v[48:51]\n"
    "ds_read_b128 v[16:19], v1 offset:13312\n"
    "v_mfma_f32_16x16x32_bf16 v[56:59], a[96:99], v[24:27], v[56:59]\n"
    "v_mfma_f32_16x16x32_bf16 v[64:67], a[152:155], v[24:27], v[64:67]\n"
    "v_mfma_f32_16x16x32_bf16 v[72:75], a[208:211], v[24:27], v[72:75]\n"
    "s_waitcnt lgkmcnt(5)\n"
    "v_mfma_f32_16x16x32_bf16 v[52:55], a[40:43], v[28:31], v[52:55]\n"
    "ds_read_b128 v[20:23], v1 offset:27648\n"
    "v_mfma_f32_16x16x32_bf16 v[60:63], a[96:99], v[28:31], v[60:63]\n"
    "v_mfma_f32_16x16x32_bf16 v[68:71], a[152:155], v[28:31], v[68:71]\n"
    "v_mfma_f32_16x16x32_bf16 v[76:79], a[208:211], v[28:31], v[76:79]\n"
    "s_waitcnt lgkmcnt(5)\n"
    "v_mfma_f32_16x16x32_bf16 v[48:51], a[44:47], v[32:35], v[48:51]\n"
    "v_mfma_f32_16x16x32_bf16 v[56:59], a[100:103], v[32:35], v[56:59]\n"
    "v_mfma_f32_16x16x32_bf16 v[64:67], a[156:159], v[32:35], v[64:67]\n"
    "v_mfma_f32_16x16x32_bf16 v[72:75], a[212:215], v[32:35], v[72:75]\n"
    "s_waitcnt lgkmcnt(4)\n"
    "v_mfma_f32_16x16x32_bf16 v[52:55], a[44:47], v[36:39], v[52:55]\n"
    "v_mfma_f32_16x16x32_bf16 v[60:63], a[100:103], v[36:39], v[60:63]\n"
    "v_mfma_f32_16x16x32_bf16 v[68:71], a[156:159], v[36:39], v[68:71]\n"
    "v_mfma_f32_16x16x32_bf16 v[76:79], a[212:215], v[36:39], v[76:79]\n"
    "s_waitcnt lgkmcnt(3)\n"
    "v_mfma_f32_16x16x32_bf16 v[48:51], a[48:51], v[8:11], v[48:51]\n"
    "v_mfma_f32_16x16x32_bf16 v[56:59], a[104:107], v[8:11], v[56:59]\n"
    "v_mfma_f32_16x16x32_bf16 v[64:67], a[160:163], v[8:11], v[64:67]\n"
    "v_mfma_f32_16x16x32_bf16 v[72:75], a[216:219], v[8:11], v[72:75]\n"
    "s_waitcnt lgkmcnt(2)\n"
    "v_mfma_f32_16x16x32_bf16 v[52:55], a[48:51], v[12:15], v[52:55]\n"
    "v_mfma_f32_16x16x32_bf16 v[60:63], a[104:107], v[12:15], v[60:63]\n"
    "v_mfma_f32_16x16x32_bf16 v[68:71], a[160:163], v[12:15], v[68:71]\n"
    "v_mfma_f32_16x16x32_bf16 v[76:79], a[216:219], v[12:15], v[76:79]\n"
    "s_waitcnt lgkmcnt(1)\n"
    "v_mfma_f32_16x16x32_bf16 v[48:51], a[52:55], v[16:19], v[48:51]\n"
    "v_mfma_f32_16x16x32_bf16 v[56:59], a[108:111], v[16:19], v[56:59]\n"
    "v_mfma_f32_16x16x32_bf16 v[64:67], a[164:167], v[16:19], v[64:67]\n"
    "v_mfma_f32_16x16x32_bf16 v[72:75], a[220:223], v[16:19], v[72:75]\n"
    "s_waitcnt lgkmcnt(0)\n"
    "v_mfma_f32_16x16x32_bf16 v[52:55], a[52:55], v[20:23], v[52:55]\n"
    "v_mfma_f32_16x16x32_bf16 v[60:63], a[108:111], v[20:23], v[60:63]\n"
    "v_mfma_f32_16x16x32_bf16 v[68:71], a[164:167], v[20:23], v[68:71]\n"
    "v_mfma_f32_16x16x32_bf16 v[76:79], a[220:223], v[20:23], v[76:79]\n"
    "v_add_u32 v1, 28672, v1\n"
    "v_cmp_le_u32 vcc, 86016, v1\n"
    "s_add_u32 s44, s44, 1\n"
    "s_nop 0\n"
    "v_cndmask_b32 v1, v1, v0, vcc\n"
    ".p2align 6\n"
    "LP0_%=_loop:\n"
    "v_mov_b32 v2, s44\n"
    "s_barrier\n"
    "ds_read_b128 v[8:11], v1 offset:0\n"
    "ds_read_b128 v[12:15], v1 offset:14336\n"
    "ds_read_b128 v[16:19], v1 offset:1024\n"
    "ds_read_b128 v[20:23], v1 offset:15360\n"
    "ds_read_b128 v[24:27], v1 offset:2048\n"
    "ds_read_b128 v[28:31], v1 offset:16384\n"
    "v_cvt_pk_bf16_f32 v112, v48, v49\n"
    "v_cvt_pk_bf16_f32 v113, v50, v51\n"
    "v_cvt_pk_bf16_f32 v114, v56, v57\n"
    "v_cvt_pk_bf16_f32 v115, v58, v59\n"
    "v_cvt_pk_bf16_f32 v116, v52, v53\n"
    "v_cvt_pk_bf16_f32 v117, v54, v55\n"
    "v_cvt_pk_bf16_f32 v118, v60, v61\n"
    "v_cvt_pk_bf16_f32 v119, v62, v63\n"
    "s_waitcnt lgkmcnt(5)\n"
    "v_mfma_f32_16x16x32_bf16 v[80:83], a[0:3], v[8:11], 0\n"
    "ds_read_b128 v[32:35], v1 offset:3072\n"
    "v_mfma_f32_16x16x32_bf16 v[88:91], a[56:59], v[8:11], 0\n"
    "v_cvt_pk_bf16_f32 v120, v64, v65\n"
    "v_mfma_f32_16x16x32_bf16 v[96:99], a[112:115], v[8:11], 0\n"
    "v_mfma_f32_16x16x32_bf16 v[104:107], a[168:171], v[8:11], 0\n"
    "v_cvt_pk_bf16_f32 v121, v66, v67\n"
    "s_waitcnt lgkmcnt(5)\n"
    "v_mfma_f32_16x16x32_bf16 v[84:87], a[0:3], v[12:15], 0\n"
    "ds_read_b128 v[36:39], v1 offset:17408\n"
    "v_mfma_f32_16x16x32_bf16 v[92:95], a[56:59], v[12:15], 0\n"
    "v_cvt_pk_bf16_f32 v122, v72, v73\n"
    "v_mfma_f32_16x16x32_bf16 v[100:103], a[112:115], v[12:15], 0\n"
    "v_mfma_f32_16x16x32_bf16 v[108:111], a[168:171], v[12:15], 0\n"
    "v_cvt_pk_bf16_f32 v123, v74, v75\n"
    "s_waitcnt lgkmcnt(5)\n"
    "v_mfma_f32_16x16x32_bf16 v[80:83], a[4:7], v[16:19], v[80:83]\n"
    "ds_read_b128 v[8:11], v1 offset:4096\n"
    "v_mfma_f32_16x16x32_bf16 v[88:91], a[60:63], v[16:19], v[88:91]\n"
    "v_cvt_pk_bf16_f32 v124, v68, v69\n"
    "v_mfma_f32_16x16x32_bf16 v[96:99], a[116:119], v[16:19], v[96:99]\n"
    "v_mfma_f32_16x16x32_bf16 v[104:107], a[172:175], v[16:19], v[104:107]\n"
    "v_cvt_pk_bf16_f32 v125, v70, v71\n"
    "s_waitcnt lgkmcnt(5)\n"
    "v_mfma_f32_16x16x32_bf16 v[84:87], a[4:7], v[20:23], v[84:87]\n"
    "ds_read_b128 v[12:15], v1 offset:18432\n"
    "v_mfma_f32_16x16x32_bf16 v[92:95], a[60:63], v[20:23], v[92:95]\n"
    "v_cvt_pk_bf16_f32 v126, v76, v77\n"
    "v_mfma_f32_16x16x32_bf16 v[100:103], a[116:119], v[20:23], v[100:103]\n"
    "v_mfma_f32_16x16x32_bf16 v[108:111], a[172:175], v[20:23], v[108:111]\n"
    "v_cvt_pk_bf16_f32 v127, v78, v79\n"
    "s_waitcnt lgkmcnt(5)\n"
    "v_mfma_f32_16x16x32_bf16 v[80:83], a[8:11], v[24:27], v[80:83]\n"
    "ds_read_b128 v[16:19], v1 offset:5120\n"
    "v_mfma_f32_16x16x32_bf16 v[88:91], a[64:67], v[24:27], v[88:91]\n"
    "s_add_u32 s49, s55, 0\n"
    "buffer_store_dwordx4 v[112:115], v0, s[40:43], s49 offen sc1\n"
    "v_mfma_f32_16x16x32_bf16 v[96:99], a[120:123], v[24:27], v[96:99]\n"
    "v_mfma_f32_16x16x32_bf16 v[104:107], a[176:179], v[24:27], v[104:107]\n"
    "s_add_u32 s49, s55, 14336\n"
    "buffer_store_dwordx4 v[116:119], v0, s[40:43], s49 offen sc1\n"
    "s_waitcnt lgkmcnt(5)\n"
    "v_mfma_f32_16x16x32_bf16 v[84:87], a[8:11], v[28:31], v[84:87]\n"
    "ds_read_b128 v[20:23], v1 offset:19456\n"
    "v_mfma_f32_16x16x32_bf16 v[92:95], a[64:67], v[28:31], v[92:95]\n"
    "s_add_u32 s49, s55, 1024\n"
    "buffer_store_dwordx4 v[120:123], v0, s[40:43], s49 offen sc1\n"
    "v_mfma_f32_16x16x32_bf16 v[100:103], a[120:123], v[28:31], v[100:103]\n"
    "v_mfma_f32_16x16x32_bf16 v[108:111], a[176:179], v[28:31], v[108:111]\n"
    "s_add_u32 s49, s55, 15360\n"
    "buffer_store_dwordx4 v[124:127], v0, s[40:43], s49 offen sc1\n"
    "s_waitcnt lgkmcnt(5)\n"
    "v_mfma_f32_16x16x32_bf16 v[80:83], a[12:15], v[32:35], v[80:83]\n"
    "ds_read_b128 v[24:27], v1 offset:6144\n"
    "v_mfma_f32_16x16x32_bf16 v[88:91], a[68:71], v[32:35], v[88:91]\n"
    "s_add_u32 s55, s55, 458752\n"
    "v_mfma_f32_16x16x32_bf16 v[96:99], a[124:127], v[32:35], v[96:99]\n"
    "v_mfma_f32_16x16x32_bf16 v[104:107], a[180:183], v[32:35], v[104:107]\n"
    "s_add_u32 m0, s58, 0\n"
    "s_add_u32 s53, s57, 0\n"
    "buffer_load_dwordx4 v0, s[40:43], s53 offen offset:0 lds\n"
    "s_waitcnt lgkmcnt(5)\n"
    "v_mfma_f32_16x16x32_bf16 v[84:87], a[12:15], v[36:39], v[84:87]\n"
    "ds_read_b128 v[28:31], v1 offset:20480\n"
    "v_mfma_f32_16x16x32_bf16 v[92:95], a[68:71], v[36:39], v[92:95]\n"
    "buffer_load_dwordx4 v0, s[40:43], s53 offen offset:1024 lds\n"
    "v_mfma_f32_16x16x32_bf16 v[100:103], a[124:127], v[36:39], v[100:103]\n"
    "v_mfma_f32_16x16x32_bf16 v[108:111], a[180:183], v[36:39], v[108:111]\n"
    "buffer_load_dwordx4 v0, s[40:43], s53 offen offset:2048 lds\n"
    "s_waitcnt lgkmcnt(5)\n"
    "v_mfma_f32_16x16x32_bf16 v[80:83], a[16:19], v[8:11], v[80:83]\n"
    "ds_read_b128 v[32:35], v1 offset:7168\n"
    "v_mfma_f32_16x16x32_bf16 v[88:91], a[72:75], v[8:11], v[88:91]\n"
    "buffer_load_dwordx4 v0, s[40:43], s53 offen offset:3072 lds\n"
    "v_mfma_f32_16x16x32_bf16 v[96:99], a[128:131], v[8:11], v[96:99]\n"
    "v_mfma_f32_16x16x32_bf16 v[104:107], a[184:187], v[8:11], v[104:107]\n"
    "s_add_u32 m0, s58, 4096\n"
    "s_add_u32 s53, s57, 4096\n"
    "buffer_load_dwordx4 v0, s[40:43], s53 offen offset:0 lds\n"
    "s_waitcnt lgkmcnt(5)\n"
    "v_mfma_f32_16x16x32_bf16 v[84:87], a[16:19], v[12:15], v[84:87]\n"
    "ds_read_b128 v[36:39], v1 offset:21504\n"
    "v_mfma_f32_16x16x32_bf16 v[92:95], a[72:75], v[12:15], v[92:95]\n"
    "buffer_load_dwordx4 v0, s[40:43], s53 offen offset:1024 lds\n"
    "v_mfma_f32_16x16x32_bf16 v[100:103], a[128:131], v[12:15], v[100:103]\n"
    "v_mfma_f32_16x16x32_bf16 v[108:111], a[184:187], v[12:15], v[108:111]\n"
    "buffer_load_dwordx4 v0, s[40:43], s53 offen offset:2048 lds\n"
    "s_waitcnt lgkmcnt(5)\n"
    "v_mfma_f32_16x16x32_bf16 v[80:83], a[20:23], v[16:19], v[80:83]\n"
    "ds_read_b128 v[8:11], v1 offset:8192\n"
    "v_mfma_f32_16x16x32_bf16 v[88:91], a[76:79], v[16:19], v[88:91]\n"
    "s_add_u32 s57, s57, 28672\n"
    "s_add_u32 s58, s58, 28672\n"
    "s_cmp_ge_u32 s58, 86016\n"
    "s_cselect_b32 s58, s59, s58\n"
    "v_mfma_f32_16x16x32_bf16 v[96:99], a[132:135], v[16:19], v[96:99]\n"
    "v_mfma_f32_16x16x32_bf16 v[104:107], a[188:191], v[16:19], v[104:107]\n"
    "s_waitcnt lgkmcnt(5)\n"
    "v_mfma_f32_16x16x32_bf16 v[84:87], a[20:23], v[20:23], v[84:87]\n"
    "ds_read_b128 v[12:15], v1 offset:22528\n"
    "v_mfma_f32_16x16x32_bf16 v[92:95], a[76:79], v[20:23], v[92:95]\n"
    "v_mfma_f32_16x16x32_bf16 v[100:103], a[132:135], v[20:23], v[100:103]\n"
    "v_mfma_f32_16x16x32_bf16 v[108:111], a[188:191], v[20:23], v[108:111]\n"
    "s_waitcnt lgkmcnt(5)\n"
    "v_mfma_f32_16x16x32_bf16 v[80:83], a[24:27], v[24:27], v[80:83]\n"
    "ds_read_b128 v[16:19], v1 offset:9216\n"
    "v_mfma_f32_16x16x32_bf16 v[88:91], a[80:83], v[24:27], v[88:91]\n"
    "v_mfma_f32_16x16x32_bf16 v[96:99], a[136:139], v[24:27], v[96:99]\n"
    "v_mfma_f32_16x16x32_bf16 v[104:107], a[192:195], v[24:27], v[104:107]\n"
    "s_waitcnt lgkmcnt(5)\n"
    "v_mfma_f32_16x16x32_bf16 v[84:87], a[24:27], v[28:31], v[84:87]\n"
    "ds_read_b128 v[20:23], v1 offset:23552\n"
    "v_mfma_f32_16x16x32_bf16 v[92:95], a[80:83], v[28:31], v[92:95]\n"
    "v_mfma_f32_16x16x32_bf16 v[100:103], a[136:139], v[28:31], v[100:103]\n"
    "v_mfma_f32_16x16x32_bf16 v[108:111], a[192:195], v[28:31], v[108:111]\n"
    "s_waitcnt lgkmcnt(5)\n"
    "v_mfma_f32_16x16x32_bf16 v[80:83], a[28:31], v[32:35], v[80:83]\n"
    "ds_read_b128 v[24:27], v1 offset:10240\n"
    "v_mfma_f32_16x16x32_bf16 v[88:91], a[84:87], v[32:35], v[88:91]\n"
    "v_mfma_f32_16x16x32_bf16 v[96:99], a[140:143], v[32:35], v[96:99]\n"
    "v_mfma_f32_16x16x32_bf16 v[104:107], a[196:199], v[32:35], v[104:107]\n"
    "s_waitcnt lgkmcnt(5)\n"
    "v_mfma_f32_16x16x32_bf16 v[84:87], a[28:31], v[36:39], v[84:87]\n"
    "ds_read_b128 v[28:31], v1 offset:24576\n"
    "v_mfma_f32_16x16x32_bf16 v[92:95], a[84:87], v[36:39], v[92:95]\n"
    "v_mfma_f32_16x16x32_bf16 v[100:103], a[140:143], v[36:39], v[100:103]\n"
    "v_mfma_f32_16x16x32_bf16 v[108:111], a[196:199], v[36:39], v[108:111]\n"
    "s_waitcnt lgkmcnt(5)\n"
    "v_mfma_f32_16x16x32_bf16 v[80:83], a[32:35], v[8:11], v[80:83]\n"
    "ds_read_b128 v[32:35], v1 offset:11264\n"
    "v_mfma_f32_16x16x32_bf16 v[88:91], a[88:91], v[8:11], v[88:91]\n"
    "v_mfma_f32_16x16x32_bf16 v[96:99], a[144:147], v[8:11], v[96:99]\n"
    "v_mfma_f32_16x16x32_bf16 v[104:107], a[200:203], v[8:11], v[104:107]\n"
    "s_waitcnt lgkmcnt(5)\n"
    "v_mfma_f32_16x16x32_bf16 v[84:87], a[32:35], v[12:15], v[84:87]\n"
    "ds_read_b128 v[36:39], v1 offset:25600\n"
    "v_mfma_f32_16x16x32_bf16 v[92:95], a[88:91], v[12:15], v[92:95]\n"
    "v_mfma_f32_16x16x32_bf16 v[100:103], a[144:147], v[12:15], v[100:103]\n"
    "v_mfma_f32_16x16x32_bf16 v[108:111], a[200:203], v[12:15], v[108:111]\n"
    "s_waitcnt lgkmcnt(5)\n"
    "v_mfma_f32_16x16x32_bf16 v[80:83], a[36:39], v[16:19], v[80:83]\n"
    "ds_read_b128 v[8:11], v1 offset:12288\n"
    "v_mfma_f32_16x16x32_bf16 v[88:91], a[92:95], v[16:19], v[88:91]\n"
    "v_mfma_f32_16x16x32_bf16 v[96:99], a[148:151], v[16:19], v[96:99]\n"
    "v_mfma_f32_16x16x32_bf16 v[104:107], a[204:207], v[16:19], v[104:107]\n"
    "s_waitcnt lgkmcnt(5)\n"
    "v_mfma_f32_16x16x32_bf16 v[84:87], a[36:39], v[20:23], v[84:87]\n"
    "ds_read_b128 v[12:15], v1 offset:26624\n"
    "v_mfma_f32_16x16x32_bf16 v[92:95], a[92:95], v[20:23], v[92:95]\n"
    "v_mfma_f32_16x16x32_bf16 v[100:103], a[148:151], v[20:23], v[100:103]\n"
    "v_mfma_f32_16x16x32_bf16 v[108:111], a[204:207], v[20:23], v[108:111]\n"
    "s_waitcnt vmcnt(7)\n"
    "s_barrier\n"
    "buffer_store_dword v2, v3, s[40:43], 0 offen sc1\n"
    "s_waitcnt lgkmcnt(5)\n"
    "v_mfma_f32_16x16x32_bf16 v[80:83], a[40:43], v[24:27], v[80:83]\n"
    "ds_read_b128 v[16:19], v1 offset:13312\n"
    "v_mfma_f32_16x16x32_bf16 v[88:91], a[96:99], v[24:27], v[88:91]\n"
    "v_mfma_f32_16x16x32_bf16 v[96:99], a[152:155], v[24:27], v[96:99]\n"
    "v_mfma_f32_16x16x32_bf16 v[104:107], a[208:211], v[24:27], v[104:107]\n"
    "s_waitcnt lgkmcnt(5)\n"
    "v_mfma_f32_16x16x32_bf16 v[84:87], a[40:43], v[28:31], v[84:87]\n"
    "ds_read_b128 v[20:23], v1 offset:27648\n"
    "v_mfma_f32_16x16x32_bf16 v[92:95], a[96:99], v[28:31], v[92:95]\n"
    "v_mfma_f32_16x16x32_bf16 v[100:103], a[152:155], v[28:31], v[100:103]\n"
    "v_mfma_f32_16x16x32_bf16 v[108:111], a[208:211], v[28:31], v[108:111]\n"
    "s_waitcnt lgkmcnt(5)\n"
    "v_mfma_f32_16x16x32_bf16 v[80:83], a[44:47], v[32:35], v[80:83]\n"
    "v_mfma_f32_16x16x32_bf16 v[88:91], a[100:103], v[32:35], v[88:91]\n"
    "v_mfma_f32_16x16x32_bf16 v[96:99], a[156:159], v[32:35], v[96:99]\n"
    "v_mfma_f32_16x16x32_bf16 v[104:107], a[212:215], v[32:35], v[104:107]\n"
    "s_waitcnt lgkmcnt(4)\n"
    "v_mfma_f32_16x16x32_bf16 v[84:87], a[44:47], v[36:39], v[84:87]\n"
    "v_mfma_f32_16x16x32_bf16 v[92:95], a[100:103], v[36:39], v[92:95]\n"
    "v_mfma_f32_16x16x32_bf16 v[100:103], a[156:159], v[36:39], v[100:103]\n"
    "v_mfma_f32_16x16x32_bf16 v[108:111], a[212:215], v[36:39], v[108:111]\n"
    "s_waitcnt lgkmcnt(3)\n"
    "v_mfma_f32_16x16x32_bf16 v[80:83], a[48:51], v[8:11], v[80:83]\n"
    "v_mfma_f32_16x16x32_bf16 v[88:91], a[104:107], v[8:11], v[88:91]\n"
    "v_mfma_f32_16x16x32_bf16 v[96:99], a[160:163], v[8:11], v[96:99]\n"
    "v_mfma_f32_16x16x32_bf16 v[104:107], a[216:219], v[8:11], v[104:107]\n"
    "s_waitcnt lgkmcnt(2)\n"
    "v_mfma_f32_16x16x32_bf16 v[84:87], a[48:51], v[12:15], v[84:87]\n"
    "v_mfma_f32_16x16x32_bf16 v[92:95], a[104:107], v[12:15], v[92:95]\n"
    "v_mfma_f32_16x16x32_bf16 v[100:103], a[160:163], v[12:15], v[100:103]\n"
    "v_mfma_f32_16x16x32_bf16 v[108:111], a[216:219], v[12:15], v[108:111]\n"
    "s_waitcnt lgkmcnt(1)\n"
    "v_mfma_f32_16x16x32_bf16 v[80:83], a[52:55], v[16:19], v[80:83]\n"
    "v_mfma_f32_16x16x32_bf16 v[88:91], a[108:111], v[16:19], v[88:91]\n"
    "v_mfma_f32_16x16x32_bf16 v[96:99], a[164:167], v[16:19], v[96:99]\n"
    "v_mfma_f32_16x16x32_bf16 v[104:107], a[220:223], v[16:19], v[104:107]\n"
    "s_waitcnt lgkmcnt(0)\n"
    "v_mfma_f32_16x16x32_bf16 v[84:87], a[52:55], v[20:23], v[84:87]\n"
    "v_mfma_f32_16x16x32_bf16 v[92:95], a[108:111], v[20:23], v[92:95]\n"
    "v_mfma_f32_16x16x32_bf16 v[100:103], a[164:167], v[20:23], v[100:103]\n"
    "v_mfma_f32_16x16x32_bf16 v[108:111], a[220:223], v[20:23], v[108:111]\n"
    "v_add_u32 v1, 28672, v1\n"
    "v_cmp_le_u32 vcc, 86016, v1\n"
    "s_add_u32 s44, s44, 1\n"
    "s_nop 0\n"
    "v_cndmask_b32 v1, v1, v0, vcc\n"
    "s_cmp_eq_u32 s44, 64\n"
    "s_cbranch_scc1 LP0_%=_end\n"
    "v_mov_b32 v2, s44\n"
    "s_barrier\n"
    "ds_read_b128 v[8:11], v1 offset:0\n"
    "ds_read_b128 v[12:15], v1 offset:14336\n"
    "ds_read_b128 v[16:19], v1 offset:1024\n"
    "ds_read_b128 v[20:23], v1 offset:15360\n"
    "ds_read_b128 v[24:27], v1 offset:2048\n"
    "ds_read_b128 v[28:31], v1 offset:16384\n"
    "v_cvt_pk_bf16_f32 v112, v80, v81\n"
    "v_cvt_pk_bf16_f32 v113, v82, v83\n"
    "v_cvt_pk_bf16_f32 v114, v88, v89\n"
    "v_cvt_pk_bf16_f32 v115, v90, v91\n"
    "v_cvt_pk_bf16_f32 v116, v84, v85\n"
    "v_cvt_pk_bf16_f32 v117, v86, v87\n"
    "v_cvt_pk_bf16_f32 v118, v92, v93\n"
    "v_cvt_pk_bf16_f32 v119, v94, v95\n"
    "s_waitcnt lgkmcnt(5)\n"
    "v_mfma_f32_16x16x32_bf16 v[48:51], a[0:3], v[8:11], 0\n"
    "ds_read_b128 v[32:35], v1 offset:3072\n"
    "v_mfma_f32_16x16x32_bf16 v[56:59], a[56:59], v[8:11], 0\n"
    "v_cvt_pk_bf16_f32 v120, v96, v97\n"
    "v_mfma_f32_16x16x32_bf16 v[64:67], a[112:115], v[8:11], 0\n"
    "v_mfma_f32_16x16x32_bf16 v[72:75], a[168:171], v[8:11], 0\n"
    "v_cvt_pk_bf16_f32 v121, v98, v99\n"
    "s_waitcnt lgkmcnt(5)\n"
    "v_mfma_f32_16x16x32_bf16 v[52:55], a[0:3], v[12:15], 0\n"
    "ds_read_b128 v[36:39], v1 offset:17408\n"
    "v_mfma_f32_16x16x32_bf16 v[60:63], a[56:59], v[12:15], 0\n"
    "v_cvt_pk_bf16_f32 v122, v104, v105\n"
    "v_mfma_f32_16x16x32_bf16 v[68:71], a[112:115], v[12:15], 0\n"
    "v_mfma_f32_16x16x32_bf16 v[76:79], a[168:171], v[12:15], 0\n"
    "v_cvt_pk_bf16_f32 v123, v106, v107\n"
    "s_waitcnt lgkmcnt(5)\n"
    "v_mfma_f32_16x16x32_bf16 v[48:51], a[4:7], v[16:19], v[48:51]\n"
    "ds_read_b128 v[8:11], v1 offset:4096\n"
    "v_mfma_f32_16x16x32_bf16 v[56:59], a[60:63], v[16:19], v[56:59]\n"
    "v_cvt_pk_bf16_f32 v124, v100, v101\n"
    "v_mfma_f32_16x16x32_bf16 v[64:67], a[116:119], v[16:19], v[64:67]\n"
    "v_mfma_f32_16x16x32_bf16 v[72:75], a[172:175], v[16:19], v[72:75]\n"
    "v_cvt_pk_bf16_f32 v125, v102, v103\n"
    "s_waitcnt lgkmcnt(5)\n"
    "v_mfma_f32_16x16x32_bf16 v[52:55], a[4:7], v[20:23], v[52:55]\n"
    "ds_read_b128 v[12:15], v1 offset:18432\n"
    "v_mfma_f32_16x16x32_bf16 v[60:63], a[60:63], v[20:23], v[60:63]\n"
    "v_cvt_pk_bf16_f32 v126, v108, v109\n"
    "v_mfma_f32_16x16x32_bf16 v[68:71], a[116:119], v[20:23], v[68:71]\n"
    "v_mfma_f32_16x16x32_bf16 v[76:79], a[172:175], v[20:23], v[76:79]\n"
    "v_cvt_pk_bf16_f32 v127, v110, v111\n"
    "s_waitcnt lgkmcnt(5)\n"
    "v_mfma_f32_16x16x32_bf16 v[48:51], a[8:11], v[24:27], v[48:51]\n"
    "ds_read_b128 v[16:19], v1 offset:5120\n"
    "v_mfma_f32_16x16x32_bf16 v[56:59], a[64:67], v[24:27], v[56:59]\n"
    "s_add_u32 s49, s55, 0\n"
    "buffer_store_dwordx4 v[112:115], v0, s[40:43], s49 offen sc1\n"
    "v_mfma_f32_16x16x32_bf16 v[64:67], a[120:123], v[24:27], v[64:67]\n"
    "v_mfma_f32_16x16x32_bf16 v[72:75], a[176:179], v[24:27], v[72:75]\n"
    "s_add_u32 s49, s55, 14336\n"
    "buffer_store_dwordx4 v[116:119], v0, s[40:43], s49 offen sc1\n"
    "s_waitcnt lgkmcnt(5)\n"
    "v_mfma_f32_16x16x32_bf16 v[52:55], a[8:11], v[28:31], v[52:55]\n"
    "ds_read_b128 v[20:23], v1 offset:19456\n"
    "v_mfma_f32_16x16x32_bf16 v[60:63], a[64:67], v[28:31], v[60:63]\n"
    "s_add_u32 s49, s55, 1024\n"
    "buffer_store_dwordx4 v[120:123], v0, s[40:43], s49 offen sc1\n"
    "v_mfma_f32_16x16x32_bf16 v[68:71], a[120:123], v[28:31], v[68:71]\n"
    "v_mfma_f32_16x16x32_bf16 v[76:79], a[176:179], v[28:31], v[76:79]\n"
    "s_add_u32 s49, s55, 15360\n"
    "buffer_store_dwordx4 v[124:127], v0, s[40:43], s49 offen sc1\n"
    "s_waitcnt lgkmcnt(5)\n"
    "v_mfma_f32_16x16x32_bf16 v[48:51], a[12:15], v[32:35], v[48:51]\n"
    "ds_read_b128 v[24:27], v1 offset:6144\n"
    "v_mfma_f32_16x16x32_bf16 v[56:59], a[68:71], v[32:35], v[56:59]\n"
    "s_add_u32 s55, s55, 458752\n"
    "v_mfma_f32_16x16x32_bf16 v[64:67], a[124:127], v[32:35], v[64:67]\n"
    "v_mfma_f32_16x16x32_bf16 v[72:75], a[180:183], v[32:35], v[72:75]\n"
    "s_add_u32 m0, s58, 0\n"
    "s_add_u32 s53, s57, 0\n"
    "buffer_load_dwordx4 v0, s[40:43], s53 offen offset:0 lds\n"
    "s_waitcnt lgkmcnt(5)\n"
    "v_mfma_f32_16x16x32_bf16 v[52:55], a[12:15], v[36:39], v[52:55]\n"
    "ds_read_b128 v[28:31], v1 offset:20480\n"
    "v_mfma_f32_16x16x32_bf16 v[60:63], a[68:71], v[36:39], v[60:63]\n"
    "buffer_load_dwordx4 v0, s[40:43], s53 offen offset:1024 lds\n"
    "v_mfma_f32_16x16x32_bf16 v[68:71], a[124:127], v[36:39], v[68:71]\n"
    "v_mfma_f32_16x16x32_bf16 v[76:79], a[180:183], v[36:39], v[76:79]\n"
    "buffer_load_dwordx4 v0, s[40:43], s53 offen offset:2048 lds\n"
    "s_waitcnt lgkmcnt(5)\n"
    "v_mfma_f32_16x16x32_bf16 v[48:51], a[16:19], v[8:11], v[48:51]\n"
    "ds_read_b128 v[32:35], v1 offset:7168\n"
    "v_mfma_f32_16x16x32_bf16 v[56:59], a[72:75], v[8:11], v[56:59]\n"
    "buffer_load_dwordx4 v0, s[40:43], s53 offen offset:3072 lds\n"
    "v_mfma_f32_16x16x32_bf16 v[64:67], a[128:131], v[8:11], v[64:67]\n"
    "v_mfma_f32_16x16x32_bf16 v[72:75], a[184:187], v[8:11], v[72:75]\n"
    "s_add_u32 m0, s58, 4096\n"
    "s_add_u32 s53, s57, 4096\n"
    "buffer_load_dwordx4 v0, s[40:43], s53 offen offset:0 lds\n"
    "s_waitcnt lgkmcnt(5)\n"
    "v_mfma_f32_16x16x32_bf16 v[52:55], a[16:19], v[12:15], v[52:55]\n"
    "ds_read_b128 v[36:39], v1 offset:21504\n"
    "v_mfma_f32_16x16x32_bf16 v[60:63], a[72:75], v[12:15], v[60:63]\n"
    "buffer_load_dwordx4 v0, s[40:43], s53 offen offset:1024 lds\n"
    "v_mfma_f32_16x16x32_bf16 v[68:71], a[128:131], v[12:15], v[68:71]\n"
    "v_mfma_f32_16x16x32_bf16 v[76:79], a[184:187], v[12:15], v[76:79]\n"
    "buffer_load_dwordx4 v0, s[40:43], s53 offen offset:2048 lds\n"
    "s_waitcnt lgkmcnt(5)\n"
    "v_mfma_f32_16x16x32_bf16 v[48:51], a[20:23], v[16:19], v[48:51]\n"
    "ds_read_b128 v[8:11], v1 offset:8192\n"
    "v_mfma_f32_16x16x32_bf16 v[56:59], a[76:79], v[16:19], v[56:59]\n"
    "s_add_u32 s57, s57, 28672\n"
    "s_add_u32 s58, s58, 28672\n"
    "s_cmp_ge_u32 s58, 86016\n"
    "s_cselect_b32 s58, s59, s58\n"
    "v_mfma_f32_16x16x32_bf16 v[64:67], a[132:135], v[16:19], v[64:67]\n"
    "v_mfma_f32_16x16x32_bf16 v[72:75], a[188:191], v[16:19], v[72:75]\n"
    "s_waitcnt lgkmcnt(5)\n"
    "v_mfma_f32_16x16x32_bf16 v[52:55], a[20:23], v[20:23], v[52:55]\n"
    "ds_read_b128 v[12:15], v1 offset:22528\n"
    "v_mfma_f32_16x16x32_bf16 v[60:63], a[76:79], v[20:23], v[60:63]\n"
    "v_mfma_f32_16x16x32_bf16 v[68:71], a[132:135], v[20:23], v[68:71]\n"
    "v_mfma_f32_16x16x32_bf16 v[76:79], a[188:191], v[20:23], v[76:79]\n"
    "s_waitcnt lgkmcnt(5)\n"
    "v_mfma_f32_16x16x32_bf16 v[48:51], a[24:27], v[24:27], v[48:51]\n"
    "ds_read_b128 v[16:19], v1 offset:9216\n"
    "v_mfma_f32_16x16x32_bf16 v[56:59], a[80:83], v[24:27], v[56:59]\n"
    "v_mfma_f32_16x16x32_bf16 v[64:67], a[136:139], v[24:27], v[64:67]\n"
    "v_mfma_f32_16x16x32_bf16 v[72:75], a[192:195], v[24:27], v[72:75]\n"
    "s_waitcnt lgkmcnt(5)\n"
    "v_mfma_f32_16x16x32_bf16 v[52:55], a[24:27], v[28:31], v[52:55]\n"
    "ds_read_b128 v[20:23], v1 offset:23552\n"
    "v_mfma_f32_16x16x32_bf16 v[60:63], a[80:83], v[28:31], v[60:63]\n"
    "v_mfma_f32_16x16x32_bf16 v[68:71], a[136:139], v[28:31], v[68:71]\n"
    "v_mfma_f32_16x16x32_bf16 v[76:79], a[192:195], v[28:31], v[76:79]\n"
    "s_waitcnt lgkmcnt(5)\n"
    "v_mfma_f32_16x16x32_bf16 v[48:51], a[28:31], v[32:35], v[48:51]\n"
    "ds_read_b128 v[24:27], v1 offset:10240\n"
    "v_mfma_f32_16x16x32_bf16 v[56:59], a[84:87], v[32:35], v[56:59]\n"
    "v_mfma_f32_16x16x32_bf16 v[64:67], a[140:143], v[32:35], v[64:67]\n"
    "v_mfma_f32_16x16x32_bf16 v[72:75], a[196:199], v[32:35], v[72:75]\n"
    "s_waitcnt lgkmcnt(5)\n"
    "v_mfma_f32_16x16x32_bf16 v[52:55], a[28:31], v[36:39], v[52:55]\n"
    "ds_read_b128 v[28:31], v1 offset:24576\n"
    "v_mfma_f32_16x16x32_bf16 v[60:63], a[84:87], v[36:39], v[60:63]\n"
    "v_mfma_f32_16x16x32_bf16 v[68:71], a[140:143], v[36:39], v[68:71]\n"
    "v_mfma_f32_16x16x32_bf16 v[76:79], a[196:199], v[36:39], v[76:79]\n"
    "s_waitcnt lgkmcnt(5)\n"
    "v_mfma_f32_16x16x32_bf16 v[48:51], a[32:35], v[8:11], v[48:51]\n"
    "ds_read_b128 v[32:35], v1 offset:11264\n"
    "v_mfma_f32_16x16x32_bf16 v[56:59], a[88:91], v[8:11], v[56:59]\n"
    "v_mfma_f32_16x16x32_bf16 v[64:67], a[144:147], v[8:11], v[64:67]\n"
    "v_mfma_f32_16x16x32_bf16 v[72:75], a[200:203], v[8:11], v[72:75]\n"
    "s_waitcnt lgkmcnt(5)\n"
    "v_mfma_f32_16x16x32_bf16 v[52:55], a[32:35], v[12:15], v[52:55]\n"
    "ds_read_b128 v[36:39], v1 offset:25600\n"
    "v_mfma_f32_16x16x32_bf16 v[60:63], a[88:91], v[12:15], v[60:63]\n"
    "v_mfma_f32_16x16x32_bf16 v[68:71], a[144:147], v[12:15], v[68:71]\n"
    "v_mfma_f32_16x16x32_bf16 v[76:79], a[200:203], v[12:15], v[76:79]\n"
    "s_waitcnt lgkmcnt(5)\n"
    "v_mfma_f32_16x16x32_bf16 v[48:51], a[36:39], v[16:19], v[48:51]\n"
    "ds_read_b128 v[8:11], v1 offset:12288\n"
    "v_mfma_f32_16x16x32_bf16 v[56:59], a[92:95], v[16:19], v[56:59]\n"
    "v_mfma_f32_16x16x32_bf16 v[64:67], a[148:151], v[16:19], v[64:67]\n"
    "v_mfma_f32_16x16x32_bf16 v[72:75], a[204:207], v[16:19], v[72:75]\n"
    "s_waitcnt lgkmcnt(5)\n"
    "v_mfma_f32_16x16x32_bf16 v[52:55], a[36:39], v[20:23], v[52:55]\n"
    "ds_read_b128 v[12:15], v1 offset:26624\n"
    "v_mfma_f32_16x16x32_bf16 v[60:63], a[92:95], v[20:23], v[60:63]\n"
    "v_mfma_f32_16x16x32_bf16 v[68:71], a[148:151], v[20:23], v[68:71]\n"
    "v_mfma_f32_16x16x32_bf16 v[76:79], a[204:207], v[20:23], v[76:79]\n"
    "s_waitcnt vmcnt(7)\n"
    "s_barrier\n"
    "buffer_store_dword v2, v3, s[40:43], 0 offen sc1\n"
    "s_waitcnt lgkmcnt(5)\n"
    "v_mfma_f32_16x16x32_bf16 v[48:51], a[40:43], v[24:27], v[48:51]\n"
    "ds_read_b128 v[16:19], v1 offset:13312\n"
    "v_mfma_f32_16x16x32_bf16 v[56:59], a[96:99], v[24:27], v[56:59]\n"
    "v_mfma_f32_16x16x32_bf16 v[64:67], a[152:155], v[24:27], v[64:67]\n"
    "v_mfma_f32_16x16x32_bf16 v[72:75], a[208:211], v[24:27], v[72:75]\n"
    "s_waitcnt lgkmcnt(5)\n"
    "v_mfma_f32_16x16x32_bf16 v[52:55], a[40:43], v[28:31], v[52:55]\n"
    "ds_read_b128 v[20:23], v1 offset:27648\n"
    "v_mfma_f32_16x16x32_bf16 v[60:63], a[96:99], v[28:31], v[60:63]\n"
    "v_mfma_f32_16x16x32_bf16 v[68:71], a[152:155], v[28:31], v[68:71]\n"
    "v_mfma_f32_16x16x32_bf16 v[76:79], a[208:211], v[28:31], v[76:79]\n"
    "s_waitcnt lgkmcnt(5)\n"
    "v_mfma_f32_16x16x32_bf16 v[48:51], a[44:47], v[32:35], v[48:51]\n"
    "v_mfma_f32_16x16x32_bf16 v[56:59], a[100:103], v[32:35], v[56:59]\n"
    "v_mfma_f32_16x16x32_bf16 v[64:67], a[156:159], v[32:35], v[64:67]\n"
    "v_mfma_f32_16x16x32_bf16 v[72:75], a[212:215], v[32:35], v[72:75]\n"
    "s_waitcnt lgkmcnt(4)\n"
    "v_mfma_f32_16x16x32_bf16 v[52:55], a[44:47], v[36:39], v[52:55]\n"
    "v_mfma_f32_16x16x32_bf16 v[60:63], a[100:103], v[36:39], v[60:63]\n"
    "v_mfma_f32_16x16x32_bf16 v[68:71], a[156:159], v[36:39], v[68:71]\n"
    "v_mfma_f32_16x16x32_bf16 v[76:79], a[212:215], v[36:39], v[76:79]\n"
    "s_waitcnt lgkmcnt(3)\n"
    "v_mfma_f32_16x16x32_bf16 v[48:51], a[48:51], v[8:11], v[48:51]\n"
    "v_mfma_f32_16x16x32_bf16 v[56:59], a[104:107], v[8:11], v[56:59]\n"
    "v_mfma_f32_16x16x32_bf16 v[64:67], a[160:163], v[8:11], v[64:67]\n"
    "v_mfma_f32_16x16x32_bf16 v[72:75], a[216:219], v[8:11], v[72:75]\n"
    "s_waitcnt lgkmcnt(2)\n"
    "v_mfma_f32_16x16x32_bf16 v[52:55], a[48:51], v[12:15], v[52:55]\n"
    "v_mfma_f32_16x16x32_bf16 v[60:63], a[104:107], v[12:15], v[60:63]\n"
    "v_mfma_f32_16x16x32_bf16 v[68:71], a[160:163], v[12:15], v[68:71]\n"
    "v_mfma_f32_16x16x32_bf16 v[76:79], a[216:219], v[12:15], v[76:79]\n"
    "s_waitcnt lgkmcnt(1)\n"
    "v_mfma_f32_16x16x32_bf16 v[48:51], a[52:55], v[16:19], v[48:51]\n"
    "v_mfma_f32_16x16x32_bf16 v[56:59], a[108:111], v[16:19], v[56:59]\n"
    "v_mfma_f32_16x16x32_bf16 v[64:67], a[164:167], v[16:19], v[64:67]\n"
    "v_mfma_f32_16x16x32_bf16 v[72:75], a[220:223], v[16:19], v[72:75]\n"
    "s_waitcnt lgkmcnt(0)\n"
    "v_mfma_f32_16x16x32_bf16 v[52:55], a[52:55], v[20:23], v[52:55]\n"
    "v_mfma_f32_16x16x32_bf16 v[60:63], a[108:111], v[20:23], v[60:63]\n"
    "v_mfma_f32_16x16x32_bf16 v[68:71], a[164:167], v[20:23], v[68:71]\n"
    "v_mfma_f32_16x16x32_bf16 v[76:79], a[220:223], v[20:23], v[76:79]\n"
    "v_add_u32 v1, 28672, v1\n"
    "v_cmp_le_u32 vcc, 86016, v1\n"
    "s_add_u32 s44, s44, 1\n"
    "s_nop 0\n"
    "v_cndmask_b32 v1, v1, v0, vcc\n"
    "s_branch LP0_%=_loop\n"
    "LP0_%=_end:\n"
    "s_nop 7\n"
    "v_cvt_pk_bf16_f32 v112, v80, v81\n"
    "v_cvt_pk_bf16_f32 v113, v82, v83\n"
    "v_cvt_pk_bf16_f32 v114, v88, v89\n"
    "v_cvt_pk_bf16_f32 v115, v90, v91\n"
    "v_cvt_pk_bf16_f32 v116, v84, v85\n"
    "v_cvt_pk_bf16_f32 v117, v86, v87\n"
    "v_cvt_pk_bf16_f32 v118, v92, v93\n"
    "v_cvt_pk_bf16_f32 v119, v94, v95\n"
    "v_cvt_pk_bf16_f32 v120, v96, v97\n"
    "v_cvt_pk_bf16_f32 v121, v98, v99\n"
    "v_cvt_pk_bf16_f32 v122, v104, v105\n"
    "v_cvt_pk_bf16_f32 v123, v106, v107\n"
    "v_cvt_pk_bf16_f32 v124, v100, v101\n"
    "v_cvt_pk_bf16_f32 v125, v102, v103\n"
    "v_cvt_pk_bf16_f32 v126, v108, v109\n"
    "v_cvt_pk_bf16_f32 v127, v110, v111\n"
    "s_add_u32 s49, s55, 0\n"
    "buffer_store_dwordx4 v[112:115], v0, s[40:43], s49 offen sc1\n"
    "s_add_u32 s49, s55, 14336\n"
    "buffer_store_dwordx4 v[116:119], v0, s[40:43], s49 offen sc1\n"
    "s_add_u32 s49, s55, 1024\n"
    "buffer_store_dwordx4 v[120:123], v0, s[40:43], s49 offen sc1\n"
    "s_add_u32 s49, s55, 15360\n"
    "buffer_store_dwordx4 v[124:127], v0, s[40:43], s49 offen sc1\n"
    "s_add_u32 s55, s55, 458752\n"
    "v_mov_b32 v2, s44\n"
    "s_waitcnt vmcnt(0)\n"
    "s_barrier\n"
    "buffer_store_dword v2, v3, s[40:43], 0 offen sc1\n"
    "s_endpgm\n"
            :: "s"(wlo), "s"(whi), "s"(p.ws_bytes), "s"(w), "s"(wo), "s"(WS_KX + h * 28672 + pr * 2048), "s"(WS_FLAGS + FL_F1K + (a * 7 + pr) * 128)
            : CHAIN_CLOBBERS);
            __builtin_unreachable();
        } else {
            asm volatile(
    "s_mov_b32 s40, %0\n"
    "s_and_b32 s41, %1, 0xffff\n"
    "s_mov_b32 s42, %2\n"
    "s_mov_b32 s43, 0x00020000\n"
    "v_mbcnt_lo_u32_b32 v10, -1, 0\n"
    "v_mbcnt_hi_u32_b32 v10, -1, v10\n"
    "v_lshlrev_b32 v0, 4, v10\n"
    "s_cmp_eq_u32 %3, 0\n"
    "s_cselect_b32 s49, %6, 0x7fffff00\n"
    "v_mov_b32 v3, 0x7fffff00\n"
    "v_mov_b32 v2, s49\n"
    "v_cmp_eq_u32 vcc, 0, v10\n"
    "s_nop 1\n"
    "v_cndmask_b32 v3, v3, v2, vcc\n"
    "s_mul_i32 s59, %3, 7168\n"
    "s_add_u32 s57, s59, 0xd13000\n"
    "s_mov_b32 s58, s59\n"
    "s_mov_b32 s55, %5\n"
    "s_add_u32 m0, s58, 0\n"
    "s_add_u32 s53, s57, 0\n"
    "buffer_load_dwordx4 v0, s[40:43], s53 offen offset:0 lds\n"
    "buffer_load_dwordx4 v0, s[40:43], s53 offen offset:1024 lds\n"
    "buffer_load_dwordx4 v0, s[40:43], s53 offen offset:2048 lds\n"
    "buffer_load_dwordx4 v0, s[40:43], s53 offen offset:3072 lds\n"
    "s_add_u32 m0, s58, 4096\n"
    "s_add_u32 s53, s57, 4096\n"
    "buffer_load_dwordx4 v0, s[40:43], s53 offen offset:0 lds\n"
    "buffer_load_dwordx4 v0, s[40:43], s53 offen offset:1024 lds\n"
    "buffer_load_dwordx4 v0, s[40:43], s53 offen offset:2048 lds\n"
    "s_add_u32 s57, s57, 28672\n"
    "s_add_u32 s58, s58, 28672\n"
    "s_cmp_ge_u32 s58, 86016\n"
    "s_cselect_b32 s58, s59, s58\n"
    "s_add_u32 m0, s58, 0\n"
    "s_add_u32 s53, s57, 0\n"
    "buffer_load_dwordx4 v0, s[40:43], s53 offen offset:0 lds\n"
    "buffer_load_dwordx4 v0, s[40:43], s53 offen offset:1024 lds\n"
    "buffer_load_dwordx4 v0, s[40:43], s53 offen offset:2048 lds\n"
    "buffer_load_dwordx4 v0, s[40:43], s53 offen offset:3072 lds\n"
    "s_add_u32 m0, s58, 4096\n"
    "s_add_u32 s53, s57, 4096\n"
    "buffer_load_dwordx4 v0, s[40:43], s53 offen offset:0 lds\n"
    "buffer_load_dwordx4 v0, s[40:43], s53 offen offset:1024 lds\n"
    "buffer_load_dwordx4 v0, s[40:43], s53 offen offset:2048 lds\n"
    "s_add_u32 s57, s57, 28672\n"
    "s_add_u32 s58, s58, 28672\n"
    "s_cmp_ge_u32 s58, 86016\n"
    "s_cselect_b32 s58, s59, s58\n"
    "s_mov_b32 s54, %4\n"
    "buffer_load_dwordx4 a[0:3], v0, s[40:43], s54 offen offset:0\n"
    "buffer_load_dwordx4 a[4:7], v0, s[40:43], s54 offen offset:1024\n"
    "buffer_load_dwordx4 a[8:11], v0, s[40:43], s54 offen offset:2048\n"
    "buffer_load_dwordx4 a[12:15], v0, s[40:43], s54 offen offset:3072\n"
    "s_add_u32 s54, s54, 4096\n"
    "buffer_load_dwordx4 a[16:19], v0, s[40:43], s54 offen offset:0\n"
    "buffer_load_dwordx4 a[20:23], v0, s[40:43], s54 offen offset:1024\n"
    "buffer_load_dwordx4 a[24:27], v0, s[40:43], s54 offen offset:2048\n"
    "buffer_load_dwordx4 a[28:31], v0, s[40:43], s54 offen offset:3072\n"
    "s_add_u32 s54, s54, 4096\n"
    "buffer_load_dwordx4 a[32:35], v0, s[40:43], s54 offen offset:0\n"
    "buffer_load_dwordx4 a[36:39], v0, s[40:43], s54 offen offset:1024\n"
    "buffer_load_dwordx4 a[40:43], v0, s[40:43], s54 offen offset:2048\n"
    "buffer_load_dwordx4 a[44:47], v0, s[40:43], s54 offen offset:3072\n"
    "s_add_u32 s54, s54, 4096\n"
    "buffer_load_dwordx4 a[48:51], v0, s[40:43], s54 offen offset:0\n"
    "buffer_load_dwordx4 a[52:55], v0, s[40:43], s54 offen offset:1024\n"
    "buffer_load_dwordx4 a[56:59], v0, s[40:43], s54 offen offset:2048\n"
    "buffer_load_dwordx4 a[60:63], v0, s[40:43], s54 offen offset:3072\n"
    "s_add_u32 s54, s54, 4096\n"
    "buffer_load_dwordx4 a[64:67], v0, s[40:43], s54 offen offset:0\n"
    "buffer_load_dwordx4 a[68:71], v0, s[40:43], s54 offen offset:1024\n"
    "buffer_load_dwordx4 a[72:75], v0, s[40:43], s54 offen offset:2048\n"
    "buffer_load_dwordx4 a[76:79], v0, s[40:43], s54 offen offset:3072\n"
    "s_add_u32 s54, s54, 4096\n"
    "buffer_load_dwordx4 a[80:83], v0, s[40:43], s54 offen offset:0\n"
    "buffer_load_dwordx4 a[84:87], v0, s[40:43], s54 offen offset:1024\n"
    "buffer_load_dwordx4 a[88:91], v0, s[40:43], s54 offen offset:2048\n"
    "buffer_load_dwordx4 a[92:95], v0, s[40:43], s54 offen offset:3072\n"
    "s_add_u32 s54, s54, 4096\n"
    "buffer_load_dwordx4 a[96:99], v0, s[40:43], s54 offen offset:0\n"
    "buffer_load_dwordx4 a[100:103], v0, s[40:43], s54 offen offset:1024\n"
    "buffer_load_dwordx4 a[104:107], v0, s[40:43], s54 offen offset:2048\n"
    "buffer_load_dwordx4 a[108:111], v0, s[40:43], s54 offen offset:3072\n"
    "s_add_u32 s54, s54, 4096\n"
    "buffer_load_dwordx4 a[112:115], v0, s[40:43], s54 offen offset:0\n"
    "buffer_load_dwordx4 a[116:119], v0, s[40:43], s54 offen offset:1024\n"
    "buffer_load_dwordx4 a[120:123], v0, s[40:43], s54 offen offset:2048\n"
    "buffer_load_dwordx4 a[124:127], v0, s[40:43], s54 offen offset:3072\n"
    "s_add_u32 s54, s54, 4096\n"
    "buffer_load_dwordx4 a[128:131], v0, s[40:43], s54 offen offset:0\n"
    "buffer_load_dwordx4 a[132:135], v0, s[40:43], s54 offen offset:1024\n"
    "buffer_load_dwordx4 a[136:139], v0, s[40:43], s54 offen offset:2048\n"
    "buffer_load_dwordx4 a[140:143], v0, s[40:43], s54 offen offset:3072\n"
    "s_add_u32 s54, s54, 4096\n"
    "buffer_load_dwordx4 a[144:147], v0, s[40:43], s54 offen offset:0\n"
    "buffer_load_dwordx4 a[148:151], v0, s[40:43], s54 offen offset:1024\n"
    "buffer_load_dwordx4 a[152:155], v0, s[40:43], s54 offen offset:2048\n"
    "buffer_load_dwordx4 a[156:159], v0, s[40:43], s54 offen offset:3072\n"
    "s_add_u32 s54, s54, 4096\n"
    "buffer_load_dwordx4 a[160:163], v0, s[40:43], s54 offen offset:0\n"
    "buffer_load_dwordx4 a[164:167], v0, s[40:43], s54 offen offset:1024\n"
    "buffer_load_dwordx4 a[168:171], v0, s[40:43], s54 offen offset:2048\n"
    "buffer_load_dwordx4 a[172:175], v0, s[40:43], s54 offen offset:3072\n"
    "s_add_u32 s54, s54, 4096\n"
    "s_waitcnt vmcnt(20)\n"
    "buffer_load_dwordx4 a[176:179], v0, s[40:43], s54 offen offset:0\n"
    "buffer_load_dwordx4 a[180:183], v0, s[40:43], s54 offen offset:1024\n"
    "buffer_load_dwordx4 a[184:187], v0, s[40:43], s54 offen offset:2048\n"
    "buffer_load_dwordx4 a[188:191], v0, s[40:43], s54 offen offset:3072\n"
    "s_add_u32 s54, s54, 4096\n"
    "buffer_load_dwordx4 a[192:195], v0, s[40:43], s54 offen offset:0\n"
    "buffer_load_dwordx4 a[196:199], v0, s[40:43], s54 offen offset:1024\n"
    "buffer_load_dwordx4 a[200:203], v0, s[40:43], s54 offen offset:2048\n"
    "buffer_load_dwordx4 a[204:207], v0, s[40:43], s54 offen offset:3072\n"
    "s_add_u32 s54, s54, 4096\n"
    "buffer_load_dwordx4 a[208:211], v0, s[40:43], s54 offen offset:0\n"
    "buffer_load_dwordx4 a[212:215], v0, s[40:43], s54 offen offset:1024\n"
    "buffer_load_dwordx4 a[216:219], v0, s[40:43], s54 offen offset:2048\n"
    "buffer_load_dwordx4 a[220:223], v0, s[40:43], s54 offen offset:3072\n"
    "s_mov_b32 s44, 0\n"
    "v_mov_b32 v1, v0\n"
    "s_waitcnt vmcnt(0)\n"
    "s_barrier\n"
    "ds_read_b128 v[8:11], v1 offset:0\n"
    "ds_read_b128 v[12:15], v1 offset:14336\n"
    "ds_read_b128 v[16:19], v1 offset:1024\n"
    "ds_read_b128 v[20:23], v1 offset:15360\n"
    "ds_read_b128 v[24:27], v1 offset:2048\n"
    "ds_read_b128 v[28:31], v1 offset:16384\n"
    "s_waitcnt lgkmcnt(5)\n"
    "v_mfma_f32_16x16x32_bf16 v[48:51], v[8:11], a[0:3], 0\n"
    "ds_read_b128 v[32:35], v1 offset:3072\n"
    "v_mfma_f32_16x16x32_bf16 v[56:59], v[8:11], a[56:59], 0\n"
    "s_add_u32 m0, s58, 0\n"
    "s_add_u32 s53, s57, 0\n"
    "buffer_load_dwordx4 v0, s[40:43], s53 offen offset:0 lds\n"
    "v_mfma_f32_16x16x32_bf16 v[64:67], v[8:11], a[112:115], 0\n"
    "v_mfma_f32_16x16x32_bf16 v[72:75], v[8:11], a[168:171], 0\n"
    "buffer_load_dwordx4 v0, s[40:43], s53 offen offset:1024 lds\n"
    "s_waitcnt lgkmcnt(5)\n"
    "v_mfma_f32_16x16x32_bf16 v[52:55], v[12:15], a[0:3], 0\n"
    "ds_read_b128 v[36:39], v1 offset:17408\n"
    "v_mfma_f32_16x16x32_bf16 v[60:63], v[12:15], a[56:59], 0\n"
    "buffer_load_dwordx4 v0, s[40:43], s53 offen offset:2048 lds\n"
    "v_mfma_f32_16x16x32_bf16 v[68:71], v[12:15], a[112:115], 0\n"
    "v_mfma_f32_16x16x32_bf16 v[76:79], v[12:15], a[168:171], 0\n"
    "buffer_load_dwordx4 v0, s[40:43], s53 offen offset:3072 lds\n"
    "s_waitcnt lgkmcnt(5)\n"
    "v_mfma_f32_16x16x32_bf16 v[48:51], v[16:19], a[4:7], v[48:51]\n"
    "ds_read_b128 v[8:11], v1 offset:4096\n"
    "v_mfma_f32_16x16x32_bf16 v[56:59], v[16:19], a[60:63], v[56:59]\n"
    "s_add_u32 m0, s58, 4096\n"
    "s_add_u32 s53, s57, 4096\n"
    "buffer_load_dwordx4 v0, s[40:43], s53 offen offset:0 lds\n"
    "v_mfma_f32_16x16x32_bf16 v[64:67], v[16:19], a[116:119], v[64:67]\n"
    "v_mfma_f32_16x16x32_bf16 v[72:75], v[16:19], a[172:175], v[72:75]\n"
    "buffer_load_dwordx4 v0, s[40:43], s53 offen offset:1024 lds\n"
    "s_waitcnt lgkmcnt(5)\n"
    "v_mfma_f32_16x16x32_bf16 v[52:55], v[20:23], a[4:7], v[52:55]\n"
    "ds_read_b128 v[12:15], v1 offset:18432\n"
    "v_mfma_f32_16x16x32_bf16 v[60:63], v[20:23], a[60:63], v[60:63]\n"
    "buffer_load_dwordx4 v0, s[40:43], s53 offen offset:2048 lds\n"
    "v_mfma_f32_16x16x32_bf16 v[68:71], v[20:23], a[116:119], v[68:71]\n"
    "v_mfma_f32_16x16x32_bf16 v[76:79], v[20:23], a[172:175], v[76:79]\n"
    "s_add_u32 s57, s57, 28672\n"
    "s_add_u32 s58, s58, 28672\n"
    "s_cmp_ge_u32 s58, 86016\n"
    "s_cselect_b32 s58, s59, s58\n"
    "s_waitcnt lgkmcnt(5)\n"
    "v_mfma_f32_16x16x32_bf16 v[48:51], v[24:27], a[8:11], v[48:51]\n"
    "ds_read_b128 v[16:19], v1 offset:5120\n"
    "v_mfma_f32_16x16x32_bf16 v[56:59], v[24:27], a[64:67], v[56:59]\n"
    "v_mfma_f32_16x16x32_bf16 v[64:67], v[24:27], a[120:123], v[64:67]\n"
    "v_mfma_f32_16x16x32_bf16 v[72:75], v[24:27], a[176:179], v[72:75]\n"
    "s_waitcnt lgkmcnt(5)\n"
    "v_mfma_f32_16x16x32_bf16 v[52:55], v[28:31], a[8:11], v[52:55]\n"
    "ds_read_b128 v[20:23], v1 offset:19456\n"
    "v_mfma_f32_16x16x32_bf16 v[60:63], v[28:31], a[64:67], v[60:63]\n"
    "v_mfma_f32_16x16x32_bf16 v[68:71], v[28:31], a[120:123], v[68:71]\n"
    "v_mfma_f32_16x16x32_bf16 v[76:79], v[28:31], a[176:179], v[76:79]\n"
    "s_waitcnt lgkmcnt(5)\n"
    "v_mfma_f32_16x16x32_bf16 v[48:51], v[32:35], a[12:15], v[48:51]\n"
    "ds_read_b128 v[24:27], v1 offset:6144\n"
    "v_mfma_f32_16x16x32_bf16 v[56:59], v[32:35], a[68:71], v[56:59]\n"
    "v_mfma_f32_16x16x32_bf16 v[64:67], v[32:35], a[124:127], v[64:67]\n"
    "v_mfma_f32_16x16x32_bf16 v[72:75], v[32:35], a[180:183], v[72:75]\n"
    "s_waitcnt lgkmcnt(5)\n"
    "v_mfma_f32_16x16x32_bf16 v[52:55], v[36:39], a[12:15], v[52:55]\n"
    "ds_read_b128 v[28:31], v1 offset:20480\n"
    "v_mfma_f32_16x16x32_bf16 v[60:63], v[36:39], a[68:71], v[60:63]\n"
    "v_mfma_f32_16x16x32_bf16 v[68:71], v[36:39], a[124:127], v[68:71]\n"
    "v_mfma_f32_16x16x32_bf16 v[76:79], v[36:39], a[180:183], v[76:79]\n"
    "s_waitcnt lgkmcnt(5)\n"
    "v_mfma_f32_16x16x32_bf16 v[48:51], v[8:11], a[16:19], v[48:51]\n"
    "ds_read_b128 v[32:35], v1 offset:7168\n"
    "v_mfma_f32_16x16x32_bf16 v[56:59], v[8:11], a[72:75], v[56:59]\n"
    "v_mfma_f32_16x16x32_bf16 v[64:67], v[8:11], a[128:131], v[64:67]\n"
    "v_mfma_f32_16x16x32_bf16 v[72:75], v[8:11], a[184:187], v[72:75]\n"
    "s_waitcnt lgkmcnt(5)\n"
    "v_mfma_f32_16x16x32_bf16 v[52:55], v[12:15], a[16:19], v[52:55]\n"
    "ds_read_b128 v[36:39], v1 offset:21504\n"
    "v_mfma_f32_16x16x32_bf16 v[60:63], v[12:15], a[72:75], v[60:63]\n"
    "v_mfma_f32_16x16x32_bf16 v[68:71], v[12:15], a[128:131], v[68:71]\n"
    "v_mfma_f32_16x16x32_bf16 v[76:79], v[12:15], a[184:187], v[76:79]\n"
    "s_waitcnt lgkmcnt(5)\n"
    "v_mfma_f32_16x16x32_bf16 v[48:51], v[16:19], a[20:23], v[48:51]\n"
    "ds_read_b128 v[8:11], v1 offset:8192\n"
    "v_mfma_f32_16x16x32_bf16 v[56:59], v[16:19], a[76:79], v[56:59]\n"
    "v_mfma_f32_16x16x32_bf16 v[64:67], v[16:19], a[132:135], v[64:67]\n"
    "v_mfma_f32_16x16x32_bf16 v[72:75], v[16:19], a[188:191], v[72:75]\n"
    "s_waitcnt lgkmcnt(5)\n"
    "v_mfma_f32_16x16x32_bf16 v[52:55], v[20:23], a[20:23], v[52:55]\n"
    "ds_read_b128 v[12:15], v1 offset:22528\n"
    "v_mfma_f32_16x16x32_bf16 v[60:63], v[20:23], a[76:79], v[60:63]\n"
    "v_mfma_f32_16x16x32_bf16 v[68:71], v[20:23], a[132:135], v[68:71]\n"
    "v_mfma_f32_16x16x32_bf16 v[76:79], v[20:23], a[188:191], v[76:79]\n"
    "s_waitcnt lgkmcnt(5)\n"
    "v_mfma_f32_16x16x32_bf16 v[48:51], v[24:27], a[24:27], v[48:51]\n"
    "ds_read_b128 v[16:19], v1 offset:9216\n"
    "v_mfma_f32_16x16x32_bf16 v[56:59], v[24:27], a[80:83], v[56:59]\n"
    "v_mfma_f32_16x16x32_bf16 v[64:67], v[24:27], a[136:139], v[64:67]\n"
    "v_mfma_f32_16x16x32_bf16 v[72:75], v[24:27], a[192:195], v[72:75]\n"
    "s_waitcnt lgkmcnt(5)\n"
    "v_mfma_f32_16x16x32_bf16 v[52:55], v[28:31], a[24:27], v[52:55]\n"
    "ds_read_b128 v[20:23], v1 offset:23552\n"
    "v_mfma_f32_16x16x32_bf16 v[60:63], v[28:31], a[80:83], v[60:63]\n"
    "v_mfma_f32_16x16x32_bf16 v[68:71], v[28:31], a[136:139], v[68:71]\n"
    "v_mfma_f32_16x16x32_bf16 v[76:79], v[28:31], a[192:195], v[76:79]\n"
    "s_waitcnt lgkmcnt(5)\n"
    "v_mfma_f32_16x16x32_bf16 v[48:51], v[32:35], a[28:31], v[48:51]\n"
    "ds_read_b128 v[24:27], v1 offset:10240\n"
    "v_mfma_f32_16x16x32_bf16 v[56:59], v[32:35], a[84:87], v[56:59]\n"
    "v_mfma_f32_16x16x32_bf16 v[64:67], v[32:35], a[140:143], v[64:67]\n"
    "v_mfma_f32_16x16x32_bf16 v[72:75], v[32:35], a[196:199], v[72:75]\n"
    "s_waitcnt lgkmcnt(5)\n"
    "v_mfma_f32_16x16x32_bf16 v[52:55], v[36:39], a[28:31], v[52:55]\n"
    "ds_read_b128 v[28:31], v1 offset:24576\n"
    "v_mfma_f32_16x16x32_bf16 v[60:63], v[36:39], a[84:87], v[60:63]\n"
    "v_mfma_f32_16x16x32_bf16 v[68:71], v[36:39], a[140:143], v[68:71]\n"
    "v_mfma_f32_16x16x32_bf16 v[76:79], v[36:39], a[196:199], v[76:79]\n"
    "s_waitcnt lgkmcnt(5)\n"
    "v_mfma_f32_16x16x32_bf16 v[48:51], v[8:11], a[32:35], v[48:51]\n"
    "ds_read_b128 v[32:35], v1 offset:11264\n"
    "v_mfma_f32_16x16x32_bf16 v[56:59], v[8:11], a[88:91], v[56:59]\n"
    "v_mfma_f32_16x16x32_bf16 v[64:67], v[8:11], a[144:147], v[64:67]\n"
    "v_mfma_f32_16x16x32_bf16 v[72:75], v[8:11], a[200:203], v[72:75]\n"
    "s_waitcnt lgkmcnt(5)\n"
    "v_mfma_f32_16x16x32_bf16 v[52:55], v[12:15], a[32:35], v[52:55]\n"
    "ds_read_b128 v[36:39], v1 offset:25600\n"
    "v_mfma_f32_16x16x32_bf16 v[60:63], v[12:15], a[88:91], v[60:63]\n"
    "v_mfma_f32_16x16x32_bf16 v[68:71], v[12:15], a[144:147], v[68:71]\n"
    "v_mfma_f32_16x16x32_bf16 v[76:79], v[12:15], a[200:203], v[76:79]\n"
    "s_waitcnt lgkmcnt(5)\n"
    "v_mfma_f32_16x16x32_bf16 v[48:51], v[16:19], a[36:39], v[48:51]\n"
    "ds_read_b128 v[8:11], v1 offset:12288\n"
    "v_mfma_f32_16x16x32_bf16 v[56:59], v[16:19], a[92:95], v[56:59]\n"
    "v_mfma_f32_16x16x32_bf16 v[64:67], v[16:19], a[148:151], v[64:67]\n"
    "v_mfma_f32_16x16x32_bf16 v[72:75], v[16:19], a[204:207], v[72:75]\n"
    "s_waitcnt lgkmcnt(5)\n"
    "v_mfma_f32_16x16x32_bf16 v[52:55], v[20:23], a[36:39], v[52:55]\n"
    "ds_read_b128 v[12:15], v1 offset:26624\n"
    "v_mfma_f32_16x16x32_bf16 v[60:63], v[20:23], a[92:95], v[60:63]\n"
    "v_mfma_f32_16x16x32_bf16 v[68:71], v[20:23], a[148:151], v[68:71]\n"
    "v_mfma_f32_16x16x32_bf16 v[76:79], v[20:23], a[204:207], v[76:79]\n"
    "s_waitcnt vmcnt(7)\n"
    "s_barrier\n"
    "s_waitcnt lgkmcnt(5)\n"
    "v_mfma_f32_16x16x32_bf16 v[48:51], v[24:27], a[40:43], v[48:51]\n"
    "ds_read_b128 v[16:19], v1 offset:13312\n"
    "v_mfma_f32_16x16x32_bf16 v[56:59], v[24:27], a[96:99], v[56:59]\n"
    "v_mfma_f32_16x16x32_bf16 v[64:67], v[24:27], a[152:155], v[64:67]\n"
    "v_mfma_f32_16x16x32_bf16 v[72:75], v[24:27], a[208:211], v[72:75]\n"
    "s_waitcnt lgkmcnt(5)\n"
    "v_mfma_f32_16x16x32_bf16 v[52:55], v[28:31], a[40:43], v[52:55]\n"
    "ds_read_b128 v[20:23], v1 offset:27648\n"
    "v_mfma_f32_16x16x32_bf16 v[60:63], v[28:31], a[96:99], v[60:63]\n"
    "v_mfma_f32_16x16x32_bf16 v[68:71], v[28:31], a[152:155], v[68:71]\n"
    "v_mfma_f32_16x16x32_bf16 v[76:79], v[28:31], a[208:211], v[76:79]\n"
    "s_waitcnt lgkmcnt(5)\n"
    "v_mfma_f32_16x16x32_bf16 v[48:51], v[32:35], a[44:47], v[48:51]\n"
    "v_mfma_f32_16x16x32_bf16 v[56:59], v[32:35], a[100:103], v[56:59]\n"
    "v_mfma_f32_16x16x32_bf16 v[64:67], v[32:35], a[156:159], v[64:67]\n"
    "v_mfma_f32_16x16x32_bf16 v[72:75], v[32:35], a[212:215], v[72:75]\n"
    "s_waitcnt lgkmcnt(4)\n"
    "v_mfma_f32_16x16x32_bf16 v[52:55], v[36:39], a[44:47], v[52:55]\n"
    "v_mfma_f32_16x16x32_bf16 v[60:63], v[36:39], a[100:103], v[60:63]\n"
    "v_mfma_f32_16x16x32_bf16 v[68:71], v[36:39], a[156:159], v[68:71]\n"
    "v_mfma_f32_16x16x32_bf16 v[76:79], v[36:39], a[212:215], v[76:79]\n"
    "s_waitcnt lgkmcnt(3)\n"
    "v_mfma_f32_16x16x32_bf16 v[48:51], v[8:11], a[48:51], v[48:51]\n"
    "v_mfma_f32_16x16x32_bf16 v[56:59], v[8:11], a[104:107], v[56:59]\n"
    "v_mfma_f32_16x16x32_bf16 v[64:67], v[8:11], a[160:163], v[64:67]\n"
    "v_mfma_f32_16x16x32_bf16 v[72:75], v[8:11], a[216:219], v[72:75]\n"
    "s_waitcnt lgkmcnt(2)\n"
    "v_mfma_f32_16x16x32_bf16 v[52:55], v[12:15], a[48:51], v[52:55]\n"
    "v_mfma_f32_16x16x32_bf16 v[60:63], v[12:15], a[104:107], v[60:63]\n"
    "v_mfma_f32_16x16x32_bf16 v[68:71], v[12:15], a[160:163], v[68:71]\n"
    "v_mfma_f32_16x16x32_bf16 v[76:79], v[12:15], a[216:219], v[76:79]\n"
    "s_waitcnt lgkmcnt(1)\n"
    "v_mfma_f32_16x16x32_bf16 v[48:51], v[16:19], a[52:55], v[48:51]\n"
    "v_mfma_f32_16x16x32_bf16 v[56:59], v[16:19], a[108:111], v[56:59]\n"
    "v_mfma_f32_16x16x32_bf16 v[64:67], v[16:19], a[164:167], v[64:67]\n"
    "v_mfma_f32_16x16x32_bf16 v[72:75], v[16:19], a[220:223], v[72:75]\n"
    "s_waitcnt lgkmcnt(0)\n"
    "v_mfma_f32_16x16x32_bf16 v[52:55], v[20:23], a[52:55], v[52:55]\n"
    "v_mfma_f32_16x16x32_bf16 v[60:63], v[20:23], a[108:111], v[60:63]\n"
    "v_mfma_f32_16x16x32_bf16 v[68:71], v[20:23], a[164:167], v[68:71]\n"
    "v_mfma_f32_16x16x32_bf16 v[76:79], v[20:23], a[220:223], v[76:79]\n"
    "v_add_u32 v1, 28672, v1\n"
    "v_cmp_le_u32 vcc, 86016, v1\n"
    "s_add_u32 s44, s44, 1\n"
    "s_nop 0\n"
    "v_cndmask_b32 v1, v1, v0, vcc\n"
    ".p2align 6\n"
    "LP1_%=_loop:\n"
    "v_mov_b32 v2, s44\n"
    "s_barrier\n"
    "ds_read_b128 v[8:11], v1 offset:0\n"
    "ds_read_b128 v[12:15], v1 offset:14336\n"
    "ds_read_b128 v[16:19], v1 offset:1024\n"
    "ds_read_b128 v[20:23], v1 offset:15360\n"
    "ds_read_b128 v[24:27], v1 offset:2048\n"
    "ds_read_b128 v[28:31], v1 offset:16384\n"
    "v_cvt_pk_bf16_f32 v112, v48, v49\n"
    "v_cvt_pk_bf16_f32 v113, v50, v51\n"
    "v_cvt_pk_bf16_f32 v114, v52, v53\n"
    "v_cvt_pk_bf16_f32 v115, v54, v55\n"
    "v_cvt_pk_bf16_f32 v116, v56, v57\n"
    "v_cvt_pk_bf16_f32 v117, v58, v59\n"
    "v_cvt_pk_bf16_f32 v118, v60, v61\n"
    "v_cvt_pk_bf16_f32 v119, v62, v63\n"
    "s_waitcnt lgkmcnt(5)\n"
    "v_mfma_f32_16x16x32_bf16 v[80:83], v[8:11], a[0:3], 0\n"
    "ds_read_b128 v[32:35], v1 offset:3072\n"
    "v_mfma_f32_16x16x32_bf16 v[88:91], v[8:11], a[56:59], 0\n"
    "v_cvt_pk_bf16_f32 v120, v64, v65\n"
    "v_mfma_f32_16x16x32_bf16 v[96:99], v[8:11], a[112:115], 0\n"
    "v_mfma_f32_16x16x32_bf16 v[104:107], v[8:11], a[168:171], 0\n"
    "v_cvt_pk_bf16_f32 v121, v66, v67\n"
    "s_waitcnt lgkmcnt(5)\n"
    "v_mfma_f32_16x16x32_bf16 v[84:87], v[12:15], a[0:3], 0\n"
    "ds_read_b128 v[36:39], v1 offset:17408\n"
    "v_mfma_f32_16x16x32_bf16 v[92:95], v[12:15], a[56:59], 0\n"
    "v_cvt_pk_bf16_f32 v122, v68, v69\n"
    "v_mfma_f32_16x16x32_bf16 v[100:103], v[12:15], a[112:115], 0\n"
    "v_mfma_f32_16x16x32_bf16 v[108:111], v[12:15], a[168:171], 0\n"
    "v_cvt_pk_bf16_f32 v123, v70, v71\n"
    "s_waitcnt lgkmcnt(5)\n"
    "v_mfma_f32_16x16x32_bf16 v[80:83], v[16:19], a[4:7], v[80:83]\n"
    "ds_read_b128 v[8:11], v1 offset:4096\n"
    "v_mfma_f32_16x16x32_bf16 v[88:91], v[16:19], a[60:63], v[88:91]\n"
    "v_cvt_pk_bf16_f32 v124, v72, v73\n"
    "v_mfma_f32_16x16x32_bf16 v[96:99], v[16:19], a[116:119], v[96:99]\n"
    "v_mfma_f32_16x16x32_bf16 v[104:107], v[16:19], a[172:175], v[104:107]\n"
    "v_cvt_pk_bf16_f32 v125, v74, v75\n"
    "s_waitcnt lgkmcnt(5)\n"
    "v_mfma_f32_16x16x32_bf16 v[84:87], v[20:23], a[4:7], v[84:87]\n"
    "ds_read_b128 v[12:15], v1 offset:18432\n"
    "v_mfma_f32_16x16x32_bf16 v[92:95], v[20:23], a[60:63], v[92:95]\n"
    "v_cvt_pk_bf16_f32 v126, v76, v77\n"
    "v_mfma_f32_16x16x32_bf16 v[100:103], v[20:23], a[116:119], v[100:103]\n"
    "v_mfma_f32_16x16x32_bf16 v[108:111], v[20:23], a[172:175], v[108:111]\n"
    "v_cvt_pk_bf16_f32 v127, v78, v79\n"
    "s_waitcnt lgkmcnt(5)\n"
    "v_mfma_f32_16x16x32_bf16 v[80:83], v[24:27], a[8:11], v[80:83]\n"
    "ds_read_b128 v[16:19], v1 offset:5120\n"
    "v_mfma_f32_16x16x32_bf16 v[88:91], v[24:27], a[64:67], v[88:91]\n"
    "s_add_u32 s49, s55, 0\n"
    "buffer_store_dwordx4 v[112:115], v0, s[40:43], s49 offen sc1\n"
    "v_mfma_f32_16x16x32_bf16 v[96:99], v[24:27], a[120:123], v[96:99]\n"
    "v_mfma_f32_16x16x32_bf16 v[104:107], v[24:27], a[176:179], v[104:107]\n"
    "s_add_u32 s49, s55, 16384\n"
    "buffer_store_dwordx4 v[116:119], v0, s[40:43], s49 offen sc1\n"
    "s_waitcnt lgkmcnt(5)\n"
    "v_mfma_f32_16x16x32_bf16 v[84:87], v[28:31], a[8:11], v[84:87]\n"
    "ds_read_b128 v[20:23], v1 offset:19456\n"
    "v_mfma_f32_16x16x32_bf16 v[92:95], v[28:31], a[64:67], v[92:95]\n"
    "s_add_u32 s49, s55, 32768\n"
    "buffer_store_dwordx4 v[120:123], v0, s[40:43], s49 offen sc1\n"
    "v_mfma_f32_16x16x32_bf16 v[100:103], v[28:31], a[120:123], v[100:103]\n"
    "v_mfma_f32_16x16x32_bf16 v[108:111], v[28:31], a[176:179], v[108:111]\n"
    "s_add_u32 s49, s55, 49152\n"
    "buffer_store_dwordx4 v[124:127], v0, s[40:43], s49 offen sc1\n"
    "s_waitcnt lgkmcnt(5)\n"
    "v_mfma_f32_16x16x32_bf16 v[80:83], v[32:35], a[12:15], v[80:83]\n"
    "ds_read_b128 v[24:27], v1 offset:6144\n"
    "v_mfma_f32_16x16x32_bf16 v[88:91], v[32:35], a[68:71], v[88:91]\n"
    "s_add_u32 s55, s55, 458752\n"
    "v_mfma_f32_16x16x32_bf16 v[96:99], v[32:35], a[124:127], v[96:99]\n"
    "v_mfma_f32_16x16x32_bf16 v[104:107], v[32:35], a[180:183], v[104:107]\n"
    "s_add_u32 m0, s58, 0\n"
    "s_add_u32 s53, s57, 0\n"
    "buffer_load_dwordx4 v0, s[40:43], s53 offen offset:0 lds\n"
    "s_waitcnt lgkmcnt(5)\n"
    "v_mfma_f32_16x16x32_bf16 v[84:87], v[36:39], a[12:15], v[84:87]\n"
    "ds_read_b128 v[28:31], v1 offset:20480\n"
    "v_mfma_f32_16x16x32_bf16 v[92:95], v[36:39], a[68:71], v[92:95]\n"
    "buffer_load_dwordx4 v0, s[40:43], s53 offen offset:1024 lds\n"
    "v_mfma_f32_16x16x32_bf16 v[100:103], v[36:39], a[124:127], v[100:103]\n"
    "v_mfma_f32_16x16x32_bf16 v[108:111], v[36:39], a[180:183], v[108:111]\n"
    "buffer_load_dwordx4 v0, s[40:43], s53 offen offset:2048 lds\n"
    "s_waitcnt lgkmcnt(5)\n"
    "v_mfma_f32_16x16x32_bf16 v[80:83], v[8:11], a[16:19], v[80:83]\n"
    "ds_read_b128 v[32:35], v1 offset:7168\n"
    "v_mfma_f32_16x16x32_bf16 v[88:91], v[8:11], a[72:75], v[88:91]\n"
    "buffer_load_dwordx4 v0, s[40:43], s53 offen offset:3072 lds\n"
    "v_mfma_f32_16x16x32_bf16 v[96:99], v[8:11], a[128:131], v[96:99]\n"
    "v_mfma_f32_16x16x32_bf16 v[104:107], v[8:11], a[184:187], v[104:107]\n"
    "s_add_u32 m0, s58, 4096\n"
    "s_add_u32 s53, s57, 4096\n"
    "buffer_load_dwordx4 v0, s[40:43], s53 offen offset:0 lds\n"
    "s_waitcnt lgkmcnt(5)\n"
    "v_mfma_f32_16x16x32_bf16 v[84:87], v[12:15], a[16:19], v[84:87]\n"
    "ds_read_b128 v[36:39], v1 offset:21504\n"
    "v_mfma_f32_16x16x32_bf16 v[92:95], v[12:15], a[72:75], v[92:95]\n"
    "buffer_load_dwordx4 v0, s[40:43], s53 offen offset:1024 lds\n"
    "v_mfma_f32_16x16x32_bf16 v[100:103], v[12:15], a[128:131], v[100:103]\n"
    "v_mfma_f32_16x16x32_bf16 v[108:111], v[12:15], a[184:187], v[108:111]\n"
    "buffer_load_dwordx4 v0, s[40:43], s53 offen offset:2048 lds\n"
    "s_waitcnt lgkmcnt(5)\n"
    "v_mfma_f32_16x16x32_bf16 v[80:83], v[16:19], a[20:23], v[80:83]\n"
    "ds_read_b128 v[8:11], v1 offset:8192\n"
    "v_mfma_f32_16x16x32_bf16 v[88:91], v[16:19], a[76:79], v[88:91]\n"
    "s_add_u32 s57, s57, 28672\n"
    "s_add_u32 s58, s58, 28672\n"
    "s_cmp_ge_u32 s58, 86016\n"
    "s_cselect_b32 s58, s59, s58\n"
    "v_mfma_f32_16x16x32_bf16 v[96:99], v[16:19], a[132:135], v[96:99]\n"
    "v_mfma_f32_16x16x32_bf16 v[104:107], v[16:19], a[188:191], v[104:107]\n"
    "s_waitcnt lgkmcnt(5)\n"
    "v_mfma_f32_16x16x32_bf16 v[84:87], v[20:23], a[20:23], v[84:87]\n"
    "ds_read_b128 v[12:15], v1 offset:22528\n"
    "v_mfma_f32_16x16x32_bf16 v[92:95], v[20:23], a[76:79], v[92:95]\n"
    "v_mfma_f32_16x16x32_bf16 v[100:103], v[20:23], a[132:135], v[100:103]\n"
    "v_mfma_f32_16x16x32_bf16 v[108:111], v[20:23], a[188:191], v[108:111]\n"
    "s_waitcnt lgkmcnt(5)\n"
    "v_mfma_f32_16x16x32_bf16 v[80:83], v[24:27], a[24:27], v[80:83]\n"
    "ds_read_b128 v[16:19], v1 offset:9216\n"
    "v_mfma_f32_16x16x32_bf16 v[88:91], v[24:27], a[80:83], v[88:91]\n"
    "v_mfma_f32_16x16x32_bf16 v[96:99], v[24:27], a[136:139], v[96:99]\n"
    "v_mfma_f32_16x16x32_bf16 v[104:107], v[24:27], a[192:195], v[104:107]\n"
    "s_waitcnt lgkmcnt(5)\n"
    "v_mfma_f32_16x16x32_bf16 v[84:87], v[28:31], a[24:27], v[84:87]\n"
    "ds_read_b128 v[20:23], v1 offset:23552\n"
    "v_mfma_f32_16x16x32_bf16 v[92:95], v[28:31], a[80:83], v[92:95]\n"
    "v_mfma_f32_16x16x32_bf16 v[100:103], v[28:31], a[136:139], v[100:103]\n"
    "v_mfma_f32_16x16x32_bf16 v[108:111], v[28:31], a[192:195], v[108:111]\n"
    "s_waitcnt lgkmcnt(5)\n"
    "v_mfma_f32_16x16x32_bf16 v[80:83], v[32:35], a[28:31], v[80:83]\n"
    "ds_read_b128 v[24:27], v1 offset:10240\n"
    "v_mfma_f32_16x16x32_bf16 v[88:91], v[32:35], a[84:87], v[88:91]\n"
    "v_mfma_f32_16x16x32_bf16 v[96:99], v[32:35], a[140:143], v[96:99]\n"
    "v_mfma_f32_16x16x32_bf16 v[104:107], v[32:35], a[196:199], v[104:107]\n"
    "s_waitcnt lgkmcnt(5)\n"
    "v_mfma_f32_16x16x32_bf16 v[84:87], v[36:39], a[28:31], v[84:87]\n"
    "ds_read_b128 v[28:31], v1 offset:24576\n"
    "v_mfma_f32_16x16x32_bf16 v[92:95], v[36:39], a[84:87], v[92:95]\n"
    "v_mfma_f32_16x16x32_bf16 v[100:103], v[36:39], a[140:143], v[100:103]\n"
    "v_mfma_f32_16x16x32_bf16 v[108:111], v[36:39], a[196:199], v[108:111]\n"
    "s_waitcnt lgkmcnt(5)\n"
    "v_mfma_f32_16x16x32_bf16 v[80:83], v[8:11], a[32:35], v[80:83]\n"
    "ds_read_b128 v[32:35], v1 offset:11264\n"
    "v_mfma_f32_16x16x32_bf16 v[88:91], v[8:11], a[88:91], v[88:91]\n"
    "v_mfma_f32_16x16x32_bf16 v[96:99], v[8:11], a[144:147], v[96:99]\n"
    "v_mfma_f32_16x16x32_bf16 v[104:107], v[8:11], a[200:203], v[104:107]\n"
    "s_waitcnt lgkmcnt(5)\n"
    "v_mfma_f32_16x16x32_bf16 v[84:87], v[12:15], a[32:35], v[84:87]\n"
    "ds_read_b128 v[36:39], v1 offset:25600\n"
    "v_mfma_f32_16x16x32_bf16 v[92:95], v[12:15], a[88:91], v[92:95]\n"
    "v_mfma_f32_16x16x32_bf16 v[100:103], v[12:15], a[144:147], v[100:103]\n"
    "v_mfma_f32_16x16x32_bf16 v[108:111], v[12:15], a[200:203], v[108:111]\n"
    "s_waitcnt lgkmcnt(5)\n"
    "v_mfma_f32_16x16x32_bf16 v[80:83], v[16:19], a[36:39], v[80:83]\n"
    "ds_read_b128 v[8:11], v1 offset:12288\n"
    "v_mfma_f32_16x16x32_bf16 v[88:91], v[16:19], a[92:95], v[88:91]\n"
    "v_mfma_f32_16x16x32_bf16 v[96:99], v[16:19], a[148:151], v[96:99]\n"
    "v_mfma_f32_16x16x32_bf16 v[104:107], v[16:19], a[204:207], v[104:107]\n"
    "s_waitcnt lgkmcnt(5)\n"
    "v_mfma_f32_16x16x32_bf16 v[84:87], v[20:23], a[36:39], v[84:87]\n"
    "ds_read_b128 v[12:15], v1 offset:26624\n"
    "v_mfma_f32_16x16x32_bf16 v[92:95], v[20:23], a[92:95], v[92:95]\n"
    "v_mfma_f32_16x16x32_bf16 v[100:103], v[20:23], a[148:151], v[100:103]\n"
    "v_mfma_f32_16x16x32_bf16 v[108:111], v[20:23], a[204:207], v[108:111]\n"
    "s_waitcnt vmcnt(7)\n"
    "s_barrier\n"
    "buffer_store_dword v2, v3, s[40:43], 0 offen sc1\n"
    "s_waitcnt lgkmcnt(5)\n"
    "v_mfma_f32_16x16x32_bf16 v[80:83], v[24:27], a[40:43], v[80:83]\n"
    "ds_read_b128 v[16:19], v1 offset:13312\n"
    "v_mfma_f32_16x16x32_bf16 v[88:91], v[24:27], a[96:99], v[88:91]\n"
    "v_mfma_f32_16x16x32_bf16 v[96:99], v[24:27], a[152:155], v[96:99]\n"
    "v_mfma_f32_16x16x32_bf16 v[104:107], v[24:27], a[208:211], v[104:107]\n"
    "s_waitcnt lgkmcnt(5)\n"
    "v_mfma_f32_16x16x32_bf16 v[84:87], v[28:31], a[40:43], v[84:87]\n"
    "ds_read_b128 v[20:23], v1 offset:27648\n"
    "v_mfma_f32_16x16x32_bf16 v[92:95], v[28:31], a[96:99], v[92:95]\n"
    "v_mfma_f32_16x16x32_bf16 v[100:103], v[28:31], a[152:155], v[100:103]\n"
    "v_mfma_f32_16x16x32_bf16 v[108:111], v[28:31], a[208:211], v[108:111]\n"
    "s_waitcnt lgkmcnt(5)\n"
    "v_mfma_f32_16x16x32_bf16 v[80:83], v[32:35], a[44:47], v[80:83]\n"
    "v_mfma_f32_16x16x32_bf16 v[88:91], v[32:35], a[100:103], v[88:91]\n"
    "v_mfma_f32_16x16x32_bf16 v[96:99], v[32:35], a[156:159], v[96:99]\n"
    "v_mfma_f32_16x16x32_bf16 v[104:107], v[32:35], a[212:215], v[104:107]\n"
    "s_waitcnt lgkmcnt(4)\n"
    "v_mfma_f32_16x16x32_bf16 v[84:87], v[36:39], a[44:47], v[84:87]\n"
    "v_mfma_f32_16x16x32_bf16 v[92:95], v[36:39], a[100:103], v[92:95]\n"
    "v_mfma_f32_16x16x32_bf16 v[100:103], v[36:39], a[156:159], v[100:103]\n"
    "v_mfma_f32_16x16x32_bf16 v[108:111], v[36:39], a[212:215], v[108:111]\n"
    "s_waitcnt lgkmcnt(3)\n"
    "v_mfma_f32_16x16x32_bf16 v[80:83], v[8:11], a[48:51], v[80:83]\n"
    "v_mfma_f32_16x16x32_bf16 v[88:91], v[8:11], a[104:107], v[88:91]\n"
    "v_mfma_f32_16x16x32_bf16 v[96:99], v[8:11], a[160:163], v[96:99]\n"
    "v_mfma_f32_16x16x32_bf16 v[104:107], v[8:11], a[216:219], v[104:107]\n"
    "s_waitcnt lgkmcnt(2)\n"
    "v_mfma_f32_16x16x32_bf16 v[84:87], v[12:15], a[48:51], v[84:87]\n"
    "v_mfma_f32_16x16x32_bf16 v[92:95], v[12:15], a[104:107], v[92:95]\n"
    "v_mfma_f32_16x16x32_bf16 v[100:103], v[12:15], a[160:163], v[100:103]\n"
    "v_mfma_f32_16x16x32_bf16 v[108:111], v[12:15], a[216:219], v[108:111]\n"
    "s_waitcnt lgkmcnt(1)\n"
    "v_mfma_f32_16x16x32_bf16 v[80:83], v[16:19], a[52:55], v[80:83]\n"
    "v_mfma_f32_16x16x32_bf16 v[88:91], v[16:19], a[108:111], v[88:91]\n"
    "v_mfma_f32_16x16x32_bf16 v[96:99], v[16:19], a[164:167], v[96:99]\n"
    "v_mfma_f32_16x16x32_bf16 v[104:107], v[16:19], a[220:223], v[104:107]\n"
    "s_waitcnt lgkmcnt(0)\n"
    "v_mfma_f32_16x16x32_bf16 v[84:87], v[20:23], a[52:55], v[84:87]\n"
    "v_mfma_f32_16x16x32_bf16 v[92:95], v[20:23], a[108:111], v[92:95]\n"
    "v_mfma_f32_16x16x32_bf16 v[100:103], v[20:23], a[164:167], v[100:103]\n"
    "v_mfma_f32_16x16x32_bf16 v[108:111], v[20:23], a[220:223], v[108:111]\n"
    "v_add_u32 v1, 28672, v1\n"
    "v_cmp_le_u32 vcc, 86016, v1\n"
    "s_add_u32 s44, s44, 1\n"
    "s_nop 0\n"
    "v_cndmask_b32 v1, v1, v0, vcc\n"
    "s_cmp_eq_u32 s44, 64\n"
    "s_cbranch_scc1 LP1_%=_end\n"
    "v_mov_b32 v2, s44\n"
    "s_barrier\n"
    "ds_read_b128 v[8:11], v1 offset:0\n"
    "ds_read_b128 v[12:15], v1 offset:14336\n"
    "ds_read_b128 v[16:19], v1 offset:1024\n"
    "ds_read_b128 v[20:23], v1 offset:15360\n"
    "ds_read_b128 v[24:27], v1 offset:2048\n"
    "ds_read_b128 v[28:31], v1 offset:16384\n"
    "v_cvt_pk_bf16_f32 v112, v80, v81\n"
    "v_cvt_pk_bf16_f32 v113, v82, v83\n"
    "v_cvt_pk_bf16_f32 v114, v84, v85\n"
    "v_cvt_pk_bf16_f32 v115, v86, v87\n"
    "v_cvt_pk_bf16_f32 v116, v88, v89\n"
    "v_cvt_pk_bf16_f32 v117, v90, v91\n"
    "v_cvt_pk_bf16_f32 v118, v92, v93\n"
    "v_cvt_pk_bf16_f32 v119, v94, v95\n"
    "s_waitcnt lgkmcnt(5)\n"
    "v_mfma_f32_16x16x32_bf16 v[48:51], v[8:11], a[0:3], 0\n"
    "ds_read_b128 v[32:35], v1 offset:3072\n"
    "v_mfma_f32_16x16x32_bf16 v[56:59], v[8:11], a[56:59], 0\n"
    "v_cvt_pk_bf16_f32 v120, v96, v97\n"
    "v_mfma_f32_16x16x32_bf16 v[64:67], v[8:11], a[112:115], 0\n"
    "v_mfma_f32_16x16x32_bf16 v[72:75], v[8:11], a[168:171], 0\n"
    "v_cvt_pk_bf16_f32 v121, v98, v99\n"
    "s_waitcnt lgkmcnt(5)\n"
    "v_mfma_f32_16x16x32_bf16 v[52:55], v[12:15], a[0:3], 0\n"
    "ds_read_b128 v[36:39], v1 offset:17408\n"
    "v_mfma_f32_16x16x32_bf16 v[60:63], v[12:15], a[56:59], 0\n"
    "v_cvt_pk_bf16_f32 v122, v100, v101\n"
    "v_mfma_f32_16x16x32_bf16 v[68:71], v[12:15], a[112:115], 0\n"
    "v_mfma_f32_16x16x32_bf16 v[76:79], v[12:15], a[168:171], 0\n"
    "v_cvt_pk_bf16_f32 v123, v102, v103\n"
    "s_waitcnt lgkmcnt(5)\n"
    "v_mfma_f32_16x16x32_bf16 v[48:51], v[16:19], a[4:7], v[48:51]\n"
    "ds_read_b128 v[8:11], v1 offset:4096\n"
    "v_mfma_f32_16x16x32_bf16 v[56:59], v[16:19], a[60:63], v[56:59]\n"
    "v_cvt_pk_bf16_f32 v124, v104, v105\n"
    "v_mfma_f32_16x16x32_bf16 v[64:67], v[16:19], a[116:119], v[64:67]\n"
    "v_mfma_f32_16x16x32_bf16 v[72:75], v[16:19], a[172:175], v[72:75]\n"
    "v_cvt_pk_bf16_f32 v125, v106, v107\n"
    "s_waitcnt lgkmcnt(5)\n"
    "v_mfma_f32_16x16x32_bf16 v[52:55], v[20:23], a[4:7], v[52:55]\n"
    "ds_read_b128 v[12:15], v1 offset:18432\n"
    "v_mfma_f32_16x16x32_bf16 v[60:63], v[20:23], a[60:63], v[60:63]\n"
    "v_cvt_pk_bf16_f32 v126, v108, v109\n"
    "v_mfma_f32_16x16x32_bf16 v[68:71], v[20:23], a[116:119], v[68:71]\n"
    "v_mfma_f32_16x16x32_bf16 v[76:79], v[20:23], a[172:175], v[76:79]\n"
    "v_cvt_pk_bf16_f32 v127, v110, v111\n"
    "s_waitcnt lgkmcnt(5)\n"
    "v_mfma_f32_16x16x32_bf16 v[48:51], v[24:27], a[8:11], v[48:51]\n"
    "ds_read_b128 v[16:19], v1 offset:5120\n"
    "v_mfma_f32_16x16x32_bf16 v[56:59], v[24:27], a[64:67], v[56:59]\n"
    "s_add_u32 s49, s55, 0\n"
    "buffer_store_dwordx4 v[112:115], v0, s[40:43], s49 offen sc1\n"
    "v_mfma_f32_16x16x32_bf16 v[64:67], v[24:27], a[120:123], v[64:67]\n"
    "v_mfma_f32_16x16x32_bf16 v[72:75], v[24:27], a[176:179], v[72:75]\n"
    "s_add_u32 s49, s55, 16384\n"
    "buffer_store_dwordx4 v[116:119], v0, s[40:43], s49 offen sc1\n"
    "s_waitcnt lgkmcnt(5)\n"
    "v_mfma_f32_16x16x32_bf16 v[52:55], v[28:31], a[8:11], v[52:55]\n"
    "ds_read_b128 v[20:23], v1 offset:19456\n"
    "v_mfma_f32_16x16x32_bf16 v[60:63], v[28:31], a[64:67], v[60:63]\n"
    "s_add_u32 s49, s55, 32768\n"
    "buffer_store_dwordx4 v[120:123], v0, s[40:43], s49 offen sc1\n"
    "v_mfma_f32_16x16x32_bf16 v[68:71], v[28:31], a[120:123], v[68:71]\n"
    "v_mfma_f32_16x16x32_bf16 v[76:79], v[28:31], a[176:179], v[76:79]\n"
    "s_add_u32 s49, s55, 49152\n"
    "buffer_store_dwordx4 v[124:127], v0, s[40:43], s49 offen sc1\n"
    "s_waitcnt lgkmcnt(5)\n"
    "v_mfma_f32_16x16x32_bf16 v[48:51], v[32:35], a[12:15], v[48:51]\n"
    "ds_read_b128 v[24:27], v1 offset:6144\n"
    "v_mfma_f32_16x16x32_bf16 v[56:59], v[32:35], a[68:71], v[56:59]\n"
    "s_add_u32 s55, s55, 458752\n"
    "v_mfma_f32_16x16x32_bf16 v[64:67], v[32:35], a[124:127], v[64:67]\n"
    "v_mfma_f32_16x16x32_bf16 v[72:75], v[32:35], a[180:183], v[72:75]\n"
    "s_add_u32 m0, s58, 0\n"
    "s_add_u32 s53, s57, 0\n"
    "buffer_load_dwordx4 v0, s[40:43], s53 offen offset:0 lds\n"
    "s_waitcnt lgkmcnt(5)\n"
    "v_mfma_f32_16x16x32_bf16 v[52:55], v[36:39], a[12:15], v[52:55]\n"
    "ds_read_b128 v[28:31], v1 offset:20480\n"
    "v_mfma_f32_16x16x32_bf16 v[60:63], v[36:39], a[68:71], v[60:63]\n"
    "buffer_load_dwordx4 v0, s[40:43], s53 offen offset:1024 lds\n"
    "v_mfma_f32_16x16x32_bf16 v[68:71], v[36:39], a[124:127], v[68:71]\n"
    "v_mfma_f32_16x16x32_bf16 v[76:79], v[36:39], a[180:183], v[76:79]\n"
    "buffer_load_dwordx4 v0, s[40:43], s53 offen offset:2048 lds\n"
    "s_waitcnt lgkmcnt(5)\n"
    "v_mfma_f32_16x16x32_bf16 v[48:51], v[8:11], a[16:19], v[48:51]\n"
    "ds_read_b128 v[32:35], v1 offset:7168\n"
    "v_mfma_f32_16x16x32_bf16 v[56:59], v[8:11], a[72:75], v[56:59]\n"
    "buffer_load_dwordx4 v0, s[40:43], s53 offen offset:3072 lds\n"
    "v_mfma_f32_16x16x32_bf16 v[64:67], v[8:11], a[128:131], v[64:67]\n"
    "v_mfma_f32_16x16x32_bf16 v[72:75], v[8:11], a[184:187], v[72:75]\n"
    "s_add_u32 m0, s58, 4096\n"
    "s_add_u32 s53, s57, 4096\n"
    "buffer_load_dwordx4 v0, s[40:43], s53 offen offset:0 lds\n"
    "s_waitcnt lgkmcnt(5)\n"
    "v_mfma_f32_16x16x32_bf16 v[52:55], v[12:15], a[16:19], v[52:55]\n"
    "ds_read_b128 v[36:39], v1 offset:21504\n"
    "v_mfma_f32_16x16x32_bf16 v[60:63], v[12:15], a[72:75], v[60:63]\n"
    "buffer_load_dwordx4 v0, s[40:43], s53 offen offset:1024 lds\n"
    "v_mfma_f32_16x16x32_bf16 v[68:71], v[12:15], a[128:131], v[68:71]\n"
    "v_mfma_f32_16x16x32_bf16 v[76:79], v[12:15], a[184:187], v[76:79]\n"
    "buffer_load_dwordx4 v0, s[40:43], s53 offen offset:2048 lds\n"
    "s_waitcnt lgkmcnt(5)\n"
    "v_mfma_f32_16x16x32_bf16 v[48:51], v[16:19], a[20:23], v[48:51]\n"
    "ds_read_b128 v[8:11], v1 offset:8192\n"
    "v_mfma_f32_16x16x32_bf16 v[56:59], v[16:19], a[76:79], v[56:59]\n"
    "s_add_u32 s57, s57, 28672\n"
    "s_add_u32 s58, s58, 28672\n"
    "s_cmp_ge_u32 s58, 86016\n"
    "s_cselect_b32 s58, s59, s58\n"
    "v_mfma_f32_16x16x32_bf16 v[64:67], v[16:19], a[132:135], v[64:67]\n"
    "v_mfma_f32_16x16x32_bf16 v[72:75], v[16:19], a[188:191], v[72:75]\n"
    "s_waitcnt lgkmcnt(5)\n"
    "v_mfma_f32_16x16x32_bf16 v[52:55], v[20:23], a[20:23], v[52:55]\n"
    "ds_read_b128 v[12:15], v1 offset:22528\n"
    "v_mfma_f32_16x16x32_bf16 v[60:63], v[20:23], a[76:79], v[60:63]\n"
    "v_mfma_f32_16x16x32_bf16 v[68:71], v[20:23], a[132:135], v[68:71]\n"
    "v_mfma_f32_16x16x32_bf16 v[76:79], v[20:23], a[188:191], v[76:79]\n"
    "s_waitcnt lgkmcnt(5)\n"
    "v_mfma_f32_16x16x32_bf16 v[48:51], v[24:27], a[24:27], v[48:51]\n"
    "ds_read_b128 v[16:19], v1 offset:9216\n"
    "v_mfma_f32_16x16x32_bf16 v[56:59], v[24:27], a[80:83], v[56:59]\n"
    "v_mfma_f32_16x16x32_bf16 v[64:67], v[24:27], a[136:139], v[64:67]\n"
    "v_mfma_f32_16x16x32_bf16 v[72:75], v[24:27], a[192:195], v[72:75]\n"
    "s_waitcnt lgkmcnt(5)\n"
    "v_mfma_f32_16x16x32_bf16 v[52:55], v[28:31], a[24:27], v[52:55]\n"
    "ds_read_b128 v[20:23], v1 offset:23552\n"
    "v_mfma_f32_16x16x32_bf16 v[60:63], v[28:31], a[80:83], v[60:63]\n"
    "v_mfma_f32_16x16x32_bf16 v[68:71], v[28:31], a[136:139], v[68:71]\n"
    "v_mfma_f32_16x16x32_bf16 v[76:79], v[28:31], a[192:195], v[76:79]\n"
    "s_waitcnt lgkmcnt(5)\n"
    "v_mfma_f32_16x16x32_bf16 v[48:51], v[32:35], a[28:31], v[48:51]\n"
    "ds_read_b128 v[24:27], v1 offset:10240\n"
    "v_mfma_f32_16x16x32_bf16 v[56:59], v[32:35], a[84:87], v[56:59]\n"
    "v_mfma_f32_16x16x32_bf16 v[64:67], v[32:35], a[140:143], v[64:67]\n"
    "v_mfma_f32_16x16x32_bf16 v[72:75], v[32:35], a[196:199], v[72:75]\n"
    "s_waitcnt lgkmcnt(5)\n"
    "v_mfma_f32_16x16x32_bf16 v[52:55], v[36:39], a[28:31], v[52:55]\n"
    "ds_read_b128 v[28:31], v1 offset:24576\n"
    "v_mfma_f32_16x16x32_bf16 v[60:63], v[36:39], a[84:87], v[60:63]\n"
    "v_mfma_f32_16x16x32_bf16 v[68:71], v[36:39], a[140:143], v[68:71]\n"
    "v_mfma_f32_16x16x32_bf16 v[76:79], v[36:39], a[196:199], v[76:79]\n"
    "s_waitcnt lgkmcnt(5)\n"
    "v_mfma_f32_16x16x32_bf16 v[48:51], v[8:11], a[32:35], v[48:51]\n"
    "ds_read_b128 v[32:35], v1 offset:11264\n"
    "v_mfma_f32_16x16x32_bf16 v[56:59], v[8:11], a[88:91], v[56:59]\n"
    "v_mfma_f32_16x16x32_bf16 v[64:67], v[8:11], a[144:147], v[64:67]\n"
    "v_mfma_f32_16x16x32_bf16 v[72:75], v[8:11], a[200:203], v[72:75]\n"
    "s_waitcnt lgkmcnt(5)\n"
    "v_mfma_f32_16x16x32_bf16 v[52:55], v[12:15], a[32:35], v[52:55]\n"
    "ds_read_b128 v[36:39], v1 offset:25600\n"
    "v_mfma_f32_16x16x32_bf16 v[60:63], v[12:15], a[88:91], v[60:63]\n"
    "v_mfma_f32_16x16x32_bf16 v[68:71], v[12:15], a[144:147], v[68:71]\n"
    "v_mfma_f32_16x16x32_bf16 v[76:79], v[12:15], a[200:203], v[76:79]\n"
    "s_waitcnt lgkmcnt(5)\n"
    "v_mfma_f32_16x16x32_bf16 v[48:51], v[16:19], a[36:39], v[48:51]\n"
    "ds_read_b128 v[8:11], v1 offset:12288\n"
    "v_mfma_f32_16x16x32_bf16 v[56:59], v[16:19], a[92:95], v[56:59]\n"
    "v_mfma_f32_16x16x32_bf16 v[64:67], v[16:19], a[148:151], v[64:67]\n"
    "v_mfma_f32_16x16x32_bf16 v[72:75], v[16:19], a[204:207], v[72:75]\n"
    "s_waitcnt lgkmcnt(5)\n"
    "v_mfma_f32_16x16x32_bf16 v[52:55], v[20:23], a[36:39], v[52:55]\n"
    "ds_read_b128 v[12:15], v1 offset:26624\n"
    "v_mfma_f32_16x16x32_bf16 v[60:63], v[20:23], a[92:95], v[60:63]\n"
    "v_mfma_f32_16x16x32_bf16 v[68:71], v[20:23], a[148:151], v[68:71]\n"
    "v_mfma_f32_16x16x32_bf16 v[76:79], v[20:23], a[204:207], v[76:79]\n"
    "s_waitcnt vmcnt(7)\n"
    "s_barrier\n"
    "buffer_store_dword v2, v3, s[40:43], 0 offen sc1\n"
    "s_waitcnt lgkmcnt(5)\n"
    "v_mfma_f32_16x16x32_bf16 v[48:51], v[24:27], a[40:43], v[48:51]\n"
    "ds_read_b128 v[16:19], v1 offset:13312\n"
    "v_mfma_f32_16x16x32_bf16 v[56:59], v[24:27], a[96:99], v[56:59]\n"
    "v_mfma_f32_16x16x32_bf16 v[64:67], v[24:27], a[152:155], v[64:67]\n"
    "v_mfma_f32_16x16x32_bf16 v[72:75], v[24:27], a[208:211], v[72:75]\n"
    "s_waitcnt lgkmcnt(5)\n"
    "v_mfma_f32_16x16x32_bf16 v[52:55], v[28:31], a[40:43], v[52:55]\n"
    "ds_read_b128 v[20:23], v1 offset:27648\n"
    "v_mfma_f32_16x16x32_bf16 v[60:63], v[28:31], a[96:99], v[60:63]\n"
    "v_mfma_f32_16x16x32_bf16 v[68:71], v[28:31], a[152:155], v[68:71]\n"
    "v_mfma_f32_16x16x32_bf16 v[76:79], v[28:31], a[208:211], v[76:79]\n"
    "s_waitcnt lgkmcnt(5)\n"
    "v_mfma_f32_16x16x32_bf16 v[48:51], v[32:35], a[44:47], v[48:51]\n"
    "v_mfma_f32_16x16x32_bf16 v[56:59], v[32:35], a[100:103], v[56:59]\n"
    "v_mfma_f32_16x16x32_bf16 v[64:67], v[32:35], a[156:159], v[64:67]\n"
    "v_mfma_f32_16x16x32_bf16 v[72:75], v[32:35], a[212:215], v[72:75]\n"
    "s_waitcnt lgkmcnt(4)\n"
    "v_mfma_f32_16x16x32_bf16 v[52:55], v[36:39], a[44:47], v[52:55]\n"
    "v_mfma_f32_16x16x32_bf16 v[60:63], v[36:39], a[100:103], v[60:63]\n"
    "v_mfma_f32_16x16x32_bf16 v[68:71], v[36:39], a[156:159], v[68:71]\n"
    "v_mfma_f32_16x16x32_bf16 v[76:79], v[36:39], a[212:215], v[76:79]\n"
    "s_waitcnt lgkmcnt(3)\n"
    "v_mfma_f32_16x16x32_bf16 v[48:51], v[8:11], a[48:51], v[48:51]\n"
    "v_mfma_f32_16x16x32_bf16 v[56:59], v[8:11], a[104:107], v[56:59]\n"
    "v_mfma_f32_16x16x32_bf16 v[64:67], v[8:11], a[160:163], v[64:67]\n"
    "v_mfma_f32_16x16x32_bf16 v[72:75], v[8:11], a[216:219], v[72:75]\n"
    "s_waitcnt lgkmcnt(2)\n"
    "v_mfma_f32_16x16x32_bf16 v[52:55], v[12:15], a[48:51], v[52:55]\n"
    "v_mfma_f32_16x16x32_bf16 v[60:63], v[12:15], a[104:107], v[60:63]\n"
    "v_mfma_f32_16x16x32_bf16 v[68:71], v[12:15], a[160:163], v[68:71]\n"
    "v_mfma_f32_16x16x32_bf16 v[76:79], v[12:15], a[216:219], v[76:79]\n"
    "s_waitcnt lgkmcnt(1)\n"
    "v_mfma_f32_16x16x32_bf16 v[48:51], v[16:19], a[52:55], v[48:51]\n"
    "v_mfma_f32_16x16x32_bf16 v[56:59], v[16:19], a[108:111], v[56:59]\n"
    "v_mfma_f32_16x16x32_bf16 v[64:67], v[16:19], a[164:167], v[64:67]\n"
    "v_mfma_f32_16x16x32_bf16 v[72:75], v[16:19], a[220:223], v[72:75]\n"
    "s_waitcnt lgkmcnt(0)\n"
    "v_mfma_f32_16x16x32_bf16 v[52:55], v[20:23], a[52:55], v[52:55]\n"
    "v_mfma_f32_16x16x32_bf16 v[60:63], v[20:23], a[108:111], v[60:63]\n"
    "v_mfma_f32_16x16x32_bf16 v[68:71], v[20:23], a[164:167], v[68:71]\n"
    "v_mfma_f32_16x16x32_bf16 v[76:79], v[20:23], a[220:223], v[76:79]\n"
    "v_add_u32 v1, 28672, v1\n"
    "v_cmp_le_u32 vcc, 86016, v1\n"
    "s_add_u32 s44, s44, 1\n"
    "s_nop 0\n"
    "v_cndmask_b32 v1, v1, v0, vcc\n"
    "s_branch LP1_%=_loop\n"
    "LP1_%=_end:\n"
    "s_nop 7\n"
    "v_cvt_pk_bf16_f32 v112, v80, v81\n"
    "v_cvt_pk_bf16_f32 v113, v82, v83\n"
    "v_cvt_pk_bf16_f32 v114, v84, v85\n"
    "v_cvt_pk_bf16_f32 v115, v86, v87\n"
    "v_cvt_pk_bf16_f32 v116, v88, v89\n"
    "v_cvt_pk_bf16_f32 v117, v90, v91\n"
    "v_cvt_pk_bf16_f32 v118, v92, v93\n"
    "v_cvt_pk_bf16_f32 v119, v94, v95\n"
    "v_cvt_pk_bf16_f32 v120, v96, v97\n"
    "v_cvt_pk_bf16_f32 v121, v98, v99\n"
    "v_cvt_pk_bf16_f32 v122, v100, v101\n"
    "v_cvt_pk_bf16_f32 v123, v102, v103\n"
    "v_cvt_pk_bf16_f32 v124, v104, v105\n"
    "v_cvt_pk_bf16_f32 v125, v106, v107\n"
    "v_cvt_pk_bf16_f32 v126, v108, v109\n"
    "v_cvt_pk_bf16_f32 v127, v110, v111\n"
    "s_add_u32 s49, s55, 0\n"
    "buffer_store_dwordx4 v[112:115], v0, s[40:43], s49 offen sc1\n"
    "s_add_u32 s49, s55, 16384\n"
    "buffer_store_dwordx4 v[116:119], v0, s[40:43], s49 offen sc1\n"
    "s_add_u32 s49, s55, 32768\n"
    "buffer_store_dwordx4 v[120:123], v0, s[40:43], s49 offen sc1\n"
    "s_add_u32 s49, s55, 49152\n"
    "buffer_store_dwordx4 v[124:127], v0, s[40:43], s49 offen sc1\n"
    "s_add_u32 s55, s55, 458752\n"
    "v_mov_b32 v2, s44\n"
    "s_waitcnt vmcnt(0)\n"
    "s_barrier\n"
    "buffer_store_dword v2, v3, s[40:43], 0 offen sc1\n"
    "s_endpgm\n"
            :: "s"(wlo), "s"(whi), "s"(p.ws_bytes), "s"(w), "s"(wo), "s"(WS_VX + pr * 65536 + h * 1024), "s"(WS_FLAGS + FL_F1V + (pr * 4 + a) * 128)
            : CHAIN_CLOBBERS);
            __builtin_unreachable();
        }
    }
    else if (role == 2) role_scores(args(), id, lane_id(), w);
    else if (role == 3) role_values(args(), id, lane_id(), w);
    else if (role == 4) role_up(args(), sm, id, lane_id(), w);
    else role_down(args(), sm, id, lane_id(), w);
}

#define WAIT_S0 7
#define WAIT_S1 0
#define WAIT_M 220
#define WAIT_V 2

extern "C" void kernel_launch(void* const* d_in, const int* in_sizes, int n_in, void* d_out, int out_size, void* d_ws, size_t ws_size,
                              hipStream_t stream) {
    Params p;
    p.x = (const unsigned short*)d_in[0]; p.mem0 = (const unsigned short*)d_in[1];
    p.wq = (const unsigned short*)d_in[2]; p.wk = (const unsigned short*)d_in[3]; p.wv = (const unsigned short*)d_in[4]; p.wo = (const unsigned short*)d_in[5];
    p.w1 = (const unsigned short*)d_in[8]; p.b1 = (const unsigned short*)d_in[9]; p.w2 = (const unsigned short*)d_in[10]; p.b2 = (const unsigned short*)d_in[11];
    p.out = (unsigned short*)d_out; p.ws = (unsigned char*)d_ws;
    p.ws_bytes = WS_END; p.ws0 = WAIT_S0; p.ws1 = WAIT_S1; p.wm = WAIT_M; p.wv_t = WAIT_V; p.pad = 0;
    (void)hipFuncSetAttribute((const void*)main_kernel, hipFuncAttributeMaxDynamicSharedMemorySize, LDS_BYTES);
    (void)hipFuncSetAttribute((const void*)prep_kernel, hipFuncAttributeMaxDynamicSharedMemorySize, 100352);
    prep_kernel<<<256, 256, 100352, stream>>>(p);
    main_kernel<<<256, 256, LDS_BYTES, stream>>>(p);
}
```
